```python
import torch

def _softmax(x, axis=-1):
    return torch.softmax(x, dim=axis)

def _gelu(x):
    return torch.nn.functional.gelu(x, approximate='tanh')

def _silu(x):
    return torch.nn.functional.silu(x)

def _sigmoid(x):
    return torch.sigmoid(x)

def _layernorm(x, axis=-1, eps=1e-05):
    m = x.mean(dim=axis, keepdim=True)
    v = x.var(dim=axis, keepdim=True, unbiased=False)
    return (x - m) / torch.sqrt(v + eps)

def _scatter_add(tgt, idx, src, axis=0):
    return tgt.clone().index_add_(axis, idx, src)

def _segment_sum(src, seg, n):
    out = torch.zeros((n,) + src.shape[1:], dtype=src.dtype)
    return out.index_add_(0, seg, src)

def _pad(x, pw):
    flat = tuple((v for pair in reversed(pw) for v in pair))
    return torch.nn.functional.pad(x, flat)
kernel_name = 'standalone__ragged_sequences_varlen_flash_attn__ragged'

def setup_inputs():

    _w = lambda a: (lambda t: t.to(torch.bfloat16) if t.is_floating_point() else t)(torch.as_tensor(a))
    cu = torch.tensor([0, 506, 648, 1109, 1416, 1494, 1644, 1941, 2399], dtype=torch.int64)
    T = int(cu[-1])
    Hq, Hkv, D = (16, 8, 192)
    assert Hq % Hkv == 0
    return {'q': _w((lambda *s: torch.randn(*s, dtype=torch.bfloat16) * 0.8)(T, Hq, D)), 'k': _w((lambda *s: torch.randn(*s, dtype=torch.bfloat16) * 0.8)(T, Hkv, D)), 'v': _w((lambda *s: torch.randn(*s, dtype=torch.bfloat16) * 0.05)(T, Hkv, D)), 'cu': _w(cu)}

def reference(q, k, v, cu):
    T, Hq, D = q.shape
    Hkv = k.shape[1]
    rep = Hq // Hkv
    scale = D ** (-0.5)
    outs = []
    for i in range(len(cu) - 1):
        s, e = (int(cu[i]), int(cu[i + 1]))
        L = e - s
        qi = q[s:e].clone()
        ki = (lambda x, n, axis: torch.repeat_interleave(x, n, dim=axis))(k[s:e], rep, axis=1)
        vi = (lambda x, n, axis: torch.repeat_interleave(x, n, dim=axis))(v[s:e], rep, axis=1)
        logits = torch.einsum('qhd,khd->hqk', qi, ki) * scale
        ar = torch.arange(L)
        neg = (lambda x, dt: x.to(dt))(ar[None, :] > ar[:, None], logits.dtype) * -1000000000.0
        p = _softmax(logits + neg[None, :, :], axis=-1)
        outs.append(torch.einsum('hqk,khd->qhd', p, vi))
    return torch.concatenate(outs, axis=0)
```

```cpp
#include <hip/hip_runtime.h>
#include <hip/hip_bf16.h>
#include <stdint.h>

typedef short bf16x8 __attribute__((ext_vector_type(8)));
typedef short s16x4 __attribute__((ext_vector_type(4)));
typedef float f32x16 __attribute__((ext_vector_type(16)));
typedef unsigned u32x4 __attribute__((ext_vector_type(4)));
typedef unsigned u32x2 __attribute__((ext_vector_type(2)));
typedef __attribute__((address_space(3))) const char* lds_cptr;
typedef short v4i16_t __attribute__((ext_vector_type(4)));

constexpr int HQ = 16, HKV = 8, D = 192;
constexpr unsigned QROW = HQ * D * 2, KROW = HKV * D * 2;
constexpr int TILE_B = 24576;
constexpr int NSLOT = 3;
constexpr int LDS_BYTES = NSLOT * TILE_B;
constexpr float C2 = 0.07216878364870322f * 1.4426950408889634f;
constexpr float THR = 8.0f;

#define DI __device__ __forceinline__
#define MFMA(a, b, c) __builtin_amdgcn_mfma_f32_32x32x16_bf16(a, b, c, 0, 0, 0)
DI unsigned cvtpk(float lo, float hi) { unsigned r; asm("v_cvt_pk_bf16_f32 %0, %1, %2" : "=v"(r) : "v"(lo), "v"(hi)); return r; }
DI s16x4 vtr(lds_cptr p) { return __builtin_bit_cast(s16x4, __builtin_amdgcn_ds_read_tr16_b64_v4i16((__attribute__((address_space(3))) v4i16_t*)p)); }
DI int rfl(int x) { return __builtin_amdgcn_readfirstlane(x); }

__global__ __launch_bounds__(256, 2) void attn_v1(const unsigned short* __restrict__ Q, const unsigned short* __restrict__ K,
                                                  const unsigned short* __restrict__ V, const int* __restrict__ cu,
                                                  unsigned short* __restrict__ O, int nseq) {
    extern __shared__ __attribute__((aligned(16))) char lds[];
    const int tid = threadIdx.x, lane = tid & 63, wid = rfl(tid >> 6), l32 = lane & 31, hi = lane >> 5;
    const int kvh = blockIdx.x & 7;
    int j = blockIdx.x >> 3;
    int s0 = 0, L = 0, tq = -1;
    {
        const int a = lane < nseq ? cu[lane] : 0, b = lane < nseq ? cu[lane + 1] : 0;
        const int nt = (b - a + 63) >> 6;
        int mx = nt;
        for (int o = 32; o; o >>= 1) mx = max(mx, __shfl_xor(mx, o));
        mx = rfl(mx);
        for (int t = mx - 1; t >= 0; --t) {
            unsigned long long m = __ballot(nt > t);
            const int c = __popcll(m);
            if (j < c) {
                for (int i = 0; i < j; ++i) m &= m - 1;
                const int sl = __builtin_ctzll(m);
                s0 = __builtin_amdgcn_readlane(a, sl); L = __builtin_amdgcn_readlane(b, sl) - s0; tq = t;
                break;
            }
            j -= c;
        }
    }
    if (tq < 0) return;
    const int q0 = tq * 64;
    const int head = 2 * kvh + (wid & 1), pb = wid >> 1;
    const int qw0 = q0 + 32 * pb;
    const int kend = min(q0 + 64, L);
    const int NT = (kend + 31) >> 5;
    const int ntw = qw0 < L ? (qw0 >> 5) + 1 : 0;

    const __amdgpu_buffer_rsrc_t srdK = __builtin_amdgcn_make_buffer_rsrc((void*)((const char*)K + (size_t)s0 * KROW + kvh * 384), (short)0, (int)((unsigned)L * KROW - kvh * 384), 0x00020000);
    const __amdgpu_buffer_rsrc_t srdV = __builtin_amdgcn_make_buffer_rsrc((void*)((const char*)V + (size_t)s0 * KROW + kvh * 384), (short)0, (int)((unsigned)L * KROW - kvh * 384), 0x00020000);
    const int drow = 8 * wid + (lane >> 3);
    unsigned vofK = drow * KROW + 16u * ((lane & 7) ^ ((drow >> 1) & 7));
    unsigned vofV = drow * KROW + 16u * ((lane & 7) ^ (((drow >> 1) & 1) << 2));
    const unsigned ldsb = (unsigned)(uintptr_t)lds;
    auto dma = [&](int slot) {
        const unsigned mb = ldsb + slot * TILE_B + wid * 1024;
#pragma unroll
        for (int s = 0; s < 3; ++s)
            asm volatile("s_mov_b32 m0, %0\n\ts_nop 0\n\tbuffer_load_dwordx4 %1, %2, 0 offen offset:%3 lds" :: "s"(mb + s * (4096 - 128)), "v"(vofK), "s"(srdK), "i"(s * 128) : "memory");
#pragma unroll
        for (int s = 0; s < 3; ++s)
            asm volatile("s_mov_b32 m0, %0\n\ts_nop 0\n\tbuffer_load_dwordx4 %1, %2, 0 offen offset:%3 lds" :: "s"(mb + 12288 + s * (4096 - 128)), "v"(vofV), "s"(srdV), "i"(s * 128) : "memory");
        vofK += 32 * KROW; vofV += 32 * KROW;
    };
    dma(0);
    if (NT > 1) dma(1);
    bf16x8 qf[12];
    {
        const int qp = min(qw0 + l32, L - 1);
        const char* qp_ = (const char*)Q + (size_t)(s0 + qp) * QROW + head * 384 + hi * 16;
#pragma unroll
        for (int ks = 0; ks < 12; ++ks) qf[ks] = *(const bf16x8*)(qp_ + ks * 32);
    }
    f32x16 o[6];
#pragma unroll
    for (int i = 0; i < 6; ++i) o[i] = f32x16{};
    float m = -1e30f, l = 0.f;
    const lds_cptr lp = (lds_cptr)lds;
    lds_cptr kb[4];
#pragma unroll
    for (int i = 0; i < 4; ++i) kb[i] = lp + l32 * 128 + 16 * ((2 * i + hi) ^ ((l32 >> 1) & 7));
    const int vq = (lane & 15) >> 2;
    lds_cptr vb[2];
#pragma unroll
    for (int i = 0; i < 2; ++i) vb[i] = lp + 12288 + (4 * hi + vq) * 128 + ((64 * i) ^ (64 * ((vq >> 1) & 1))) + 32 * ((lane >> 4) & 1) + 8 * (lane & 3);
    const int qrel = qw0 + l32;

    int slot = 0;
    for (int t = 0; t < NT; ++t) {
        if (t + 1 < NT) asm volatile("s_waitcnt vmcnt(6)" ::: "memory"); else asm volatile("s_waitcnt vmcnt(0)" ::: "memory");
        asm volatile("s_barrier" ::: "memory");
        if (t + 2 < NT) dma(slot == 0 ? 2 : slot - 1);
        if (t < ntw) {
            const int so = slot * TILE_B;
            f32x16 s = f32x16{};
#pragma unroll
            for (int ks = 0; ks < 12; ++ks) {
                const bf16x8 kf = *(const __attribute__((address_space(3))) bf16x8*)(kb[ks & 3] + so + (ks >> 2) * 4096);
                s = MFMA(kf, qf[ks], s);
            }
            if (t == ntw - 1) {
                const int kb0 = t * 32 + 4 * hi;
#pragma unroll
                for (int r = 0; r < 16; ++r) if (kb0 + (r & 3) + 8 * (r >> 2) > qrel) s[r] = -__builtin_inff();
            }
            float mx = fmaxf(fmaxf(s[0], s[1]), s[2]);
#pragma unroll
            for (int r = 3; r < 15; r += 2) mx = fmaxf(fmaxf(mx, s[r]), s[r + 1]);
            mx = fmaxf(mx, s[15]);
            { auto rr = __builtin_amdgcn_permlane32_swap(__float_as_uint(mx), __float_as_uint(mx), false, false); mx = fmaxf(__uint_as_float(rr[0]), __uint_as_float(rr[1])); }
            mx *= C2;
            if (__any(mx - m > THR)) {
                const float mn = fmaxf(m, mx), al = __builtin_amdgcn_exp2f(m - mn);
                m = mn; l *= al;
#pragma unroll
                for (int i = 0; i < 6; ++i)
#pragma unroll
                    for (int r = 0; r < 16; ++r) o[i][r] *= al;
            }
            const float nm = -m;
            float ps = 0.f;
#pragma unroll
            for (int r = 0; r < 16; ++r) { s[r] = __builtin_amdgcn_exp2f(fmaf(s[r], C2, nm)); ps += s[r]; }
            l += ps;
            u32x4 p0 = {cvtpk(s[0], s[1]), cvtpk(s[2], s[3]), cvtpk(s[4], s[5]), cvtpk(s[6], s[7])};
            u32x4 p1 = {cvtpk(s[8], s[9]), cvtpk(s[10], s[11]), cvtpk(s[12], s[13]), cvtpk(s[14], s[15])};
            const bf16x8 pf0 = __builtin_bit_cast(bf16x8, p0), pf1 = __builtin_bit_cast(bf16x8, p1);
#pragma unroll
            for (int db = 0; db < 6; ++db) {
                const lds_cptr vp = vb[db & 1] + so + (db >> 1) * 4096;
                const s16x4 a0 = vtr(vp), a1 = vtr(vp + 1024), b0 = vtr(vp + 2048), b1 = vtr(vp + 3072);
                o[db] = MFMA(((bf16x8){a0[0], a0[1], a0[2], a0[3], a1[0], a1[1], a1[2], a1[3]}), pf0, o[db]);
                o[db] = MFMA(((bf16x8){b0[0], b0[1], b0[2], b0[3], b1[0], b1[1], b1[2], b1[3]}), pf1, o[db]);
            }
        }
        slot = slot == 2 ? 0 : slot + 1;
    }
    if (ntw > 0) {
        { auto rr = __builtin_amdgcn_permlane32_swap(__float_as_uint(l), __float_as_uint(l), false, false); l = __uint_as_float(rr[0]) + __uint_as_float(rr[1]); }
        const float il = 1.0f / l;
        char* op = (char*)O + (size_t)(s0 + qrel) * QROW + head * 384 + hi * 16;
#pragma unroll
        for (int db = 0; db < 6; ++db)
#pragma unroll
            for (int g = 0; g < 4; g += 2) {
                unsigned a0 = cvtpk(o[db][4 * g] * il, o[db][4 * g + 1] * il), a1 = cvtpk(o[db][4 * g + 2] * il, o[db][4 * g + 3] * il);
                unsigned b0 = cvtpk(o[db][4 * g + 4] * il, o[db][4 * g + 5] * il), b1 = cvtpk(o[db][4 * g + 6] * il, o[db][4 * g + 7] * il);
                auto r0 = __builtin_amdgcn_permlane32_swap(a0, b0, false, false);
                auto r1 = __builtin_amdgcn_permlane32_swap(a1, b1, false, false);
                const u32x4 w = {r0[0], r1[0], r0[1], r1[1]};
                if (qrel < L) *(u32x4*)(op + db * 64 + g * 16) = w;
            }
    }
}

extern "C" void kernel_launch(void* const* d_in, const int* in_sizes, int n_in, void* d_out, int out_size, void* d_ws, size_t ws_size, hipStream_t stream) {
    const int T = in_sizes[0] / (HQ * D), nseq = in_sizes[3] - 1;
    const int maxitems = (T + 63) / 64 + nseq;
    static bool attr = false;
    if (!attr) { attr = true; (void)hipFuncSetAttribute((const void*)attn_v1, hipFuncAttributeMaxDynamicSharedMemorySize, LDS_BYTES); }
    attn_v1<<<maxitems * 8, 256, LDS_BYTES, stream>>>((const unsigned short*)d_in[0], (const unsigned short*)d_in[1], (const unsigned short*)d_in[2],
                                                    (const int*)d_in[3], (unsigned short*)d_out, nseq);
}
```

```cpp
#define PA -1
#define PB -1
#define USECYC 0
#define AMP 10
#include <hip/hip_runtime.h>
#include <hip/hip_bf16.h>
#include <stdint.h>

typedef short bf16x8 __attribute__((ext_vector_type(8)));
typedef short s16x4 __attribute__((ext_vector_type(4)));
typedef float f32x16 __attribute__((ext_vector_type(16)));
typedef unsigned u32x4 __attribute__((ext_vector_type(4)));
typedef unsigned u32x2 __attribute__((ext_vector_type(2)));
typedef __attribute__((address_space(3))) const char* lds_cptr;
typedef short v4i16_t __attribute__((ext_vector_type(4)));

constexpr int HQ = 16, HKV = 8, D = 192;
constexpr unsigned QROW = HQ * D * 2, KROW = HKV * D * 2;
constexpr int TILE_B = 24576;
constexpr int NSLOT = 3;
constexpr int LDS_BYTES = NSLOT * TILE_B;
constexpr float C2 = 0.07216878364870322f * 1.4426950408889634f;
constexpr float THR = 8.0f;

#define DI __device__ __forceinline__
#define MFMA(a, b, c) __builtin_amdgcn_mfma_f32_32x32x16_bf16(a, b, c, 0, 0, 0)
DI unsigned cvtpk(float lo, float hi) { unsigned r; asm("v_cvt_pk_bf16_f32 %0, %1, %2" : "=v"(r) : "v"(lo), "v"(hi)); return r; }
DI s16x4 vtr(lds_cptr p) { return __builtin_bit_cast(s16x4, __builtin_amdgcn_ds_read_tr16_b64_v4i16((__attribute__((address_space(3))) v4i16_t*)p)); }
DI int rfl(int x) { return __builtin_amdgcn_readfirstlane(x); }

__global__ __launch_bounds__(256, 2) void attn_v1(const unsigned short* __restrict__ Q, const unsigned short* __restrict__ K,
                                                  const unsigned short* __restrict__ V, const int* __restrict__ cu,
                                                  unsigned short* __restrict__ O, int nseq) {
    extern __shared__ __attribute__((aligned(16))) char lds[];
    unsigned long long rt[6], ct[6];
#define STAMP(i) do { __builtin_amdgcn_sched_barrier(0); asm volatile("s_memrealtime %0\n\ts_memtime %1\n\ts_waitcnt lgkmcnt(0)" : "=s"(rt[i]), "=s"(ct[i]) :: "memory"); __builtin_amdgcn_sched_barrier(0); } while (0)
    STAMP(0);
    const int tid = threadIdx.x, lane = tid & 63, wid = rfl(tid >> 6), l32 = lane & 31, hi = lane >> 5;
    const int kvh = blockIdx.x & 7;
    int j = blockIdx.x >> 3;
    int s0 = 0, L = 0, tq = -1;
    {
        const int a = lane < nseq ? cu[lane] : 0, b = lane < nseq ? cu[lane + 1] : 0;
        const int nt = (b - a + 63) >> 6;
        int mx = nt;
        for (int o = 32; o; o >>= 1) mx = max(mx, __shfl_xor(mx, o));
        mx = rfl(mx);
        for (int t = mx - 1; t >= 0; --t) {
            unsigned long long m = __ballot(nt > t);
            const int c = __popcll(m);
            if (j < c) {
                for (int i = 0; i < j; ++i) m &= m - 1;
                const int sl = __builtin_ctzll(m);
                s0 = __builtin_amdgcn_readlane(a, sl); L = __builtin_amdgcn_readlane(b, sl) - s0; tq = t;
                break;
            }
            j -= c;
        }
    }
    if (tq < 0) return;
    STAMP(1);
    const int q0 = tq * 64;
    const int head = 2 * kvh + (wid & 1), pb = wid >> 1;
    const int qw0 = q0 + 32 * pb;
    const int kend = min(q0 + 64, L);
    const int NT = (kend + 31) >> 5;
    const int ntw = qw0 < L ? (qw0 >> 5) + 1 : 0;

    const __amdgpu_buffer_rsrc_t srdK = __builtin_amdgcn_make_buffer_rsrc((void*)((const char*)K + (size_t)s0 * KROW + kvh * 384), (short)0, (int)((unsigned)L * KROW - kvh * 384), 0x00020000);
    const __amdgpu_buffer_rsrc_t srdV = __builtin_amdgcn_make_buffer_rsrc((void*)((const char*)V + (size_t)s0 * KROW + kvh * 384), (short)0, (int)((unsigned)L * KROW - kvh * 384), 0x00020000);
    const int drow = 8 * wid + (lane >> 3);
    unsigned vofK = drow * KROW + 16u * ((lane & 7) ^ ((drow >> 1) & 7));
    unsigned vofV = drow * KROW + 16u * ((lane & 7) ^ (((drow >> 1) & 1) << 2));
    const unsigned ldsb = (unsigned)(uintptr_t)lds;
    auto dma = [&](int slot) {
        const unsigned mb = ldsb + slot * TILE_B + wid * 1024;
#pragma unroll
        for (int s = 0; s < 3; ++s)
            asm volatile("s_mov_b32 m0, %0\n\ts_nop 0\n\tbuffer_load_dwordx4 %1, %2, 0 offen offset:%3 lds" :: "s"(mb + s * (4096 - 128)), "v"(vofK), "s"(srdK), "i"(s * 128) : "memory");
#pragma unroll
        for (int s = 0; s < 3; ++s)
            asm volatile("s_mov_b32 m0, %0\n\ts_nop 0\n\tbuffer_load_dwordx4 %1, %2, 0 offen offset:%3 lds" :: "s"(mb + 12288 + s * (4096 - 128)), "v"(vofV), "s"(srdV), "i"(s * 128) : "memory");
        vofK += 32 * KROW; vofV += 32 * KROW;
    };
    dma(0);
    if (NT > 1) dma(1);
    bf16x8 qf[12];
    {
        const int qp = min(qw0 + l32, L - 1);
        const char* qp_ = (const char*)Q + (size_t)(s0 + qp) * QROW + head * 384 + hi * 16;
#pragma unroll
        for (int ks = 0; ks < 12; ++ks) qf[ks] = *(const bf16x8*)(qp_ + ks * 32);
    }
    f32x16 o[6];
#pragma unroll
    for (int i = 0; i < 6; ++i) o[i] = f32x16{};
    float m = -1e30f, l = 0.f;
    const lds_cptr lp = (lds_cptr)lds;
    lds_cptr kb[4];
#pragma unroll
    for (int i = 0; i < 4; ++i) kb[i] = lp + l32 * 128 + 16 * ((2 * i + hi) ^ ((l32 >> 1) & 7));
    const int vq = (lane & 15) >> 2;
    lds_cptr vb[2];
#pragma unroll
    for (int i = 0; i < 2; ++i) vb[i] = lp + 12288 + (4 * hi + vq) * 128 + ((64 * i) ^ (64 * ((vq >> 1) & 1))) + 32 * ((lane >> 4) & 1) + 8 * (lane & 3);
    const int qrel = qw0 + l32;

    int slot = 0;
    for (int t = 0; t < NT; ++t) {
        if (t + 1 < NT) asm volatile("s_waitcnt vmcnt(6)" ::: "memory"); else asm volatile("s_waitcnt vmcnt(0)" ::: "memory");
        asm volatile("s_barrier" ::: "memory");
        if (t == 0) STAMP(2);
        if (t + 2 < NT) dma(slot == 0 ? 2 : slot - 1);
        if (t < ntw) {
            const int so = slot * TILE_B;
            f32x16 s = f32x16{};
#pragma unroll
            for (int ks = 0; ks < 12; ++ks) {
                const bf16x8 kf = *(const __attribute__((address_space(3))) bf16x8*)(kb[ks & 3] + so + (ks >> 2) * 4096);
                s = MFMA(kf, qf[ks], s);
            }
            if (t == ntw - 1) {
                const int kb0 = t * 32 + 4 * hi;
#pragma unroll
                for (int r = 0; r < 16; ++r) if (kb0 + (r & 3) + 8 * (r >> 2) > qrel) s[r] = -__builtin_inff();
            }
            float mx = fmaxf(fmaxf(s[0], s[1]), s[2]);
#pragma unroll
            for (int r = 3; r < 15; r += 2) mx = fmaxf(fmaxf(mx, s[r]), s[r + 1]);
            mx = fmaxf(mx, s[15]);
            { auto rr = __builtin_amdgcn_permlane32_swap(__float_as_uint(mx), __float_as_uint(mx), false, false); mx = fmaxf(__uint_as_float(rr[0]), __uint_as_float(rr[1])); }
            mx *= C2;
            if (__any(mx - m > THR)) {
                const float mn = fmaxf(m, mx), al = __builtin_amdgcn_exp2f(m - mn);
                m = mn; l *= al;
#pragma unroll
                for (int i = 0; i < 6; ++i)
#pragma unroll
                    for (int r = 0; r < 16; ++r) o[i][r] *= al;
            }
            const float nm = -m;
            float ps = 0.f;
#pragma unroll
            for (int r = 0; r < 16; ++r) { s[r] = __builtin_amdgcn_exp2f(fmaf(s[r], C2, nm)); ps += s[r]; }
            l += ps;
            u32x4 p0 = {cvtpk(s[0], s[1]), cvtpk(s[2], s[3]), cvtpk(s[4], s[5]), cvtpk(s[6], s[7])};
            u32x4 p1 = {cvtpk(s[8], s[9]), cvtpk(s[10], s[11]), cvtpk(s[12], s[13]), cvtpk(s[14], s[15])};
            const bf16x8 pf0 = __builtin_bit_cast(bf16x8, p0), pf1 = __builtin_bit_cast(bf16x8, p1);
#pragma unroll
            for (int db = 0; db < 6; ++db) {
                const lds_cptr vp = vb[db & 1] + so + (db >> 1) * 4096;
                const s16x4 a0 = vtr(vp), a1 = vtr(vp + 1024), b0 = vtr(vp + 2048), b1 = vtr(vp + 3072);
                o[db] = MFMA(((bf16x8){a0[0], a0[1], a0[2], a0[3], a1[0], a1[1], a1[2], a1[3]}), pf0, o[db]);
                o[db] = MFMA(((bf16x8){b0[0], b0[1], b0[2], b0[3], b1[0], b1[1], b1[2], b1[3]}), pf1, o[db]);
            }
        }
        slot = slot == 2 ? 0 : slot + 1;
    }
    STAMP(3);
    if (ntw > 0) {
        { auto rr = __builtin_amdgcn_permlane32_swap(__float_as_uint(l), __float_as_uint(l), false, false); l = __uint_as_float(rr[0]) + __uint_as_float(rr[1]); }
        const float il = 1.0f / l;
        char* op = (char*)O + (size_t)(s0 + qrel) * QROW + head * 384 + hi * 16;
#pragma unroll
        for (int db = 0; db < 6; ++db)
#pragma unroll
            for (int g = 0; g < 4; g += 2) {
                unsigned a0 = cvtpk(o[db][4 * g] * il, o[db][4 * g + 1] * il), a1 = cvtpk(o[db][4 * g + 2] * il, o[db][4 * g + 3] * il);
                unsigned b0 = cvtpk(o[db][4 * g + 4] * il, o[db][4 * g + 5] * il), b1 = cvtpk(o[db][4 * g + 6] * il, o[db][4 * g + 7] * il);
                auto r0 = __builtin_amdgcn_permlane32_swap(a0, b0, false, false);
                auto r1 = __builtin_amdgcn_permlane32_swap(a1, b1, false, false);
                const u32x4 w = {r0[0], r1[0], r0[1], r1[1]};
                if (qrel < L) *(u32x4*)(op + db * 64 + g * 16) = w;
            }
    }
    asm volatile("s_waitcnt vmcnt(0)" ::: "memory");
    STAMP(4);
    if (blockIdx.x < 8 && wid == 3) {
        unsigned long long d;
        if (PA < 0) d = 0;
        else if (USECYC) d = (ct[PB > 0 ? PB : 0] - ct[PA > 0 ? PA : 0]) / 24;
        else d = rt[PB > 0 ? PB : 0] - rt[PA > 0 ? PA : 0];
        const unsigned long long until = rt[4] + AMP * d;
        while (__builtin_amdgcn_s_memrealtime() < until) __builtin_amdgcn_s_sleep(2);
    }
}

extern "C" void kernel_launch(void* const* d_in, const int* in_sizes, int n_in, void* d_out, int out_size, void* d_ws, size_t ws_size, hipStream_t stream) {
    const int T = in_sizes[0] / (HQ * D), nseq = in_sizes[3] - 1;
    const int maxitems = (T + 63) / 64 + nseq;
    static bool attr = false;
    if (!attr) { attr = true; (void)hipFuncSetAttribute((const void*)attn_v1, hipFuncAttributeMaxDynamicSharedMemorySize, LDS_BYTES); }
    attn_v1<<<maxitems * 8, 256, LDS_BYTES, stream>>>((const unsigned short*)d_in[0], (const unsigned short*)d_in[1], (const unsigned short*)d_in[2],
                                                    (const int*)d_in[3], (unsigned short*)d_out, nseq);
}
```

```cpp
#define MODE 4
#define STORE 1
#include <hip/hip_runtime.h>
#include <hip/hip_bf16.h>
#include <stdint.h>

typedef short bf16x8 __attribute__((ext_vector_type(8)));
typedef short s16x4 __attribute__((ext_vector_type(4)));
typedef float f32x16 __attribute__((ext_vector_type(16)));
typedef unsigned u32x4 __attribute__((ext_vector_type(4)));
typedef unsigned u32x2 __attribute__((ext_vector_type(2)));
typedef __attribute__((address_space(3))) const char* lds_cptr;
typedef short v4i16_t __attribute__((ext_vector_type(4)));

constexpr int HQ = 16, HKV = 8, D = 192;
constexpr unsigned QROW = HQ * D * 2, KROW = HKV * D * 2;
constexpr int TILE_B = 24576;
constexpr int NSLOT = 3;
constexpr int LDS_BYTES = NSLOT * TILE_B;
constexpr float C2 = 0.07216878364870322f * 1.4426950408889634f;
constexpr float THR = 8.0f;

#define DI __device__ __forceinline__
#define MFMA(a, b, c) __builtin_amdgcn_mfma_f32_32x32x16_bf16(a, b, c, 0, 0, 0)
DI unsigned cvtpk(float lo, float hi) { unsigned r; asm("v_cvt_pk_bf16_f32 %0, %1, %2" : "=v"(r) : "v"(lo), "v"(hi)); return r; }
DI s16x4 vtr(lds_cptr p) { return __builtin_bit_cast(s16x4, __builtin_amdgcn_ds_read_tr16_b64_v4i16((__attribute__((address_space(3))) v4i16_t*)p)); }
DI int rfl(int x) { return __builtin_amdgcn_readfirstlane(x); }

__global__ __launch_bounds__(256, 2) void attn_v1(const unsigned short* __restrict__ Q, const unsigned short* __restrict__ K,
                                                  const unsigned short* __restrict__ V, const int* __restrict__ cu,
                                                  unsigned short* __restrict__ O, int nseq, unsigned long long* ws) {
    extern __shared__ __attribute__((aligned(16))) char lds[];
    const unsigned long long rt0 = __builtin_amdgcn_s_memrealtime();
    const int tid = threadIdx.x, lane = tid & 63, wid = rfl(tid >> 6), l32 = lane & 31, hi = lane >> 5;
    const int kvh = blockIdx.x & 7;
    int j = blockIdx.x >> 3;
    int s0 = 0, L = 0, tq = -1;
    {
        const int a = lane < nseq ? cu[lane] : 0, b = lane < nseq ? cu[lane + 1] : 0;
        const int nt = (b - a + 63) >> 6;
        int mx = nt;
        for (int o = 32; o; o >>= 1) mx = max(mx, __shfl_xor(mx, o));
        mx = rfl(mx);
        for (int t = mx - 1; t >= 0; --t) {
            unsigned long long m = __ballot(nt > t);
            const int c = __popcll(m);
            if (j < c) {
                for (int i = 0; i < j; ++i) m &= m - 1;
                const int sl = __builtin_ctzll(m);
                s0 = __builtin_amdgcn_readlane(a, sl); L = __builtin_amdgcn_readlane(b, sl) - s0; tq = t;
                break;
            }
            j -= c;
        }
    }
    if (tq < 0) { if (MODE == 2 || MODE == 3) { if (tid == 0) ws[64 + blockIdx.x] = __builtin_amdgcn_s_memrealtime(); } return; }
    const int q0 = tq * 64;
    const int head = 2 * kvh + (wid & 1), pb = wid >> 1;
    const int qw0 = q0 + 32 * pb;
    const int kend = min(q0 + 64, L);
    const int NT = (kend + 31) >> 5;
    const int ntw = qw0 < L ? (qw0 >> 5) + 1 : 0;

    const __amdgpu_buffer_rsrc_t srdK = __builtin_amdgcn_make_buffer_rsrc((void*)((const char*)K + (size_t)s0 * KROW + kvh * 384), (short)0, (int)((unsigned)L * KROW - kvh * 384), 0x00020000);
    const __amdgpu_buffer_rsrc_t srdV = __builtin_amdgcn_make_buffer_rsrc((void*)((const char*)V + (size_t)s0 * KROW + kvh * 384), (short)0, (int)((unsigned)L * KROW - kvh * 384), 0x00020000);
    const int drow = 8 * wid + (lane >> 3);
    unsigned vofK = drow * KROW + 16u * ((lane & 7) ^ ((drow >> 1) & 7));
    unsigned vofV = drow * KROW + 16u * ((lane & 7) ^ (((drow >> 1) & 1) << 2));
    const unsigned ldsb = (unsigned)(uintptr_t)lds;
    auto dma = [&](int slot) {
        const unsigned mb = ldsb + slot * TILE_B + wid * 1024;
#pragma unroll
        for (int s = 0; s < 3; ++s)
            asm volatile("s_mov_b32 m0, %0\n\ts_nop 0\n\tbuffer_load_dwordx4 %1, %2, 0 offen offset:%3 lds" :: "s"(mb + s * (4096 - 128)), "v"(vofK), "s"(srdK), "i"(s * 128) : "memory");
#pragma unroll
        for (int s = 0; s < 3; ++s)
            asm volatile("s_mov_b32 m0, %0\n\ts_nop 0\n\tbuffer_load_dwordx4 %1, %2, 0 offen offset:%3 lds" :: "s"(mb + 12288 + s * (4096 - 128)), "v"(vofV), "s"(srdV), "i"(s * 128) : "memory");
        vofK += 32 * KROW; vofV += 32 * KROW;
    };
    dma(0);
    if (NT > 1) dma(1);
    bf16x8 qf[12];
    {
        const int qp = min(qw0 + l32, L - 1);
        const char* qp_ = (const char*)Q + (size_t)(s0 + qp) * QROW + head * 384 + hi * 16;
#pragma unroll
        for (int ks = 0; ks < 12; ++ks) qf[ks] = *(const bf16x8*)(qp_ + ks * 32);
    }
    f32x16 o[6];
#pragma unroll
    for (int i = 0; i < 6; ++i) o[i] = f32x16{};
    float m = -1e30f, l = 0.f;
    const lds_cptr lp = (lds_cptr)lds;
    lds_cptr kb[4];
#pragma unroll
    for (int i = 0; i < 4; ++i) kb[i] = lp + l32 * 128 + 16 * ((2 * i + hi) ^ ((l32 >> 1) & 7));
    const int vq = (lane & 15) >> 2;
    lds_cptr vb[2];
#pragma unroll
    for (int i = 0; i < 2; ++i) vb[i] = lp + 12288 + (4 * hi + vq) * 128 + ((64 * i) ^ (64 * ((vq >> 1) & 1))) + 32 * ((lane >> 4) & 1) + 8 * (lane & 3);
    const int qrel = qw0 + l32;

    int slot = 0;
    for (int t = 0; t < NT; ++t) {
        if (t + 1 < NT) asm volatile("s_waitcnt vmcnt(6)" ::: "memory"); else asm volatile("s_waitcnt vmcnt(0)" ::: "memory");
        asm volatile("s_barrier" ::: "memory");
        if (t + 2 < NT) dma(slot == 0 ? 2 : slot - 1);
        if (t < ntw) {
            const int so = slot * TILE_B;
            f32x16 s = f32x16{};
#pragma unroll
            for (int ks = 0; ks < 12; ++ks) {
                const bf16x8 kf = *(const __attribute__((address_space(3))) bf16x8*)(kb[ks & 3] + so + (ks >> 2) * 4096);
                s = MFMA(kf, qf[ks], s);
            }
            if (t == ntw - 1) {
                const int kb0 = t * 32 + 4 * hi;
#pragma unroll
                for (int r = 0; r < 16; ++r) if (kb0 + (r & 3) + 8 * (r >> 2) > qrel) s[r] = -__builtin_inff();
            }
            float mx = fmaxf(fmaxf(s[0], s[1]), s[2]);
#pragma unroll
            for (int r = 3; r < 15; r += 2) mx = fmaxf(fmaxf(mx, s[r]), s[r + 1]);
            mx = fmaxf(mx, s[15]);
            { auto rr = __builtin_amdgcn_permlane32_swap(__float_as_uint(mx), __float_as_uint(mx), false, false); mx = fmaxf(__uint_as_float(rr[0]), __uint_as_float(rr[1])); }
            mx *= C2;
            if (__any(mx - m > THR)) {
                const float mn = fmaxf(m, mx), al = __builtin_amdgcn_exp2f(m - mn);
                m = mn; l *= al;
#pragma unroll
                for (int i = 0; i < 6; ++i)
#pragma unroll
                    for (int r = 0; r < 16; ++r) o[i][r] *= al;
            }
            const float nm = -m;
            float ps = 0.f;
#pragma unroll
            for (int r = 0; r < 16; ++r) { s[r] = __builtin_amdgcn_exp2f(fmaf(s[r], C2, nm)); ps += s[r]; }
            l += ps;
            u32x4 p0 = {cvtpk(s[0], s[1]), cvtpk(s[2], s[3]), cvtpk(s[4], s[5]), cvtpk(s[6], s[7])};
            u32x4 p1 = {cvtpk(s[8], s[9]), cvtpk(s[10], s[11]), cvtpk(s[12], s[13]), cvtpk(s[14], s[15])};
            const bf16x8 pf0 = __builtin_bit_cast(bf16x8, p0), pf1 = __builtin_bit_cast(bf16x8, p1);
#pragma unroll
            for (int db = 0; db < 6; ++db) {
                const lds_cptr vp = vb[db & 1] + so + (db >> 1) * 4096;
                const s16x4 a0 = vtr(vp), a1 = vtr(vp + 1024), b0 = vtr(vp + 2048), b1 = vtr(vp + 3072);
                o[db] = MFMA(((bf16x8){a0[0], a0[1], a0[2], a0[3], a1[0], a1[1], a1[2], a1[3]}), pf0, o[db]);
                o[db] = MFMA(((bf16x8){b0[0], b0[1], b0[2], b0[3], b1[0], b1[1], b1[2], b1[3]}), pf1, o[db]);
            }
        }
        slot = slot == 2 ? 0 : slot + 1;
    }
    if (ntw > 0) {
        { auto rr = __builtin_amdgcn_permlane32_swap(__float_as_uint(l), __float_as_uint(l), false, false); l = __uint_as_float(rr[0]) + __uint_as_float(rr[1]); }
        const float il = 1.0f / l;
        char* op = (char*)O + (size_t)(s0 + qrel) * QROW + head * 384 + hi * 16;
#pragma unroll
        for (int db = 0; db < 6; ++db)
#pragma unroll
            for (int g = 0; g < 4; g += 2) {
                unsigned a0 = cvtpk(o[db][4 * g] * il, o[db][4 * g + 1] * il), a1 = cvtpk(o[db][4 * g + 2] * il, o[db][4 * g + 3] * il);
                unsigned b0 = cvtpk(o[db][4 * g + 4] * il, o[db][4 * g + 5] * il), b1 = cvtpk(o[db][4 * g + 6] * il, o[db][4 * g + 7] * il);
                auto r0 = __builtin_amdgcn_permlane32_swap(a0, b0, false, false);
                auto r1 = __builtin_amdgcn_permlane32_swap(a1, b1, false, false);
                const u32x4 w = {r0[0], r1[0], r0[1], r1[1]};
                if (qrel < L) { if (STORE == 0) *(u32x4*)(op + db * 64 + g * 16) = w; else if (STORE == 1) __builtin_nontemporal_store(w, (u32x4*)(op + db * 64 + g * 16));
                   else asm volatile("global_store_dwordx4 %0, %1, off sc0 sc1\n\ts_nop 1" :: "v"(op + db * 64 + g * 16), "v"(w) : "memory"); }
            }
    }
    if (MODE == 2 || MODE == 3) { asm volatile("s_waitcnt vmcnt(0)" ::: "memory"); __syncthreads(); if (tid == 0) ws[64 + blockIdx.x] = __builtin_amdgcn_s_memrealtime(); }
    if (MODE == 0 && blockIdx.x < 8 && wid == 3) {
        asm volatile("s_waitcnt vmcnt(0)" ::: "memory");
        const unsigned long long d = rt0 - *(volatile unsigned long long*)ws;
        const unsigned long long until = __builtin_amdgcn_s_memrealtime() + 10 * d;
        while (__builtin_amdgcn_s_memrealtime() < until) __builtin_amdgcn_s_sleep(2);
    }
}

__global__ void pre_k(unsigned long long* ws) { if (threadIdx.x == 0) ws[0] = __builtin_amdgcn_s_memrealtime(); }
__global__ void post_k(unsigned long long* ws, int n) {
    const unsigned long long t0 = __builtin_amdgcn_s_memrealtime();
    unsigned long long m = 0;
    for (int i = threadIdx.x; i < n; i += 64) { unsigned long long v = ws[64 + i]; m = v > m ? v : m; }
    for (int o = 32; o; o >>= 1) { unsigned long long v = __shfl_xor(m, o); m = v > m ? v : m; }
    if (MODE == 2) { const unsigned long long until = __builtin_amdgcn_s_memrealtime() + 10 * (t0 - m);
        while (__builtin_amdgcn_s_memrealtime() < until) __builtin_amdgcn_s_sleep(2); }
    if (m == 1) ws[1] = 0;
}
extern "C" void kernel_launch(void* const* d_in, const int* in_sizes, int n_in, void* d_out, int out_size, void* d_ws, size_t ws_size, hipStream_t stream) {
    const int T = in_sizes[0] / (HQ * D), nseq = in_sizes[3] - 1;
    const int maxitems = (T + 63) / 64 + nseq;
    static bool attr = false;
    if (!attr) { attr = true; (void)hipFuncSetAttribute((const void*)attn_v1, hipFuncAttributeMaxDynamicSharedMemorySize, LDS_BYTES); }
    if (MODE == 0 || MODE == 1) pre_k<<<1, 64, 0, stream>>>((unsigned long long*)d_ws);
    attn_v1<<<maxitems * 8, 256, LDS_BYTES, stream>>>((const unsigned short*)d_in[0], (const unsigned short*)d_in[1], (const unsigned short*)d_in[2],
                                                    (const int*)d_in[3], (unsigned short*)d_out, nseq, (unsigned long long*)d_ws);
    if (MODE == 2 || MODE == 3) post_k<<<1, 64, 0, stream>>>((unsigned long long*)d_ws, maxitems * 8);
}
```

```cpp
#define MODE 0
#define LO 0
#define HI 0
#include <hip/hip_runtime.h>
#include <hip/hip_bf16.h>
#include <stdint.h>

typedef short bf16x8 __attribute__((ext_vector_type(8)));
typedef short s16x4 __attribute__((ext_vector_type(4)));
typedef float f32x16 __attribute__((ext_vector_type(16)));
typedef unsigned u32x4 __attribute__((ext_vector_type(4)));
typedef unsigned u32x2 __attribute__((ext_vector_type(2)));
typedef __attribute__((address_space(3))) const char* lds_cptr;
typedef short v4i16_t __attribute__((ext_vector_type(4)));

constexpr int HQ = 16, HKV = 8, D = 192;
constexpr unsigned QROW = HQ * D * 2, KROW = HKV * D * 2;
constexpr int TILE_B = 24576;
constexpr int NSLOT = 3;
constexpr int LDS_BYTES = NSLOT * TILE_B;
constexpr float C2 = 0.07216878364870322f * 1.4426950408889634f;
constexpr float THR = 8.0f;

#define DI __device__ __forceinline__
#define MFMA(a, b, c) __builtin_amdgcn_mfma_f32_32x32x16_bf16(a, b, c, 0, 0, 0)
DI unsigned cvtpk(float lo, float hi) { unsigned r; asm("v_cvt_pk_bf16_f32 %0, %1, %2" : "=v"(r) : "v"(lo), "v"(hi)); return r; }
DI s16x4 vtr(lds_cptr p) { return __builtin_bit_cast(s16x4, __builtin_amdgcn_ds_read_tr16_b64_v4i16((__attribute__((address_space(3))) v4i16_t*)p)); }
DI int rfl(int x) { return __builtin_amdgcn_readfirstlane(x); }

__global__ __launch_bounds__(256, 2) void attn_v1(const unsigned short* __restrict__ Q, const unsigned short* __restrict__ K,
                                                  const unsigned short* __restrict__ V, const int* __restrict__ cu,
                                                  unsigned short* __restrict__ O, int nseq, unsigned long long* ws) {
    extern __shared__ __attribute__((aligned(16))) char lds[];
    const unsigned long long rt0 = __builtin_amdgcn_s_memrealtime();
    if (MODE != 0 && blockIdx.x == 0 && threadIdx.x == 0) { __hip_atomic_store(ws, rt0, __ATOMIC_RELAXED, __HIP_MEMORY_SCOPE_AGENT); }
    const int tid = threadIdx.x, lane = tid & 63, wid = rfl(tid >> 6), l32 = lane & 31, hi = lane >> 5;
    const int kvh = blockIdx.x & 7;
    int j = blockIdx.x >> 3;
    int s0 = 0, L = 0, tq = -1;
    {
        const int a = lane < nseq ? cu[lane] : 0, b = lane < nseq ? cu[lane + 1] : 0;
        const int nt = (b - a + 63) >> 6;
        int mx = nt;
        for (int o = 32; o; o >>= 1) mx = max(mx, __shfl_xor(mx, o));
        mx = rfl(mx);
        for (int t = mx - 1; t >= 0; --t) {
            unsigned long long m = __ballot(nt > t);
            const int c = __popcll(m);
            if (j < c) {
                for (int i = 0; i < j; ++i) m &= m - 1;
                const int sl = __builtin_ctzll(m);
                s0 = __builtin_amdgcn_readlane(a, sl); L = __builtin_amdgcn_readlane(b, sl) - s0; tq = t;
                break;
            }
            j -= c;
        }
    }
    if (tq < 0) return;
    const unsigned long long rt1 = __builtin_amdgcn_s_memrealtime();
    const int q0 = tq * 64;
    const int head = 2 * kvh + (wid & 1), pb = wid >> 1;
    const int qw0 = q0 + 32 * pb;
    const int kend = min(q0 + 64, L);
    const int NT = (kend + 31) >> 5;
    const int ntw = qw0 < L ? (qw0 >> 5) + 1 : 0;

    const __amdgpu_buffer_rsrc_t srdK = __builtin_amdgcn_make_buffer_rsrc((void*)((const char*)K + (size_t)s0 * KROW + kvh * 384), (short)0, (int)((unsigned)L * KROW - kvh * 384), 0x00020000);
    const __amdgpu_buffer_rsrc_t srdV = __builtin_amdgcn_make_buffer_rsrc((void*)((const char*)V + (size_t)s0 * KROW + kvh * 384), (short)0, (int)((unsigned)L * KROW - kvh * 384), 0x00020000);
    const int drow = 8 * wid + (lane >> 3);
    unsigned vofK = drow * KROW + 16u * ((lane & 7) ^ ((drow >> 1) & 7));
    unsigned vofV = drow * KROW + 16u * ((lane & 7) ^ (((drow >> 1) & 1) << 2));
    const unsigned ldsb = (unsigned)(uintptr_t)lds;
    auto dma = [&](int slot) {
        const unsigned mb = ldsb + slot * TILE_B + wid * 1024;
#pragma unroll
        for (int s = 0; s < 3; ++s)
            asm volatile("s_mov_b32 m0, %0\n\ts_nop 0\n\tbuffer_load_dwordx4 %1, %2, 0 offen offset:%3 lds" :: "s"(mb + s * (4096 - 128)), "v"(vofK), "s"(srdK), "i"(s * 128) : "memory");
#pragma unroll
        for (int s = 0; s < 3; ++s)
            asm volatile("s_mov_b32 m0, %0\n\ts_nop 0\n\tbuffer_load_dwordx4 %1, %2, 0 offen offset:%3 lds" :: "s"(mb + 12288 + s * (4096 - 128)), "v"(vofV), "s"(srdV), "i"(s * 128) : "memory");
        vofK += 32 * KROW; vofV += 32 * KROW;
    };
    dma(0);
    if (NT > 1) dma(1);
    bf16x8 qf[12];
    {
        const int qp = min(qw0 + l32, L - 1);
        const char* qp_ = (const char*)Q + (size_t)(s0 + qp) * QROW + head * 384 + hi * 16;
#pragma unroll
        for (int ks = 0; ks < 12; ++ks) qf[ks] = *(const bf16x8*)(qp_ + ks * 32);
    }
    f32x16 o[6];
#pragma unroll
    for (int i = 0; i < 6; ++i) o[i] = f32x16{};
    float m = -1e30f, l = 0.f;
    const lds_cptr lp = (lds_cptr)lds;
    lds_cptr kb[4];
#pragma unroll
    for (int i = 0; i < 4; ++i) kb[i] = lp + l32 * 128 + 16 * ((2 * i + hi) ^ ((l32 >> 1) & 7));
    const int vq = (lane & 15) >> 2;
    lds_cptr vb[2];
#pragma unroll
    for (int i = 0; i < 2; ++i) vb[i] = lp + 12288 + (4 * hi + vq) * 128 + ((64 * i) ^ (64 * ((vq >> 1) & 1))) + 32 * ((lane >> 4) & 1) + 8 * (lane & 3);
    const int qrel = qw0 + l32;

    int slot = 0;
    for (int t = 0; t < NT; ++t) {
        if (t + 1 < NT) asm volatile("s_waitcnt vmcnt(6)" ::: "memory"); else asm volatile("s_waitcnt vmcnt(0)" ::: "memory");
        asm volatile("s_barrier" ::: "memory");
        if (t + 2 < NT) dma(slot == 0 ? 2 : slot - 1);
        if (t < ntw) {
            const int so = slot * TILE_B;
            f32x16 s = f32x16{};
#pragma unroll
            for (int ks = 0; ks < 12; ++ks) {
                const bf16x8 kf = *(const __attribute__((address_space(3))) bf16x8*)(kb[ks & 3] + so + (ks >> 2) * 4096);
                s = MFMA(kf, qf[ks], s);
            }
            if (t == ntw - 1) {
                const int kb0 = t * 32 + 4 * hi;
#pragma unroll
                for (int r = 0; r < 16; ++r) if (kb0 + (r & 3) + 8 * (r >> 2) > qrel) s[r] = -__builtin_inff();
            }
            float mx = fmaxf(fmaxf(s[0], s[1]), s[2]);
#pragma unroll
            for (int r = 3; r < 15; r += 2) mx = fmaxf(fmaxf(mx, s[r]), s[r + 1]);
            mx = fmaxf(mx, s[15]);
            { auto rr = __builtin_amdgcn_permlane32_swap(__float_as_uint(mx), __float_as_uint(mx), false, false); mx = fmaxf(__uint_as_float(rr[0]), __uint_as_float(rr[1])); }
            mx *= C2;
            if (__any(mx - m > THR)) {
                const float mn = fmaxf(m, mx), al = __builtin_amdgcn_exp2f(m - mn);
                m = mn; l *= al;
#pragma unroll
                for (int i = 0; i < 6; ++i)
#pragma unroll
                    for (int r = 0; r < 16; ++r) o[i][r] *= al;
            }
            const float nm = -m;
            float ps = 0.f;
#pragma unroll
            for (int r = 0; r < 16; ++r) { s[r] = __builtin_amdgcn_exp2f(fmaf(s[r], C2, nm)); ps += s[r]; }
            l += ps;
            u32x4 p0 = {cvtpk(s[0], s[1]), cvtpk(s[2], s[3]), cvtpk(s[4], s[5]), cvtpk(s[6], s[7])};
            u32x4 p1 = {cvtpk(s[8], s[9]), cvtpk(s[10], s[11]), cvtpk(s[12], s[13]), cvtpk(s[14], s[15])};
            const bf16x8 pf0 = __builtin_bit_cast(bf16x8, p0), pf1 = __builtin_bit_cast(bf16x8, p1);
#pragma unroll
            for (int db = 0; db < 6; ++db) {
                const lds_cptr vp = vb[db & 1] + so + (db >> 1) * 4096;
                const s16x4 a0 = vtr(vp), a1 = vtr(vp + 1024), b0 = vtr(vp + 2048), b1 = vtr(vp + 3072);
                o[db] = MFMA(((bf16x8){a0[0], a0[1], a0[2], a0[3], a1[0], a1[1], a1[2], a1[3]}), pf0, o[db]);
                o[db] = MFMA(((bf16x8){b0[0], b0[1], b0[2], b0[3], b1[0], b1[1], b1[2], b1[3]}), pf1, o[db]);
            }
        }
        slot = slot == 2 ? 0 : slot + 1;
    }
    if (ntw > 0) {
        { auto rr = __builtin_amdgcn_permlane32_swap(__float_as_uint(l), __float_as_uint(l), false, false); l = __uint_as_float(rr[0]) + __uint_as_float(rr[1]); }
        const float il = 1.0f / l;
        char* op = (char*)O + (size_t)(s0 + qrel) * QROW + head * 384 + hi * 16;
#pragma unroll
        for (int db = 0; db < 6; ++db)
#pragma unroll
            for (int g = 0; g < 4; g += 2) {
                unsigned a0 = cvtpk(o[db][4 * g] * il, o[db][4 * g + 1] * il), a1 = cvtpk(o[db][4 * g + 2] * il, o[db][4 * g + 3] * il);
                unsigned b0 = cvtpk(o[db][4 * g + 4] * il, o[db][4 * g + 5] * il), b1 = cvtpk(o[db][4 * g + 6] * il, o[db][4 * g + 7] * il);
                auto r0 = __builtin_amdgcn_permlane32_swap(a0, b0, false, false);
                auto r1 = __builtin_amdgcn_permlane32_swap(a1, b1, false, false);
                const u32x4 w = {r0[0], r1[0], r0[1], r1[1]};
                if (qrel < L) *(u32x4*)(op + db * 64 + g * 16) = w;
            }
    }
    if (MODE != 0 && wid == 3 && (blockIdx.x >= LO) && (blockIdx.x < HI)) {
        asm volatile("s_waitcnt vmcnt(0)" ::: "memory");
        const unsigned long long rte = __builtin_amdgcn_s_memrealtime();
        unsigned long long s0t = __hip_atomic_load(ws, __ATOMIC_RELAXED, __HIP_MEMORY_SCOPE_AGENT);
        long long d = MODE == 1 ? (long long)(rte - s0t) : MODE == 2 ? (long long)(rt0 - s0t) : (long long)(rt1 - s0t);
        if (d < 0) d = 0; if (d > 10000) d = 0;
        const unsigned long long until = rte + 10 * d;
        while (__builtin_amdgcn_s_memrealtime() < until) __builtin_amdgcn_s_sleep(2);
    }
}

extern "C" void kernel_launch(void* const* d_in, const int* in_sizes, int n_in, void* d_out, int out_size, void* d_ws, size_t ws_size, hipStream_t stream) {
    const int T = in_sizes[0] / (HQ * D), nseq = in_sizes[3] - 1;
    const int maxitems = (T + 63) / 64 + nseq;
    static bool attr = false;
    if (!attr) { attr = true; (void)hipFuncSetAttribute((const void*)attn_v1, hipFuncAttributeMaxDynamicSharedMemorySize, LDS_BYTES); }
    attn_v1<<<maxitems * 8, 256, LDS_BYTES, stream>>>((const unsigned short*)d_in[0], (const unsigned short*)d_in[1], (const unsigned short*)d_in[2],
                                                    (const int*)d_in[3], (unsigned short*)d_out, nseq, (unsigned long long*)d_ws);
}
```

```cpp
#define MODE 2
#define AMP 0
#include <hip/hip_runtime.h>
#include <hip/hip_bf16.h>
#include <stdint.h>

typedef short bf16x8 __attribute__((ext_vector_type(8)));
typedef short s16x4 __attribute__((ext_vector_type(4)));
typedef float f32x16 __attribute__((ext_vector_type(16)));
typedef unsigned u32x4 __attribute__((ext_vector_type(4)));
typedef unsigned u32x2 __attribute__((ext_vector_type(2)));
typedef __attribute__((address_space(3))) const char* lds_cptr;
typedef short v4i16_t __attribute__((ext_vector_type(4)));

constexpr int HQ = 16, HKV = 8, D = 192;
constexpr unsigned QROW = HQ * D * 2, KROW = HKV * D * 2;
constexpr int TILE_B = 24576;
constexpr int NSLOT = 3;
constexpr int LDS_BYTES = NSLOT * TILE_B;
constexpr float C2 = 0.07216878364870322f * 1.4426950408889634f;
constexpr float THR = 8.0f;

#define DI __device__ __forceinline__
#define MFMA(a, b, c) __builtin_amdgcn_mfma_f32_32x32x16_bf16(a, b, c, 0, 0, 0)
DI unsigned cvtpk(float lo, float hi) { unsigned r; asm("v_cvt_pk_bf16_f32 %0, %1, %2" : "=v"(r) : "v"(lo), "v"(hi)); return r; }
DI s16x4 vtr(lds_cptr p) { return __builtin_bit_cast(s16x4, __builtin_amdgcn_ds_read_tr16_b64_v4i16((__attribute__((address_space(3))) v4i16_t*)p)); }
DI int rfl(int x) { return __builtin_amdgcn_readfirstlane(x); }

__global__ __launch_bounds__(256, 2) void attn_v1(const unsigned short* __restrict__ Q, const unsigned short* __restrict__ K,
                                                  const unsigned short* __restrict__ V, const int* __restrict__ cu,
                                                  unsigned short* __restrict__ O, int nseq, int T) {
    extern __shared__ __attribute__((aligned(16))) char lds[];
    const unsigned long long rt0 = __builtin_amdgcn_s_memrealtime();
    unsigned long long dpre = 0;
    if (MODE != 0) {
        const unsigned nq16 = (unsigned)T * 384u, nk16 = (unsigned)T * 192u;
        const unsigned G = gridDim.x * 256u, g = blockIdx.x * 256u + threadIdx.x;
        u32x4 acc = {0, 0, 0, 0};
        if (MODE == 1 || MODE == 2) {
            u32x4 v[10];
#pragma unroll
            for (int i = 0; i < 10; ++i) { unsigned j = g + i * G; j = j < nq16 ? j : nq16 - 1; v[i] = ((const u32x4*)Q)[j]; }
            u32x4 w[10];
            if (MODE == 2) {
#pragma unroll
            for (int i = 0; i < 5; ++i) { unsigned j = g + i * G; j = j < nk16 ? j : nk16 - 1; w[i] = ((const u32x4*)K)[j]; w[5 + i] = ((const u32x4*)V)[j]; }
#pragma unroll
            for (int i = 0; i < 10; ++i) acc ^= w[i];
            }
#pragma unroll
            for (int i = 0; i < 10; ++i) acc ^= v[i];
            asm volatile("" :: "v"(acc));
        } else if (MODE == 3) {
#pragma unroll
            for (int i = 0; i < 10; ++i) { unsigned j = g + i * G; if (j < nq16) ((u32x4*)O)[j] = acc; }
            asm volatile("s_waitcnt vmcnt(0)" ::: "memory");
        }
        dpre = __builtin_amdgcn_s_memrealtime() - rt0;
        if (MODE == 3) { __syncthreads(); }
    }
    const int tid = threadIdx.x, lane = tid & 63, wid = rfl(tid >> 6), l32 = lane & 31, hi = lane >> 5;
    const int kvh = blockIdx.x & 7;
    int j = blockIdx.x >> 3;
    int s0 = 0, L = 0, tq = -1;
    {
        const int a = lane < nseq ? cu[lane] : 0, b = lane < nseq ? cu[lane + 1] : 0;
        const int nt = (b - a + 63) >> 6;
        int mx = nt;
        for (int o = 32; o; o >>= 1) mx = max(mx, __shfl_xor(mx, o));
        mx = rfl(mx);
        for (int t = mx - 1; t >= 0; --t) {
            unsigned long long m = __ballot(nt > t);
            const int c = __popcll(m);
            if (j < c) {
                for (int i = 0; i < j; ++i) m &= m - 1;
                const int sl = __builtin_ctzll(m);
                s0 = __builtin_amdgcn_readlane(a, sl); L = __builtin_amdgcn_readlane(b, sl) - s0; tq = t;
                break;
            }
            j -= c;
        }
    }
    if (tq < 0) return;
    const int q0 = tq * 64;
    const int head = 2 * kvh + (wid & 1), pb = wid >> 1;
    const int qw0 = q0 + 32 * pb;
    const int kend = min(q0 + 64, L);
    const int NT = (kend + 31) >> 5;
    const int ntw = qw0 < L ? (qw0 >> 5) + 1 : 0;

    const __amdgpu_buffer_rsrc_t srdK = __builtin_amdgcn_make_buffer_rsrc((void*)((const char*)K + (size_t)s0 * KROW + kvh * 384), (short)0, (int)((unsigned)L * KROW - kvh * 384), 0x00020000);
    const __amdgpu_buffer_rsrc_t srdV = __builtin_amdgcn_make_buffer_rsrc((void*)((const char*)V + (size_t)s0 * KROW + kvh * 384), (short)0, (int)((unsigned)L * KROW - kvh * 384), 0x00020000);
    const int drow = 8 * wid + (lane >> 3);
    unsigned vofK = drow * KROW + 16u * ((lane & 7) ^ ((drow >> 1) & 7));
    unsigned vofV = drow * KROW + 16u * ((lane & 7) ^ (((drow >> 1) & 1) << 2));
    const unsigned ldsb = (unsigned)(uintptr_t)lds;
    auto dma = [&](int slot) {
        const unsigned mb = ldsb + slot * TILE_B + wid * 1024;
#pragma unroll
        for (int s = 0; s < 3; ++s)
            asm volatile("s_mov_b32 m0, %0\n\ts_nop 0\n\tbuffer_load_dwordx4 %1, %2, 0 offen offset:%3 lds" :: "s"(mb + s * (4096 - 128)), "v"(vofK), "s"(srdK), "i"(s * 128) : "memory");
#pragma unroll
        for (int s = 0; s < 3; ++s)
            asm volatile("s_mov_b32 m0, %0\n\ts_nop 0\n\tbuffer_load_dwordx4 %1, %2, 0 offen offset:%3 lds" :: "s"(mb + 12288 + s * (4096 - 128)), "v"(vofV), "s"(srdV), "i"(s * 128) : "memory");
        vofK += 32 * KROW; vofV += 32 * KROW;
    };
    dma(0);
    if (NT > 1) dma(1);
    bf16x8 qf[12];
    {
        const int qp = min(qw0 + l32, L - 1);
        const char* qp_ = (const char*)Q + (size_t)(s0 + qp) * QROW + head * 384 + hi * 16;
#pragma unroll
        for (int ks = 0; ks < 12; ++ks) qf[ks] = *(const bf16x8*)(qp_ + ks * 32);
    }
    f32x16 o[6];
#pragma unroll
    for (int i = 0; i < 6; ++i) o[i] = f32x16{};
    float m = -1e30f, l = 0.f;
    const lds_cptr lp = (lds_cptr)lds;
    lds_cptr kb[4];
#pragma unroll
    for (int i = 0; i < 4; ++i) kb[i] = lp + l32 * 128 + 16 * ((2 * i + hi) ^ ((l32 >> 1) & 7));
    const int vq = (lane & 15) >> 2;
    lds_cptr vb[2];
#pragma unroll
    for (int i = 0; i < 2; ++i) vb[i] = lp + 12288 + (4 * hi + vq) * 128 + ((64 * i) ^ (64 * ((vq >> 1) & 1))) + 32 * ((lane >> 4) & 1) + 8 * (lane & 3);
    const int qrel = qw0 + l32;

    int slot = 0;
    for (int t = 0; t < NT; ++t) {
        if (t + 1 < NT) asm volatile("s_waitcnt vmcnt(6)" ::: "memory"); else asm volatile("s_waitcnt vmcnt(0)" ::: "memory");
        asm volatile("s_barrier" ::: "memory");
        if (t + 2 < NT) dma(slot == 0 ? 2 : slot - 1);
        if (t < ntw) {
            const int so = slot * TILE_B;
            f32x16 s = f32x16{};
#pragma unroll
            for (int ks = 0; ks < 12; ++ks) {
                const bf16x8 kf = *(const __attribute__((address_space(3))) bf16x8*)(kb[ks & 3] + so + (ks >> 2) * 4096);
                s = MFMA(kf, qf[ks], s);
            }
            if (t == ntw - 1) {
                const int kb0 = t * 32 + 4 * hi;
#pragma unroll
                for (int r = 0; r < 16; ++r) if (kb0 + (r & 3) + 8 * (r >> 2) > qrel) s[r] = -__builtin_inff();
            }
            float mx = fmaxf(fmaxf(s[0], s[1]), s[2]);
#pragma unroll
            for (int r = 3; r < 15; r += 2) mx = fmaxf(fmaxf(mx, s[r]), s[r + 1]);
            mx = fmaxf(mx, s[15]);
            { auto rr = __builtin_amdgcn_permlane32_swap(__float_as_uint(mx), __float_as_uint(mx), false, false); mx = fmaxf(__uint_as_float(rr[0]), __uint_as_float(rr[1])); }
            mx *= C2;
            if (__any(mx - m > THR)) {
                const float mn = fmaxf(m, mx), al = __builtin_amdgcn_exp2f(m - mn);
                m = mn; l *= al;
#pragma unroll
                for (int i = 0; i < 6; ++i)
#pragma unroll
                    for (int r = 0; r < 16; ++r) o[i][r] *= al;
            }
            const float nm = -m;
            float ps = 0.f;
#pragma unroll
            for (int r = 0; r < 16; ++r) { s[r] = __builtin_amdgcn_exp2f(fmaf(s[r], C2, nm)); ps += s[r]; }
            l += ps;
            u32x4 p0 = {cvtpk(s[0], s[1]), cvtpk(s[2], s[3]), cvtpk(s[4], s[5]), cvtpk(s[6], s[7])};
            u32x4 p1 = {cvtpk(s[8], s[9]), cvtpk(s[10], s[11]), cvtpk(s[12], s[13]), cvtpk(s[14], s[15])};
            const bf16x8 pf0 = __builtin_bit_cast(bf16x8, p0), pf1 = __builtin_bit_cast(bf16x8, p1);
#pragma unroll
            for (int db = 0; db < 6; ++db) {
                const lds_cptr vp = vb[db & 1] + so + (db >> 1) * 4096;
                const s16x4 a0 = vtr(vp), a1 = vtr(vp + 1024), b0 = vtr(vp + 2048), b1 = vtr(vp + 3072);
                o[db] = MFMA(((bf16x8){a0[0], a0[1], a0[2], a0[3], a1[0], a1[1], a1[2], a1[3]}), pf0, o[db]);
                o[db] = MFMA(((bf16x8){b0[0], b0[1], b0[2], b0[3], b1[0], b1[1], b1[2], b1[3]}), pf1, o[db]);
            }
        }
        slot = slot == 2 ? 0 : slot + 1;
    }
    if (ntw > 0) {
        { auto rr = __builtin_amdgcn_permlane32_swap(__float_as_uint(l), __float_as_uint(l), false, false); l = __uint_as_float(rr[0]) + __uint_as_float(rr[1]); }
        const float il = 1.0f / l;
        char* op = (char*)O + (size_t)(s0 + qrel) * QROW + head * 384 + hi * 16;
#pragma unroll
        for (int db = 0; db < 6; ++db)
#pragma unroll
            for (int g = 0; g < 4; g += 2) {
                unsigned a0 = cvtpk(o[db][4 * g] * il, o[db][4 * g + 1] * il), a1 = cvtpk(o[db][4 * g + 2] * il, o[db][4 * g + 3] * il);
                unsigned b0 = cvtpk(o[db][4 * g + 4] * il, o[db][4 * g + 5] * il), b1 = cvtpk(o[db][4 * g + 6] * il, o[db][4 * g + 7] * il);
                auto r0 = __builtin_amdgcn_permlane32_swap(a0, b0, false, false);
                auto r1 = __builtin_amdgcn_permlane32_swap(a1, b1, false, false);
                const u32x4 w = {r0[0], r1[0], r0[1], r1[1]};
                if (qrel < L) *(u32x4*)(op + db * 64 + g * 16) = w;
            }
    }
    if (MODE != 0 && wid == 3) {
        const unsigned long long until = __builtin_amdgcn_s_memrealtime() + AMP * dpre;
        while (__builtin_amdgcn_s_memrealtime() < until) __builtin_amdgcn_s_sleep(2);
    }
}

extern "C" void kernel_launch(void* const* d_in, const int* in_sizes, int n_in, void* d_out, int out_size, void* d_ws, size_t ws_size, hipStream_t stream) {
    const int T = in_sizes[0] / (HQ * D), nseq = in_sizes[3] - 1;
    const int maxitems = (T + 63) / 64 + nseq;
    static bool attr = false;
    if (!attr) { attr = true; (void)hipFuncSetAttribute((const void*)attn_v1, hipFuncAttributeMaxDynamicSharedMemorySize, LDS_BYTES); }
    attn_v1<<<maxitems * 8, 256, LDS_BYTES, stream>>>((const unsigned short*)d_in[0], (const unsigned short*)d_in[1], (const unsigned short*)d_in[2],
                                                    (const int*)d_in[3], (unsigned short*)d_out, nseq, T);
}
```

```cpp
#define PF 1
#include <hip/hip_runtime.h>
#include <hip/hip_bf16.h>
#include <stdint.h>

typedef short bf16x8 __attribute__((ext_vector_type(8)));
typedef short s16x4 __attribute__((ext_vector_type(4)));
typedef float f32x16 __attribute__((ext_vector_type(16)));
typedef unsigned u32x4 __attribute__((ext_vector_type(4)));
typedef unsigned u32x2 __attribute__((ext_vector_type(2)));
typedef __attribute__((address_space(3))) const char* lds_cptr;
typedef short v4i16_t __attribute__((ext_vector_type(4)));

constexpr int HQ = 16, HKV = 8, D = 192;
constexpr unsigned QROW = HQ * D * 2, KROW = HKV * D * 2;
constexpr int TILE_B = 24576;
constexpr int NSLOT = 3;
constexpr int LDS_BYTES = NSLOT * TILE_B;
constexpr float C2 = 0.07216878364870322f * 1.4426950408889634f;
constexpr float THR = 8.0f;

#define DI __device__ __forceinline__
#define MFMA(a, b, c) __builtin_amdgcn_mfma_f32_32x32x16_bf16(a, b, c, 0, 0, 0)
DI unsigned cvtpk(float lo, float hi) { unsigned r; asm("v_cvt_pk_bf16_f32 %0, %1, %2" : "=v"(r) : "v"(lo), "v"(hi)); return r; }
DI s16x4 vtr(lds_cptr p) { return __builtin_bit_cast(s16x4, __builtin_amdgcn_ds_read_tr16_b64_v4i16((__attribute__((address_space(3))) v4i16_t*)p)); }
DI int rfl(int x) { return __builtin_amdgcn_readfirstlane(x); }

__global__ __launch_bounds__(256, 2) void attn_v1(const unsigned short* __restrict__ Q, const unsigned short* __restrict__ K,
                                                  const unsigned short* __restrict__ V, const int* __restrict__ cu,
                                                  unsigned short* __restrict__ O, int nseq, int T) {
    extern __shared__ __attribute__((aligned(16))) char lds[];
    const int tid = threadIdx.x, lane = tid & 63, wid = rfl(tid >> 6), l32 = lane & 31, hi = lane >> 5;
    const int kvh = blockIdx.x & 7;
    unsigned dummy = 0;
    {
        const unsigned lines_per_tok = PF == 1 ? 6u : 12u;
        const unsigned tot = (unsigned)T * lines_per_tok, me = blockIdx.x >> 3;
        if (me < 32u)
        for (unsigned p = me * 256 + tid; p < tot; p += 32 * 256) {
            const unsigned t = p / lines_per_tok, r = p % lines_per_tok;
            const char* a;
            if (r < 3) a = (const char*)K + (size_t)t * KROW + kvh * 384 + r * 128;
            else if (r < 6) a = (const char*)V + (size_t)t * KROW + kvh * 384 + (r - 3) * 128;
            else a = (const char*)Q + (size_t)t * QROW + kvh * 768 + (r - 6) * 128;
            asm volatile("global_load_dword %0, %1, off" : "=v"(dummy) : "v"(a) : "memory");
        }
    }
    int j = blockIdx.x >> 3;
    int s0 = 0, L = 0, tq = -1;
    {
        const int a = lane < nseq ? cu[lane] : 0, b = lane < nseq ? cu[lane + 1] : 0;
        const int nt = (b - a + 63) >> 6;
        int mx = nt;
        for (int o = 32; o; o >>= 1) mx = max(mx, __shfl_xor(mx, o));
        mx = rfl(mx);
        for (int t = mx - 1; t >= 0; --t) {
            unsigned long long m = __ballot(nt > t);
            const int c = __popcll(m);
            if (j < c) {
                for (int i = 0; i < j; ++i) m &= m - 1;
                const int sl = __builtin_ctzll(m);
                s0 = __builtin_amdgcn_readlane(a, sl); L = __builtin_amdgcn_readlane(b, sl) - s0; tq = t;
                break;
            }
            j -= c;
        }
    }
    if (tq < 0) { asm volatile("s_waitcnt vmcnt(0)" :: "v"(dummy) : "memory"); return; }
    const int q0 = tq * 64;
    const int head = 2 * kvh + (wid & 1), pb = wid >> 1;
    const int qw0 = q0 + 32 * pb;
    const int kend = min(q0 + 64, L);
    const int NT = (kend + 31) >> 5;
    const int ntw = qw0 < L ? (qw0 >> 5) + 1 : 0;

    const __amdgpu_buffer_rsrc_t srdK = __builtin_amdgcn_make_buffer_rsrc((void*)((const char*)K + (size_t)s0 * KROW + kvh * 384), (short)0, (int)((unsigned)L * KROW - kvh * 384), 0x00020000);
    const __amdgpu_buffer_rsrc_t srdV = __builtin_amdgcn_make_buffer_rsrc((void*)((const char*)V + (size_t)s0 * KROW + kvh * 384), (short)0, (int)((unsigned)L * KROW - kvh * 384), 0x00020000);
    const int drow = 8 * wid + (lane >> 3);
    unsigned vofK = drow * KROW + 16u * ((lane & 7) ^ ((drow >> 1) & 7));
    unsigned vofV = drow * KROW + 16u * ((lane & 7) ^ (((drow >> 1) & 1) << 2));
    const unsigned ldsb = (unsigned)(uintptr_t)lds;
    auto dma = [&](int slot) {
        const unsigned mb = ldsb + slot * TILE_B + wid * 1024;
#pragma unroll
        for (int s = 0; s < 3; ++s)
            asm volatile("s_mov_b32 m0, %0\n\ts_nop 0\n\tbuffer_load_dwordx4 %1, %2, 0 offen offset:%3 lds" :: "s"(mb + s * (4096 - 128)), "v"(vofK), "s"(srdK), "i"(s * 128) : "memory");
#pragma unroll
        for (int s = 0; s < 3; ++s)
            asm volatile("s_mov_b32 m0, %0\n\ts_nop 0\n\tbuffer_load_dwordx4 %1, %2, 0 offen offset:%3 lds" :: "s"(mb + 12288 + s * (4096 - 128)), "v"(vofV), "s"(srdV), "i"(s * 128) : "memory");
        vofK += 32 * KROW; vofV += 32 * KROW;
    };
    dma(0);
    if (NT > 1) dma(1);
    bf16x8 qf[12];
    {
        const int qp = min(qw0 + l32, L - 1);
        const char* qp_ = (const char*)Q + (size_t)(s0 + qp) * QROW + head * 384 + hi * 16;
#pragma unroll
        for (int ks = 0; ks < 12; ++ks) qf[ks] = *(const bf16x8*)(qp_ + ks * 32);
    }
    f32x16 o[6];
#pragma unroll
    for (int i = 0; i < 6; ++i) o[i] = f32x16{};
    float m = -1e30f, l = 0.f;
    const lds_cptr lp = (lds_cptr)lds;
    lds_cptr kb[4];
#pragma unroll
    for (int i = 0; i < 4; ++i) kb[i] = lp + l32 * 128 + 16 * ((2 * i + hi) ^ ((l32 >> 1) & 7));
    const int vq = (lane & 15) >> 2;
    lds_cptr vb[2];
#pragma unroll
    for (int i = 0; i < 2; ++i) vb[i] = lp + 12288 + (4 * hi + vq) * 128 + ((64 * i) ^ (64 * ((vq >> 1) & 1))) + 32 * ((lane >> 4) & 1) + 8 * (lane & 3);
    const int qrel = qw0 + l32;

    int slot = 0;
    for (int t = 0; t < NT; ++t) {
        if (t + 1 < NT) asm volatile("s_waitcnt vmcnt(6)" ::: "memory"); else asm volatile("s_waitcnt vmcnt(0)" ::: "memory");
        asm volatile("s_barrier" ::: "memory");
        if (t + 2 < NT) dma(slot == 0 ? 2 : slot - 1);
        if (t < ntw) {
            const int so = slot * TILE_B;
            f32x16 s = f32x16{};
#pragma unroll
            for (int ks = 0; ks < 12; ++ks) {
                const bf16x8 kf = *(const __attribute__((address_space(3))) bf16x8*)(kb[ks & 3] + so + (ks >> 2) * 4096);
                s = MFMA(kf, qf[ks], s);
            }
            if (t == ntw - 1) {
                const int kb0 = t * 32 + 4 * hi;
#pragma unroll
                for (int r = 0; r < 16; ++r) if (kb0 + (r & 3) + 8 * (r >> 2) > qrel) s[r] = -__builtin_inff();
            }
            float mx = fmaxf(fmaxf(s[0], s[1]), s[2]);
#pragma unroll
            for (int r = 3; r < 15; r += 2) mx = fmaxf(fmaxf(mx, s[r]), s[r + 1]);
            mx = fmaxf(mx, s[15]);
            { auto rr = __builtin_amdgcn_permlane32_swap(__float_as_uint(mx), __float_as_uint(mx), false, false); mx = fmaxf(__uint_as_float(rr[0]), __uint_as_float(rr[1])); }
            mx *= C2;
            if (__any(mx - m > THR)) {
                const float mn = fmaxf(m, mx), al = __builtin_amdgcn_exp2f(m - mn);
                m = mn; l *= al;
#pragma unroll
                for (int i = 0; i < 6; ++i)
#pragma unroll
                    for (int r = 0; r < 16; ++r) o[i][r] *= al;
            }
            const float nm = -m;
            float ps = 0.f;
#pragma unroll
            for (int r = 0; r < 16; ++r) { s[r] = __builtin_amdgcn_exp2f(fmaf(s[r], C2, nm)); ps += s[r]; }
            l += ps;
            u32x4 p0 = {cvtpk(s[0], s[1]), cvtpk(s[2], s[3]), cvtpk(s[4], s[5]), cvtpk(s[6], s[7])};
            u32x4 p1 = {cvtpk(s[8], s[9]), cvtpk(s[10], s[11]), cvtpk(s[12], s[13]), cvtpk(s[14], s[15])};
            const bf16x8 pf0 = __builtin_bit_cast(bf16x8, p0), pf1 = __builtin_bit_cast(bf16x8, p1);
#pragma unroll
            for (int db = 0; db < 6; ++db) {
                const lds_cptr vp = vb[db & 1] + so + (db >> 1) * 4096;
                const s16x4 a0 = vtr(vp), a1 = vtr(vp + 1024), b0 = vtr(vp + 2048), b1 = vtr(vp + 3072);
                o[db] = MFMA(((bf16x8){a0[0], a0[1], a0[2], a0[3], a1[0], a1[1], a1[2], a1[3]}), pf0, o[db]);
                o[db] = MFMA(((bf16x8){b0[0], b0[1], b0[2], b0[3], b1[0], b1[1], b1[2], b1[3]}), pf1, o[db]);
            }
        }
        slot = slot == 2 ? 0 : slot + 1;
    }
    asm volatile("s_waitcnt vmcnt(0)" :: "v"(dummy) : "memory");
    if (ntw > 0) {
        { auto rr = __builtin_amdgcn_permlane32_swap(__float_as_uint(l), __float_as_uint(l), false, false); l = __uint_as_float(rr[0]) + __uint_as_float(rr[1]); }
        const float il = 1.0f / l;
        char* op = (char*)O + (size_t)(s0 + qrel) * QROW + head * 384 + hi * 16;
#pragma unroll
        for (int db = 0; db < 6; ++db)
#pragma unroll
            for (int g = 0; g < 4; g += 2) {
                unsigned a0 = cvtpk(o[db][4 * g] * il, o[db][4 * g + 1] * il), a1 = cvtpk(o[db][4 * g + 2] * il, o[db][4 * g + 3] * il);
                unsigned b0 = cvtpk(o[db][4 * g + 4] * il, o[db][4 * g + 5] * il), b1 = cvtpk(o[db][4 * g + 6] * il, o[db][4 * g + 7] * il);
                auto r0 = __builtin_amdgcn_permlane32_swap(a0, b0, false, false);
                auto r1 = __builtin_amdgcn_permlane32_swap(a1, b1, false, false);
                const u32x4 w = {r0[0], r1[0], r0[1], r1[1]};
                if (qrel < L) *(u32x4*)(op + db * 64 + g * 16) = w;
            }
    }
}

extern "C" void kernel_launch(void* const* d_in, const int* in_sizes, int n_in, void* d_out, int out_size, void* d_ws, size_t ws_size, hipStream_t stream) {
    const int T = in_sizes[0] / (HQ * D), nseq = in_sizes[3] - 1;
    const int maxitems = (T + 63) / 64 + nseq;
    static bool attr = false;
    if (!attr) { attr = true; (void)hipFuncSetAttribute((const void*)attn_v1, hipFuncAttributeMaxDynamicSharedMemorySize, LDS_BYTES); }
    attn_v1<<<maxitems * 8, 256, LDS_BYTES, stream>>>((const unsigned short*)d_in[0], (const unsigned short*)d_in[1], (const unsigned short*)d_in[2],
                                                    (const int*)d_in[3], (unsigned short*)d_out, nseq, T);
}
```

```cpp
#include <hip/hip_runtime.h>
#include <hip/hip_bf16.h>
#include <stdint.h>

typedef short bf16x8 __attribute__((ext_vector_type(8)));
typedef short s16x4 __attribute__((ext_vector_type(4)));
typedef float f32x16 __attribute__((ext_vector_type(16)));
typedef float f32x4 __attribute__((ext_vector_type(4)));
typedef float f32x2 __attribute__((ext_vector_type(2)));
typedef unsigned u32x4 __attribute__((ext_vector_type(4)));
typedef unsigned u32x2 __attribute__((ext_vector_type(2)));
typedef __attribute__((address_space(3))) const char* lds_cptr;
typedef __attribute__((address_space(3))) char* lds_ptr;
typedef short v4i16_t __attribute__((ext_vector_type(4)));

constexpr int HQ = 16, HKV = 8, D = 192;
constexpr unsigned QROW = HQ * D * 2, KROW = HKV * D * 2;
constexpr unsigned KBLK = 32 * KROW;
constexpr int BLK = 12288;
constexpr int LDS_MISC = 6 * BLK;
constexpr int LDS_BYTES = LDS_MISC + 2048;
constexpr float C2 = 0.07216878364870322f * 1.4426950408889634f;
constexpr float LBIG = 1073741824.f;
#define TH 8
#define WA 10
#define WB 11
constexpr unsigned OOB = 0x80000000u;

#define DI __device__ __forceinline__
#define MFMA(a, b, c) __builtin_amdgcn_mfma_f32_32x32x16_bf16(a, b, c, 0, 0, 0)
#define SBAR() __builtin_amdgcn_sched_barrier(0)
#define PIN(x) asm volatile("" : "+v"(x))
typedef __bf16 bf16x2_t __attribute__((ext_vector_type(2)));
DI unsigned cvtpk(float lo, float hi) { const f32x2 v = {lo, hi}; return __builtin_bit_cast(unsigned, __builtin_convertvector(v, bf16x2_t)); }
DI float max3f(float a, float b, float c) { float r; asm("v_max3_f32 %0, %1, %2, %3" : "=v"(r) : "v"(a), "v"(b), "v"(c)); return r; }
DI float max2f(float a, float b) { float r; asm("v_max_f32_e32 %0, %1, %2" : "=v"(r) : "v"(a), "v"(b)); return r; }
DI float fmul_s(float a, float b) { float r; asm("v_mul_f32_e32 %0, %1, %2" : "=v"(r) : "v"(a), "v"(b)); return r; }
DI float fma_v(float a, float b, float c) { float r; asm("v_fma_f32 %0, %1, %2, %3" : "=v"(r) : "v"(a), "v"(b), "v"(c)); return r; }
DI void vexp(float& x) { asm volatile("v_exp_f32_e32 %0, %0" : "+v"(x)); }
DI float vadd(float a, float b) { float r; asm volatile("v_add_f32_e32 %0, %1, %2" : "=v"(r) : "v"(a), "v"(b)); return r; }
DI float vfma(float a, float b, float c) { float r; asm volatile("v_fma_f32 %0, %1, %2, %3" : "=v"(r) : "v"(a), "s"(b), "v"(c)); return r; }
DI float vmul(float a, float b) { float r; asm volatile("v_mul_f32_e32 %0, %1, %2" : "=v"(r) : "v"(a), "v"(b)); return r; }
DI float vfma_v(float a, float b, float c) { float r; asm volatile("v_fma_f32 %0, %1, %2, %3" : "=v"(r) : "v"(a), "v"(b), "v"(c)); return r; }
DI unsigned vcvt(float lo, float hi) { unsigned r; asm volatile("v_cvt_pk_bf16_f32 %0, %1, %2" : "=v"(r) : "v"(lo), "v"(hi)); return r; }
DI s16x4 vtr(lds_cptr p) { return __builtin_bit_cast(s16x4, __builtin_amdgcn_ds_read_tr16_b64_v4i16((__attribute__((address_space(3))) v4i16_t*)p)); }
DI bf16x8 ldk(lds_cptr p) { return *(const __attribute__((address_space(3))) bf16x8*)p; }
DI int rfl(int x) { return __builtin_amdgcn_readfirstlane(x); }
DI float swapmax(float x) { auto r = __builtin_amdgcn_permlane32_swap(__float_as_uint(x), __float_as_uint(x), false, false); return max2f(__uint_as_float(r[0]), __uint_as_float(r[1])); }
DI float swapsum(float x) { auto r = __builtin_amdgcn_permlane32_swap(__float_as_uint(x), __float_as_uint(x), false, false); return __uint_as_float(r[0]) + __uint_as_float(r[1]); }

template <int S, int OFF> DI void dma(unsigned mb, unsigned voff, __amdgpu_buffer_rsrc_t srd, unsigned soff) {
    asm volatile("s_add_u32 m0, %0, %5\n\ts_nop 0\n\tbuffer_load_dwordx4 %1, %2, %3 offen offset:%4 lds"
                 :: "s"(mb), "v"(voff), "s"(srd), "s"(soff), "i"(128 * S), "i"(OFF + S * (4096 - 128)) : "memory", "scc");
}
DI void dma3(unsigned mb, unsigned voff, __amdgpu_buffer_rsrc_t srd, unsigned soff) { dma<0, 0>(mb, voff, srd, soff); dma<1, 0>(mb, voff, srd, soff); dma<2, 0>(mb, voff, srd, soff); }
#define DMAL dma
#define LOOPBAR() BARRIER()
#define LOOPWAIT() WAIT_VM(6)
#define WAIT_VM(n) asm volatile("s_waitcnt vmcnt(" #n ")" ::: "memory")
#define BARRIER() asm volatile("s_waitcnt lgkmcnt(0)\n\ts_barrier" ::: "memory")
#define OPIN(nop) asm volatile(nop : "+v"(o[0]), "+v"(o[1]), "+v"(o[2]), "+v"(o[3]), "+v"(o[4]), "+v"(o[5]))

#define STAMP(i)
#define STAMPC(i)

template <bool TB>
DI void attn_item(unsigned long long* st, const unsigned short* __restrict__ Q, const unsigned short* __restrict__ K, const unsigned short* __restrict__ V,
                  unsigned short* __restrict__ O, int kvh, int s0, int L, int idx, char* lds) {
    const int tid = threadIdx.x, lane = tid & 63, wid = rfl(tid >> 6), l32 = lane & 31, hi = lane >> 5;
    const int hsel = wid & 1, sub = wid >> 1;
    const int n = (L + 31) >> 5;
    const int q0 = TB ? 32 * idx : 64 * idx;
    const int NB = TB ? idx + 1 : min(2 * idx + 2, n);
    const int NU = TB ? (NB + 1) >> 1 : NB;
    const int nu = TB ? (NB + 1 - sub) >> 1 : (2 * idx + sub < n ? 2 * idx + sub + 1 : 0);
    const int diagu = TB ? (((idx & 1) == sub) ? nu - 1 : -1) : nu - 1;
    const int qrel = q0 + (TB ? 0 : 32 * sub) + l32;

    const unsigned krec = (unsigned)min(L, 32 * NB) * KROW - kvh * 384;
    const __amdgpu_buffer_rsrc_t srdK = __builtin_amdgcn_make_buffer_rsrc((void*)((const char*)K + (size_t)s0 * KROW + kvh * 384), (short)0, (int)krec, 0x00020000);
    const __amdgpu_buffer_rsrc_t srdV = __builtin_amdgcn_make_buffer_rsrc((void*)((const char*)V + (size_t)s0 * KROW + kvh * 384), (short)0, (int)krec, 0x00020000);
    const __amdgpu_buffer_rsrc_t srdQ = __builtin_amdgcn_make_buffer_rsrc((void*)((const char*)Q + (size_t)s0 * QROW + kvh * 768), (short)0, (int)((unsigned)L * QROW - kvh * 768), 0x00020000);
    const unsigned ldsb = (unsigned)(uintptr_t)lds;
    const unsigned wb = ldsb + wid * 1024;
    const int dr = (lane >> 2) & 7, dh = lane >> 5, dc = lane & 3, dg = (lane >> 4) & 1;
    const unsigned vK = (8 * wid + dr) * KROW + 64u * dh + 16u * (dc ^ ((2 * wid + dg) & 3));
    const unsigned vV = (8 * wid + dr) * KROW + 64u * dh + 16u * dc;

    dma3(wb, vK, srdK, 0);
    if (TB) dma3(wb + BLK, vK, srdK, KBLK);
    {
        const unsigned vQ0 = dr * QROW + 64u * dh + 16u * (dc ^ dg), vQ1 = dr * QROW + 64u * dh + 16u * (dc ^ (2 + dg));
        const unsigned qreg = TB ? 4 * BLK + hsel * BLK : (wid == 3 ? 2 * BLK : (3 + wid) * BLK);
        const unsigned qs = (unsigned)(q0 + (TB ? 16 * sub : 32 * sub)) * QROW + hsel * 384;
        const unsigned m = ldsb + qreg + (TB ? 2048 * sub : 0);
#pragma unroll
        for (int g = 0; g < (TB ? 2 : 4); ++g) dma3(m + g * 1024, (g & 1) ? vQ1 : vQ0, srdQ, qs + g * 8 * QROW);
    }
    if (TB) { dma3(wb + 2 * BLK, vK, srdK, 2 * KBLK); dma3(wb + 3 * BLK, vK, srdK, 3 * KBLK); } else dma3(wb + BLK, vK, srdK, KBLK);
    {
        const int per = TB ? 8 : 16;
        const int tk = (lane * 43) >> 7;
        const unsigned vp = tk < per ? (unsigned)(q0 + wid * per + tk) * KROW + (lane - 3 * tk) * 128 : OOB;
        const unsigned pm = ldsb + LDS_MISC + wid * 256;
        asm volatile("s_mov_b32 m0, %0\n\ts_nop 0\n\tbuffer_load_dword %1, %2, 0 offen lds" :: "s"(pm), "v"(vp), "s"(srdK) : "memory");
        asm volatile("s_mov_b32 m0, %0\n\ts_nop 0\n\tbuffer_load_dword %1, %2, 0 offen lds" :: "s"(pm), "v"(vp), "s"(srdV) : "memory");
    }
    const lds_cptr lp = (lds_cptr)lds;
    lds_cptr kb[2], vb;
#pragma unroll
    for (int i = 0; i < 2; ++i) kb[i] = lp + (l32 >> 3) * 1024 + (l32 & 7) * 64 + 16 * ((2 * i + hi) ^ ((l32 >> 2) & 3));
    vb = lp + (4 * hi + ((lane & 15) >> 2)) * 64 + 32 * ((lane >> 4) & 1) + 8 * (lane & 3);
#define KADDR(ks) (kb[(ks) & 1] + ((ks) >> 2) * 4096 + (((ks) >> 1) & 1) * 512)
    f32x16 o[6];
#pragma unroll
    for (int i = 0; i < 6; ++i) o[i] = f32x16{};
    OPIN("");

    STAMP(2);
    bf16x8 qf[12];
    if (TB) { WAIT_VM(8); BARRIER(); } else { WAIT_VM(5); }
    {
        const int qo = TB ? 4 * BLK + hsel * BLK : (wid == 3 ? 2 * BLK : (3 + wid) * BLK);
#pragma unroll
        for (int ks = 0; ks < 12; ++ks) qf[ks] = ldk(KADDR(ks) + qo);
    }
    BARRIER();
    STAMP(3);
    kb[0] += TB ? sub * BLK : 0; kb[1] += TB ? sub * BLK : 0;
    vb += TB ? sub * BLK : 3 * BLK;

    f32x16 S, X;
    float m = 0.f, l = 0.f;
    auto mask = [&](int kblock) {
        const int k0 = kblock * 32 + 4 * hi - qrel;
#pragma unroll
        for (int r = 0; r < 16; ++r) if (k0 + (r & 3) + 8 * (r >> 2) > 0) S[r] = -__builtin_inff();
    };
    auto rowmax = [&]() -> float {
        float a = max3f(S[0], S[1], S[2]), b = max3f(S[3], S[4], S[5]);
        a = max3f(a, S[6], S[7]); b = max3f(b, S[8], S[9]);
        a = max3f(a, S[10], S[11]); b = max3f(b, S[12], S[13]);
        a = max3f(a, S[14], S[15]);
        return swapmax(max2f(a, b));
    };
    const unsigned vK1 = vK + KBLK, vV1 = vV + KBLK;
    u32x4 p0, p1;
    bf16x8 kf[4];
    s16x4 vlo[4], vhi[4];
#define EXP(e) do { float t_ = X[e]; vexp(t_); X[e] = t_; } while (0)
#define FM(e) X[e] = vfma(S[e], C2, nm)
#define KRD(k) do { kf[(k) & 3] = ldk(KADDR(k)); SBAR(); } while (0)
#define VRD_(k, B) do { const lds_cptr vp_ = (B) + (((k) % 6) >> 1) * 4096 + (((k) % 6) & 1) * 512 + ((k) / 6) * 2048; vlo[(k) & 3] = vtr(vp_); vhi[(k) & 3] = vtr(vp_ + 1024); SBAR(); } while (0)
#define VRD(k) VRD_(k, vb)
#define VFR(k) ((bf16x8){vlo[(k) & 3][0], vlo[(k) & 3][1], vlo[(k) & 3][2], vlo[(k) & 3][3], vhi[(k) & 3][0], vhi[(k) & 3][1], vhi[(k) & 3][2], vhi[(k) & 3][3]})
#define QK(k) do { S = MFMA(kf[(k) & 3], qf[k], (k) == 0 ? f32x16{} : S); PIN(S); SBAR(); } while (0)
#define PV(k) do { o[(k) % 6] = MFMA(VFR(k), __builtin_bit_cast(bf16x8, (k) < 6 ? p0 : p1), o[(k) % 6]); PIN(o[(k) % 6]); SBAR(); } while (0)
#define ADD0(e) ps0 = vadd(ps0, X[e])
#define ADD1(e) ps1 = vadd(ps1, X[e])
#define CV(P, i, e) P[i] = vcvt(X[e], X[(e) + 1])

    int r0 = 0, r1 = TB ? 2 * BLK : BLK, r2 = TB ? 4 * BLK : 2 * BLK;
    if (nu > 0) {
        KRD(1); KRD(0); KRD(3); KRD(2);
        QK(0); dma<0, 0>(wb + (TB ? 4 : 3) * BLK, vV, srdV, 0); SBAR();
        QK(1); KRD(5); dma<1, 0>(wb + (TB ? 4 : 3) * BLK, vV, srdV, 0); SBAR();
        QK(2); KRD(4); dma<2, 0>(wb + (TB ? 4 : 3) * BLK, vV, srdV, 0); SBAR();
        QK(3); KRD(7); dma<0, 0>(wb + (TB ? 5 : 4) * BLK, vV, srdV, KBLK); SBAR();
        QK(4); KRD(6); dma<1, 0>(wb + (TB ? 5 : 4) * BLK, vV, srdV, KBLK); SBAR();
        QK(5); KRD(9); dma<2, 0>(wb + (TB ? 5 : 4) * BLK, vV, srdV, KBLK); SBAR();
        QK(6); KRD(8); if (!TB) dma<0, 0>(wb + 2 * BLK, vK, srdK, 2 * KBLK); SBAR();
        QK(7); KRD(11); if (!TB) dma<1, 0>(wb + 2 * BLK, vK, srdK, 2 * KBLK); SBAR();
        QK(8); KRD(10); if (!TB) dma<2, 0>(wb + 2 * BLK, vK, srdK, 2 * KBLK); SBAR();
        QK(9); QK(10); QK(11);
    } else {
        dma3(wb + 3 * BLK, vV, srdV, 0); dma3(wb + 4 * BLK, vV, srdV, KBLK); dma3(wb + 2 * BLK, vK, srdK, 2 * KBLK);
    }
    WAIT_VM(6); BARRIER();
    kb[0] += r1; kb[1] += r1; if (TB) vb += r2;
    if (nu > 1) { KRD(1); KRD(0); KRD(3); KRD(2); }
    if (nu > 0) {
        asm volatile("s_nop 11" : "+v"(S));
        if (diagu == 0) mask(TB ? sub : 0);
        m = rowmax() * C2;
        const float nm = -m;
        FM(0); FM(1); FM(2); FM(3); FM(4); FM(5); FM(6); FM(7); FM(8); FM(9); FM(10); FM(11); FM(12); FM(13); FM(14); FM(15);
        EXP(0); EXP(1); EXP(2); EXP(3);
    } else X = f32x16{};
    const float nm = -m;

#define ROTATE() do { const int t_ = TB ? r2 : r0; if (TB) { r2 = r1; r1 = r0; r0 = t_; } else { r0 = r1; r1 = r2; r2 = t_; } so += (TB ? 2 : 1) * KBLK; ++u; } while (0)
#define BK_WAIT() do { if (TB) { LOOPWAIT(); } else asm volatile("s_waitcnt vmcnt(6)\n\ts_cmp_lg_u32 %0, %1\n\ts_cbranch_scc1 1f\n\ts_waitcnt vmcnt(0)\n1:" :: "s"(u + 2), "s"(NU) : "scc", "memory"); } while (0)
    STAMP(4); STAMPC(8);
    int u = 0;
    unsigned so = 0;
    while (u + 1 < nu) {
        float ps0, ps1;
        const unsigned soK = so + (TB ? 4 : 3) * KBLK, soV = so + 2 * KBLK, mK = wb + r0, mV = wb + (TB ? r1 : 3 * BLK + r2);
        const int dk = TB ? r0 - r1 : r2 - r1, dv = TB ? r1 - r2 : r1 - r0;
        if (TB) {
            QK(0);  DMAL<0, 0>(mK, vK, srdK, soK); EXP(4); ps0 = vadd(X[0], X[1]); SBAR();
            QK(1);  KRD(5); EXP(5); EXP(6); ps1 = vadd(X[2], X[3]); SBAR();
            QK(2);  KRD(4); DMAL<1, 0>(mK, vK, srdK, soK); EXP(7); SBAR();
            QK(3);  KRD(7); EXP(8); CV(p0, 0, 0); ADD0(4); ADD1(5); SBAR();
            QK(4);  KRD(6); DMAL<2, 0>(mK, vK, srdK, soK); EXP(9); SBAR();
            QK(5);  KRD(9); EXP(10); CV(p0, 1, 2); CV(p0, 2, 4); ADD0(6); SBAR();
            QK(6);  KRD(8); DMAL<0, BLK>(mK, vK1, srdK, soK); EXP(11); SBAR();
            QK(7);  KRD(11); EXP(12); CV(p0, 3, 6); ADD1(7); ADD0(8); SBAR();
            QK(8);  KRD(10); DMAL<1, BLK>(mK, vK1, srdK, soK); EXP(13); SBAR();
            QK(9);  DMAL<2, BLK>(mK, vK1, srdK, soK); EXP(14); SBAR();
            LOOPWAIT(); LOOPBAR();
            VRD(1); VRD(0); VRD(3); VRD(2);
            QK(10); EXP(15); ADD1(9); CV(p1, 0, 8); ADD0(10); SBAR();
            QK(11); ADD1(11); CV(p1, 1, 10); ADD0(12); SBAR();
            PV(0);  DMAL<0, 0>(mV, vV, srdV, soV); ADD1(13); CV(p1, 2, 12); SBAR();
            PV(1);  VRD(5); ADD0(14); ADD1(15); CV(p1, 3, 14); SBAR();
            if (u + 1 == diagu) { asm volatile("s_nop 7" : "+v"(S)); mask(2 * u + 2 + sub); }
            PV(2);  VRD(4); DMAL<1, 0>(mV, vV, srdV, soV); FM(0); SBAR();
            PV(3);  VRD(7); FM(1); FM(2); FM(3); ps0 = vadd(ps0, ps1); SBAR();
            PV(4);  VRD(6); DMAL<2, 0>(mV, vV, srdV, soV); FM(4); SBAR();
            PV(5);  VRD(9); FM(5); FM(6); FM(7); l = vadd(l, ps0); SBAR();
            PV(6);  VRD(8); DMAL<0, BLK>(mV, vV1, srdV, soV); FM(8); SBAR();
            PV(7);  VRD(11); FM(9); FM(10); FM(11); SBAR();
            PV(8);  VRD(10); DMAL<1, BLK>(mV, vV1, srdV, soV); FM(12); SBAR();
            PV(9);  DMAL<2, BLK>(mV, vV1, srdV, soV); FM(13); FM(14); FM(15); kb[0] += dk; kb[1] += dk; PIN(kb[0]); PIN(kb[1]); SBAR();
        } else {
            QK(0);  EXP(4); EXP(5); ps0 = vadd(X[0], X[1]); SBAR();
            QK(1);  KRD(5); EXP(6); ps1 = vadd(X[2], X[3]); SBAR();
            QK(2);  KRD(4); EXP(7); CV(p0, 0, 0); ADD0(4); SBAR();
            QK(3);  KRD(7); EXP(8); ADD1(5); SBAR();
            QK(4);  KRD(6); EXP(9); CV(p0, 1, 2); CV(p0, 2, 4); SBAR();
            QK(5);  DMAL<0, 0>(mK, vK, srdK, soK); KRD(9); EXP(10); SBAR();
            QK(6);  KRD(8); EXP(11); ADD0(6); CV(p0, 3, 6); ADD1(7); SBAR();
            QK(7);  DMAL<1, 0>(mK, vK, srdK, soK); KRD(11); EXP(12); SBAR();
            QK(8);  KRD(10); EXP(13); ADD0(8); ADD1(9); CV(p1, 0, 8); SBAR();
            QK(9);  VRD(1); EXP(14); ADD0(10); SBAR();
            QK(10); DMAL<2, 0>(mK, vK, srdK, soK); VRD(0); EXP(15); SBAR();
            QK(11); VRD(3); ADD1(11); CV(p1, 1, 10); ADD0(12); SBAR();
            PV(0);  DMAL<0, 0>(mV, vV, srdV, soV); VRD(2); ADD1(13); SBAR();
            PV(1);  VRD(5); CV(p1, 2, 12); ADD0(14); ADD1(15); SBAR();
            if (u + 1 == diagu) { asm volatile("s_nop 7" : "+v"(S)); mask(u + 1); }
            PV(2);  DMAL<1, 0>(mV, vV, srdV, soV); VRD(4); CV(p1, 3, 14); SBAR();
            PV(3);  VRD(7); FM(0); FM(1); FM(2); ps0 = vadd(ps0, ps1); SBAR();
            PV(4);  DMAL<2, 0>(mV, vV, srdV, soV); VRD(6); FM(3); SBAR();
            PV(5);  VRD(9); FM(4); FM(5); FM(6); l = vadd(l, ps0); SBAR();
            PV(6);  VRD(8); FM(7); FM(8); FM(9); FM(10); SBAR();
            PV(7);  VRD(11); FM(11); FM(12); FM(13); SBAR();
            PV(8);  VRD(10); FM(14); FM(15); kb[0] += dk; kb[1] += dk; PIN(kb[0]); PIN(kb[1]); SBAR();
            PV(9);
        }
        BK_WAIT(); LOOPBAR();
        KRD(1); KRD(0); KRD(3); KRD(2);
        PV(10); EXP(0); EXP(1); vb += dv; PIN(vb); SBAR();
        PV(11); EXP(2); EXP(3); SBAR();
        ROTATE();
    }
    STAMPC(9);
#define TAIL_HEAD() \
        const bool last = u == NU - 1; \
        if (!last) { \
            dma3(wb + r0, vK, srdK, so + (TB ? 4 : 3) * KBLK); \
            if (TB) dma3(wb + r0 + BLK, vK1, srdK, so + 4 * KBLK); \
            else dma3(wb + 3 * BLK + r2, vV, srdV, so + 2 * KBLK); \
        }
#define TAIL_MID() \
        if (TB) { \
            if (last) { WAIT_VM(0); } else { WAIT_VM(6); } \
            BARRIER(); \
            if (!last) { dma3(wb + r1, vV, srdV, so + 2 * KBLK); dma3(wb + r1 + BLK, vV1, srdV, so + 2 * KBLK); } \
        }
#define TAIL_END() \
        if (!last) { BK_WAIT(); BARRIER(); }
    if (u < nu) {
        TAIL_HEAD()
#pragma unroll
        for (int e = 4; e < 16; ++e) EXP(e);
        float ps0 = vadd(X[0], X[1]), ps1 = vadd(X[2], X[3]);
#pragma unroll
        for (int e = 4; e < 16; e += 2) { ADD0(e); ADD1(e + 1); }
        CV(p0, 0, 0); CV(p0, 1, 2); CV(p0, 2, 4); CV(p0, 3, 6); CV(p1, 0, 8); CV(p1, 1, 10); CV(p1, 2, 12); CV(p1, 3, 14);
        l += ps0 + ps1;
        TAIL_MID()
        VRD(1); VRD(0); VRD(3); VRD(2);
        PV(0); PV(1); VRD(5); PV(2); VRD(4); PV(3); VRD(7); PV(4); VRD(6);
        PV(5); VRD(9); PV(6); VRD(8); PV(7); VRD(11); PV(8); VRD(10); PV(9); PV(10); PV(11);
        TAIL_END()
        ROTATE();
    }
    while (u < NU) {
        TAIL_HEAD()
        TAIL_MID()
        TAIL_END()
        ROTATE();
    }
#undef TAIL_HEAD
#undef TAIL_MID
#undef TAIL_END
    if (TB) WAIT_VM(0);
    OPIN("s_nop 15");
    int lane2;
    asm volatile("v_mbcnt_lo_u32_b32 %0, -1, 0\n\tv_mbcnt_hi_u32_b32 %0, -1, %0" : "=v"(lane2));

    const int l32e = lane2 & 31, hie = lane2 >> 5, r8e = lane2 >> 3, c8e = lane2 & 7, r16e = lane2 >> 4;
    STAMP(5);
    const int reg = TB ? (sub ? r2 : r1) + hsel * BLK : (wid < 3 ? wid * BLK : 3 * BLK + r0);
    l = swapsum(l);
    if (__builtin_expect(__any(!(l < LBIG)), 0)) {
        const unsigned mb = ldsb + reg;
        const int dr2 = (lane2 >> 2) & 7, dh2 = lane2 >> 5, dc2 = lane2 & 3, dg2 = (lane2 >> 4) & 1;
        const unsigned vK0 = dr2 * KROW + 64u * dh2 + 16u * (dc2 ^ dg2), vK1_ = dr2 * KROW + 64u * dh2 + 16u * (dc2 ^ (2 + dg2)), vV0 = dr2 * KROW + 64u * dh2 + 16u * dc2;
        kb[0] = lp + reg + (l32e >> 3) * 1024 + (l32e & 7) * 64 + 16 * (hie ^ ((l32e >> 2) & 3));
        kb[1] = lp + reg + (l32e >> 3) * 1024 + (l32e & 7) * 64 + 16 * ((2 + hie) ^ ((l32e >> 2) & 3));
        vb = lp + reg + (4 * hie + ((lane2 & 15) >> 2)) * 64 + 32 * ((lane2 >> 4) & 1) + 8 * (lane2 & 3);
        bf16x8 q2[12];
        {
            const unsigned vQ0 = dr2 * QROW + 64u * dh2 + 16u * (dc2 ^ dg2), vQ1 = dr2 * QROW + 64u * dh2 + 16u * (dc2 ^ (2 + dg2));
            const unsigned qs = (unsigned)(q0 + (TB ? 0 : 32 * sub)) * QROW + hsel * 384;
#pragma unroll
            for (int g = 0; g < 4; ++g) dma3(mb + g * 1024, (g & 1) ? vQ1 : vQ0, srdQ, qs + g * 8 * QROW);
            WAIT_VM(0);
#pragma unroll
            for (int ks = 0; ks < 12; ++ks) q2[ks] = ldk(KADDR(ks));
        }
        auto scores = [&](int t) {
            const int b = TB ? 2 * t + sub : t;
            asm volatile("s_waitcnt lgkmcnt(0)" ::: "memory");
#pragma unroll
            for (int g = 0; g < 4; ++g) dma3(mb + g * 1024, (g & 1) ? vK1_ : vK0, srdK, b * KBLK + g * 8 * KROW);
            WAIT_VM(0);
            S = MFMA(ldk(KADDR(0)), q2[0], f32x16{});
#pragma unroll
            for (int ks = 1; ks < 12; ++ks) S = MFMA(ldk(KADDR(ks)), q2[ks], S);
            if (t == diagu) {
                const int k0 = b * 32 + 4 * hie - (q0 + (TB ? 0 : 32 * sub) + l32e);
#pragma unroll
                for (int r = 0; r < 16; ++r) if (k0 + (r & 3) + 8 * (r >> 2) > 0) S[r] = -__builtin_inff();
            }
        };
        float mx = -__builtin_inff();
#pragma unroll 1
        for (int t = 0; t < nu; ++t) { scores(t); asm volatile("s_nop 15" : "+v"(S)); mx = max2f(mx, rowmax()); }
        m = mx * C2;
#pragma unroll
        for (int i = 0; i < 6; ++i) o[i] = f32x16{};
        l = 0.f;
#pragma unroll 1
        for (int t = 0; t < nu; ++t) {
            scores(t);
            const int b = TB ? 2 * t + sub : t;
            asm volatile("s_waitcnt lgkmcnt(0)" ::: "memory");
#pragma unroll
            for (int g = 0; g < 4; ++g) dma3(mb + g * 1024, vV0, srdV, b * KBLK + g * 8 * KROW);
            float ps = 0.f;
#pragma unroll
            for (int e = 0; e < 16; ++e) { X[e] = __builtin_amdgcn_exp2f(__builtin_fmaf(S[e], C2, -m)); ps += X[e]; }
            l += ps;
            p0 = (u32x4){cvtpk(X[0], X[1]), cvtpk(X[2], X[3]), cvtpk(X[4], X[5]), cvtpk(X[6], X[7])};
            p1 = (u32x4){cvtpk(X[8], X[9]), cvtpk(X[10], X[11]), cvtpk(X[12], X[13]), cvtpk(X[14], X[15])};
            WAIT_VM(0);
            s16x4 vlo[4], vhi[4];
#pragma unroll
            for (int k = 0; k < 12; ++k) { VRD(k); o[k % 6] = MFMA(VFR(k), __builtin_bit_cast(bf16x8, k < 6 ? p0 : p1), o[k % 6]); }
        }
        l = swapsum(l);
        asm volatile("s_waitcnt lgkmcnt(0)" ::: "memory");
    }

    const __amdgpu_buffer_rsrc_t srdO = __builtin_amdgcn_make_buffer_rsrc((void*)((char*)O + (size_t)s0 * QROW + kvh * 768), (short)0, (int)((unsigned)L * QROW - kvh * 768), 0x00020000);
    const lds_ptr lw = (lds_ptr)lds;
    const unsigned vO = r8e * QROW + c8e * 16;
#define STG(base, db, g, lo, hi_) *(__attribute__((address_space(3))) u32x2*)((base) + ((db) >> 1) * 4096 + 16 * (((4 * (db) + (g)) & 7) ^ (l32e & 7))) = (u32x2){lo, hi_}
    if constexpr (!TB) {
        if (nu > 0) {
            float il = __builtin_amdgcn_rcpf(l);
            asm volatile("s_nop 1" : "+v"(il));
            const lds_ptr wp = lw + reg + l32e * 128 + 8 * hie;
            const lds_ptr rp = lw + reg + r8e * 128 + 16 * (c8e ^ r8e);
            const unsigned ob = (unsigned)(q0 + 32 * sub) * QROW + hsel * 384;
            u32x4 w[4];
            auto scale = [&](int db) {
                float y[16];
#pragma unroll
                for (int r = 0; r < 16; ++r) y[r] = vmul(o[db][r], il);
                unsigned c[8];
#pragma unroll
                for (int r = 0; r < 8; ++r) c[r] = vcvt(y[2 * r], y[2 * r + 1]);
#pragma unroll
                for (int g = 0; g < 4; ++g) STG(wp, db, g, c[2 * g], c[2 * g + 1]);
                SBAR();
            };
            auto rd = [&](int s) {
#pragma unroll
                for (int g = 0; g < 4; ++g) w[g] = *(const __attribute__((address_space(3))) u32x4*)(rp + s * 4096 + g * 1024);
                SBAR();
            };
            auto st_ = [&](int s) {
#pragma unroll
                for (int g = 0; g < 4; ++g) __builtin_amdgcn_raw_buffer_store_b128(w[g], srdO, vO + s * 128, ob + g * 8 * QROW, 0);
                SBAR();
            };
            scale(0); scale(1); rd(0); scale(2); st_(0); scale(3); rd(1); scale(4); st_(1); scale(5); rd(2); st_(2);
        }
    } else {
        const lds_ptr xp = lw + reg + lane2 * 16;
        const lds_ptr xq = lw + (sub ? r1 : r2) + hsel * BLK + lane2 * 16;
        __attribute__((address_space(3))) f32x2* ml = (__attribute__((address_space(3))) f32x2*)(lw + LDS_MISC);
        ml[wid * 64 + lane2] = (f32x2){m, l};
        auto put = [&](const f32x16& a, int i) {
#pragma unroll
            for (int g = 0; g < 4; ++g) *(__attribute__((address_space(3))) f32x4*)(xp + (4 * i + g) * 1024) = (f32x4){a[4 * g], a[4 * g + 1], a[4 * g + 2], a[4 * g + 3]};
        };
        if (sub == 0) { asm volatile("; keep 0-2"); put(o[3], 0); put(o[4], 1); put(o[5], 2); } else { asm volatile("; keep 3-5"); put(o[0], 0); put(o[1], 1); put(o[2], 2); }
        BARRIER();
        const f32x2 pm = ml[(wid ^ 2) * 64 + lane2];
        f32x4 x[12];
#pragma unroll
        for (int i = 0; i < 12; ++i) x[i] = *(const __attribute__((address_space(3))) f32x4*)(xq + i * 1024);
        SBAR();
        const float mm = max2f(m, pm[0]);
        const float fa = __builtin_amdgcn_exp2f(m - mm), fb = __builtin_amdgcn_exp2f(pm[0] - mm);
        const float il = __builtin_amdgcn_rcpf(l * fa + pm[1] * fb);
        float wa = fa * il, wb2 = fb * il;
        asm volatile("s_nop 1" : "+v"(wa), "+v"(wb2));
        const lds_ptr wp = lw + r0 + hsel * BLK + l32e * 128 + 8 * hie;
        auto fin = [&](const f32x16& a, int i, int db) {
            float y[16];
#pragma unroll
            for (int r = 0; r < 16; ++r) y[r] = vmul(x[4 * i + (r >> 2)][r & 3], wb2);
#pragma unroll
            for (int r = 0; r < 16; ++r) y[r] = vfma_v(a[r], wa, y[r]);
            unsigned c[8];
#pragma unroll
            for (int r = 0; r < 8; ++r) c[r] = vcvt(y[2 * r], y[2 * r + 1]);
#pragma unroll
            for (int g = 0; g < 4; ++g) STG(wp, db, g, c[2 * g], c[2 * g + 1]);
            SBAR();
        };
        if (sub == 0) { asm volatile("; low"); fin(o[0], 0, 0); fin(o[1], 1, 1); fin(o[2], 2, 2); } else { asm volatile("; high"); fin(o[3], 0, 3); fin(o[4], 1, 4); fin(o[5], 2, 5); }
        BARRIER();
        const lds_ptr rp = lw + r0 + hsel * BLK + sub * 2048 + r8e * 128 + 16 * (c8e ^ r8e);
        const unsigned ob = (unsigned)(q0 + 16 * sub) * QROW + hsel * 384;
        u32x4 w[6];
#pragma unroll
        for (int i = 0; i < 6; ++i) w[i] = *(const __attribute__((address_space(3))) u32x4*)(rp + (i % 3) * 4096 + (i / 3) * 1024);
#pragma unroll
        for (int i = 0; i < 6; ++i) __builtin_amdgcn_raw_buffer_store_b128(w[i], srdO, vO + (i % 3) * 128, ob + (i / 3) * 8 * QROW, 0);
    }
#undef STG
    STAMP(6);
#undef EXP
#undef FM
#undef KRD
#undef VRD
#undef VFR
#undef QK
#undef PV
#undef ADD0
#undef ADD1
#undef ROTATE
#undef BK_WAIT
#undef CV
#undef KADDR
#undef VRD_
}

DI int wave_max63(int x) {
    x = max(x, __builtin_amdgcn_update_dpp(x, x, 0x111, 0xf, 0xf, false));
    x = max(x, __builtin_amdgcn_update_dpp(x, x, 0x112, 0xf, 0xf, false));
    x = max(x, __builtin_amdgcn_update_dpp(x, x, 0x114, 0xf, 0xf, false));
    x = max(x, __builtin_amdgcn_update_dpp(x, x, 0x118, 0xf, 0xf, false));
    x = max(x, __builtin_amdgcn_update_dpp(x, x, 0x142, 0xa, 0xf, false));
    x = max(x, __builtin_amdgcn_update_dpp(x, x, 0x143, 0xc, 0xf, false));
    return x;
}
constexpr int first_pure_b() { int i = TH; while (WB * ((i + 2) >> 1) < WA * TH) ++i; return i; }
constexpr int I0 = first_pure_b();

__global__ __launch_bounds__(256, 2) void attn_v4(const unsigned short* __restrict__ Q, const unsigned short* __restrict__ K,
                                                  const unsigned short* __restrict__ V, const int* __restrict__ cu,
                                                  unsigned short* __restrict__ O, int nseq, unsigned long long* dbg) {
    extern __shared__ __attribute__((aligned(16))) char lds[];
    unsigned long long st[16];
    STAMP(0);
    asm volatile("" :: "s"(Q), "s"(K), "s"(V), "s"(O));
    const int lane = threadIdx.x & 63;
    const int kvh = blockIdx.x & 7, j = blockIdx.x >> 3;
    int r = j < 32 ? j : (j < 64 ? 95 - j : j);
    const int cl = min(lane, nseq - 1);
    const int a = cu[cl], b = cu[cl + 1];
    const int n = lane < nseq ? (b - a + 31) >> 5 : 0;
    const int nmax = __builtin_amdgcn_readlane(wave_max63(n), 63);
    int tb = 1, idx = -1;
    unsigned long long msk = 0;
    for (int i = nmax - 1; i >= I0; --i) {
        msk = __ballot(n > i);
        const int c = __popcll(msk);
        if (r < c) { idx = i; break; }
        r -= c;
    }
    if (idx < 0) {
        int i = I0 - 1, jj = TH / 2 - 1;
#pragma unroll
        for (int lev = 0; lev < (I0 - TH) + TH / 2; ++lev) {
            const bool useB = i >= TH && (jj < 0 || WB * ((i + 2) >> 1) >= WA * (2 * jj + 2));
            const int thr = useB ? i : 2 * jj;
            if (idx < 0) {
                msk = __ballot(n > thr);
                const int c = __popcll(msk);
                if (r < c) { idx = useB ? i : jj; tb = useB; } else r -= c;
            }
            if (useB) --i; else --jj;
        }
    }
    if (idx < 0) return;
    const unsigned below = __builtin_amdgcn_mbcnt_hi((unsigned)(msk >> 32), __builtin_amdgcn_mbcnt_lo((unsigned)msk, 0u));
    const unsigned long long hit = __ballot(((msk >> lane) & 1) && below == (unsigned)r);
    const int sl = __builtin_ctzll(hit);
    const int s0 = __builtin_amdgcn_readlane(a, sl), L = __builtin_amdgcn_readlane(b, sl) - s0;
    STAMP(1);
    if (tb) attn_item<true>(st, Q, K, V, O, kvh, s0, L, idx, lds);
    else attn_item<false>(st, Q, K, V, O, kvh, s0, L, idx, lds);
}

extern "C" void kernel_launch(void* const* d_in, const int* in_sizes, int n_in, void* d_out, int out_size, void* d_ws, size_t ws_size, hipStream_t stream) {
    const int T = in_sizes[0] / (HQ * D), nseq = in_sizes[3] - 1;
    int slots = (T + 31) / 32 + nseq;
    if (slots < 64) slots = 64;
    (void)hipFuncSetAttribute((const void*)attn_v4, hipFuncAttributeMaxDynamicSharedMemorySize, LDS_BYTES);
    attn_v4<<<slots * 8, 256, LDS_BYTES, stream>>>((const unsigned short*)d_in[0], (const unsigned short*)d_in[1], (const unsigned short*)d_in[2],
                                                 (const int*)d_in[3], (unsigned short*)d_out, nseq, (unsigned long long*)d_ws);
}
```

```cpp
#define STAMPS
#define AMPL 0
#include <hip/hip_runtime.h>
#include <hip/hip_bf16.h>
#include <stdint.h>

typedef short bf16x8 __attribute__((ext_vector_type(8)));
typedef short s16x4 __attribute__((ext_vector_type(4)));
typedef float f32x16 __attribute__((ext_vector_type(16)));
typedef float f32x4 __attribute__((ext_vector_type(4)));
typedef float f32x2 __attribute__((ext_vector_type(2)));
typedef unsigned u32x4 __attribute__((ext_vector_type(4)));
typedef unsigned u32x2 __attribute__((ext_vector_type(2)));
typedef __attribute__((address_space(3))) const char* lds_cptr;
typedef __attribute__((address_space(3))) char* lds_ptr;
typedef short v4i16_t __attribute__((ext_vector_type(4)));

constexpr int HQ = 16, HKV = 8, D = 192;
constexpr unsigned QROW = HQ * D * 2, KROW = HKV * D * 2;
constexpr unsigned KBLK = 32 * KROW;
constexpr int BLK = 12288;
constexpr int LDS_MISC = 6 * BLK;
constexpr int LDS_BYTES = LDS_MISC + 2048;
constexpr float C2 = 0.07216878364870322f * 1.4426950408889634f;
constexpr float LBIG = 1073741824.f;
#define TH 8
#define PFKV 1
#define PFFIRST 0
#define PFQ 0
#define WA 10
#define WB 11
constexpr unsigned OOB = 0x80000000u;

#define DI __device__ __forceinline__
#define MFMA(a, b, c) __builtin_amdgcn_mfma_f32_32x32x16_bf16(a, b, c, 0, 0, 0)
#define SBAR() __builtin_amdgcn_sched_barrier(0)
#define PIN(x) asm volatile("" : "+v"(x))
typedef __bf16 bf16x2_t __attribute__((ext_vector_type(2)));
DI unsigned cvtpk(float lo, float hi) { const f32x2 v = {lo, hi}; return __builtin_bit_cast(unsigned, __builtin_convertvector(v, bf16x2_t)); }
DI float max3f(float a, float b, float c) { float r; asm("v_max3_f32 %0, %1, %2, %3" : "=v"(r) : "v"(a), "v"(b), "v"(c)); return r; }
DI float max2f(float a, float b) { float r; asm("v_max_f32_e32 %0, %1, %2" : "=v"(r) : "v"(a), "v"(b)); return r; }
DI float fmul_s(float a, float b) { float r; asm("v_mul_f32_e32 %0, %1, %2" : "=v"(r) : "v"(a), "v"(b)); return r; }
DI float fma_v(float a, float b, float c) { float r; asm("v_fma_f32 %0, %1, %2, %3" : "=v"(r) : "v"(a), "v"(b), "v"(c)); return r; }
DI void vexp(float& x) { asm volatile("v_exp_f32_e32 %0, %0" : "+v"(x)); }
DI float vadd(float a, float b) { float r; asm volatile("v_add_f32_e32 %0, %1, %2" : "=v"(r) : "v"(a), "v"(b)); return r; }
DI float vfma(float a, float b, float c) { float r; asm volatile("v_fma_f32 %0, %1, %2, %3" : "=v"(r) : "v"(a), "s"(b), "v"(c)); return r; }
DI float vmul(float a, float b) { float r; asm volatile("v_mul_f32_e32 %0, %1, %2" : "=v"(r) : "v"(a), "v"(b)); return r; }
DI float vfma_v(float a, float b, float c) { float r; asm volatile("v_fma_f32 %0, %1, %2, %3" : "=v"(r) : "v"(a), "v"(b), "v"(c)); return r; }
DI unsigned vcvt(float lo, float hi) { unsigned r; asm volatile("v_cvt_pk_bf16_f32 %0, %1, %2" : "=v"(r) : "v"(lo), "v"(hi)); return r; }
DI s16x4 vtr(lds_cptr p) { return __builtin_bit_cast(s16x4, __builtin_amdgcn_ds_read_tr16_b64_v4i16((__attribute__((address_space(3))) v4i16_t*)p)); }
DI bf16x8 ldk(lds_cptr p) { return *(const __attribute__((address_space(3))) bf16x8*)p; }
DI int rfl(int x) { return __builtin_amdgcn_readfirstlane(x); }
DI float swapmax(float x) { auto r = __builtin_amdgcn_permlane32_swap(__float_as_uint(x), __float_as_uint(x), false, false); return max2f(__uint_as_float(r[0]), __uint_as_float(r[1])); }
DI float swapsum(float x) { auto r = __builtin_amdgcn_permlane32_swap(__float_as_uint(x), __float_as_uint(x), false, false); return __uint_as_float(r[0]) + __uint_as_float(r[1]); }

template <int S, int OFF> DI void dma(unsigned mb, unsigned voff, __amdgpu_buffer_rsrc_t srd, unsigned soff) {
    asm volatile("s_add_u32 m0, %0, %5\n\ts_nop 0\n\tbuffer_load_dwordx4 %1, %2, %3 offen offset:%4 lds"
                 :: "s"(mb), "v"(voff), "s"(srd), "s"(soff), "i"(128 * S), "i"(OFF + S * (4096 - 128)) : "memory", "scc");
}
DI void dma3(unsigned mb, unsigned voff, __amdgpu_buffer_rsrc_t srd, unsigned soff) { dma<0, 0>(mb, voff, srd, soff); dma<1, 0>(mb, voff, srd, soff); dma<2, 0>(mb, voff, srd, soff); }
#define DMAL dma
#define LOOPBAR() BARRIER()
#define LOOPWAIT() WAIT_VM(6)
#define WAIT_VM(n) asm volatile("s_waitcnt vmcnt(" #n ")" ::: "memory")
#define BARRIER() asm volatile("s_waitcnt lgkmcnt(0)\n\ts_barrier" ::: "memory")
#define OPIN(nop) asm volatile(nop : "+v"(o[0]), "+v"(o[1]), "+v"(o[2]), "+v"(o[3]), "+v"(o[4]), "+v"(o[5]))

#define STAMP(i) do { SBAR(); unsigned long long t_; asm volatile("s_memrealtime %0\n\ts_waitcnt lgkmcnt(0)" : "=s"(t_) :: "memory"); st[i] = t_; SBAR(); } while (0)
#define STAMPC(i) do { SBAR(); unsigned long long t_; asm volatile("s_memtime %0\n\ts_waitcnt lgkmcnt(0)" : "=s"(t_) :: "memory"); st[i] = t_; SBAR(); } while (0)

template <bool TB>
DI void attn_item(unsigned long long* st, const unsigned short* __restrict__ Q, const unsigned short* __restrict__ K, const unsigned short* __restrict__ V,
                  unsigned short* __restrict__ O, int kvh, int s0, int L, int idx, char* lds) {
    const int tid = threadIdx.x, lane = tid & 63, wid = rfl(tid >> 6), l32 = lane & 31, hi = lane >> 5;
    const int hsel = wid & 1, sub = wid >> 1;
    const int n = (L + 31) >> 5;
    const int q0 = TB ? 32 * idx : 64 * idx;
    const int NB = TB ? idx + 1 : min(2 * idx + 2, n);
    const int NU = TB ? (NB + 1) >> 1 : NB;
    const int nu = TB ? (NB + 1 - sub) >> 1 : (2 * idx + sub < n ? 2 * idx + sub + 1 : 0);
    const int diagu = TB ? (((idx & 1) == sub) ? nu - 1 : -1) : nu - 1;
    const int qrel = q0 + (TB ? 0 : 32 * sub) + l32;

    const unsigned krec = (unsigned)min(L, 32 * NB) * KROW - kvh * 384;
    const __amdgpu_buffer_rsrc_t srdK = __builtin_amdgcn_make_buffer_rsrc((void*)((const char*)K + (size_t)s0 * KROW + kvh * 384), (short)0, (int)krec, 0x00020000);
    const __amdgpu_buffer_rsrc_t srdV = __builtin_amdgcn_make_buffer_rsrc((void*)((const char*)V + (size_t)s0 * KROW + kvh * 384), (short)0, (int)krec, 0x00020000);
    const __amdgpu_buffer_rsrc_t srdQ = __builtin_amdgcn_make_buffer_rsrc((void*)((const char*)Q + (size_t)s0 * QROW + kvh * 768), (short)0, (int)((unsigned)L * QROW - kvh * 768), 0x00020000);
    const unsigned ldsb = (unsigned)(uintptr_t)lds;
    const unsigned wb = ldsb + wid * 1024;
    const int dr = (lane >> 2) & 7, dh = lane >> 5, dc = lane & 3, dg = (lane >> 4) & 1;
    const unsigned vK = (8 * wid + dr) * KROW + 64u * dh + 16u * (dc ^ ((2 * wid + dg) & 3));
    const unsigned vV = (8 * wid + dr) * KROW + 64u * dh + 16u * dc;

    auto pf_own = [&]() {
        const int per = TB ? 8 : 16;
        const int tk = (lane * 43) >> 7;
        const unsigned vp = (PFKV && tk < per) ? (unsigned)(q0 + wid * per + tk) * KROW + (lane - 3 * tk) * 128 : OOB;
        const unsigned pm = ldsb + LDS_MISC + wid * 256;
        asm volatile("s_mov_b32 m0, %0\n\ts_nop 0\n\tbuffer_load_dword %1, %2, 0 offen lds" :: "s"(pm), "v"(vp), "s"(srdK) : "memory");
        asm volatile("s_mov_b32 m0, %0\n\ts_nop 0\n\tbuffer_load_dword %1, %2, 0 offen lds" :: "s"(pm), "v"(vp), "s"(srdV) : "memory");
    };
    if (PFFIRST && !TB) pf_own();
    dma3(wb, vK, srdK, 0);
    if (TB) dma3(wb + BLK, vK, srdK, KBLK);
    {
        const unsigned vQ0 = dr * QROW + 64u * dh + 16u * (dc ^ dg), vQ1 = dr * QROW + 64u * dh + 16u * (dc ^ (2 + dg));
        const unsigned qreg = TB ? 4 * BLK + hsel * BLK : (wid == 3 ? 2 * BLK : (3 + wid) * BLK);
        const unsigned qs = (unsigned)(q0 + (TB ? 16 * sub : 32 * sub)) * QROW + hsel * 384;
        const unsigned m = ldsb + qreg + (TB ? 2048 * sub : 0);
#pragma unroll
        for (int g = 0; g < (TB ? 2 : 4); ++g) dma3(m + g * 1024, (g & 1) ? vQ1 : vQ0, srdQ, qs + g * 8 * QROW);
    }
    if (TB) { dma3(wb + 2 * BLK, vK, srdK, 2 * KBLK); dma3(wb + 3 * BLK, vK, srdK, 3 * KBLK); } else dma3(wb + BLK, vK, srdK, KBLK);
    if (!(PFFIRST && !TB)) pf_own();
    const lds_cptr lp = (lds_cptr)lds;
    lds_cptr kb[2], vb;
#pragma unroll
    for (int i = 0; i < 2; ++i) kb[i] = lp + (l32 >> 3) * 1024 + (l32 & 7) * 64 + 16 * ((2 * i + hi) ^ ((l32 >> 2) & 3));
    vb = lp + (4 * hi + ((lane & 15) >> 2)) * 64 + 32 * ((lane >> 4) & 1) + 8 * (lane & 3);
#define KADDR(ks) (kb[(ks) & 1] + ((ks) >> 2) * 4096 + (((ks) >> 1) & 1) * 512)
    f32x16 o[6];
#pragma unroll
    for (int i = 0; i < 6; ++i) o[i] = f32x16{};
    OPIN("");

    STAMP(2);
    bf16x8 qf[12];
    if (TB) { WAIT_VM(8); BARRIER(); } else if (PFFIRST) { WAIT_VM(3); } else { WAIT_VM(5); }
    {
        const int qo = TB ? 4 * BLK + hsel * BLK : (wid == 3 ? 2 * BLK : (3 + wid) * BLK);
#pragma unroll
        for (int ks = 0; ks < 12; ++ks) qf[ks] = ldk(KADDR(ks) + qo);
    }
    BARRIER();
    STAMP(3);
    kb[0] += TB ? sub * BLK : 0; kb[1] += TB ? sub * BLK : 0;
    vb += TB ? sub * BLK : 3 * BLK;

    f32x16 S, X;
    float m = 0.f, l = 0.f;
    auto mask = [&](int kblock) {
        const int k0 = kblock * 32 + 4 * hi - qrel;
#pragma unroll
        for (int r = 0; r < 16; ++r) if (k0 + (r & 3) + 8 * (r >> 2) > 0) S[r] = -__builtin_inff();
    };
    auto rowmax = [&]() -> float {
        float a = max3f(S[0], S[1], S[2]), b = max3f(S[3], S[4], S[5]);
        a = max3f(a, S[6], S[7]); b = max3f(b, S[8], S[9]);
        a = max3f(a, S[10], S[11]); b = max3f(b, S[12], S[13]);
        a = max3f(a, S[14], S[15]);
        return swapmax(max2f(a, b));
    };
    const unsigned vK1 = vK + KBLK, vV1 = vV + KBLK;
    u32x4 p0, p1;
    bf16x8 kf[4];
    s16x4 vlo[4], vhi[4];
#define EXP(e) do { float t_ = X[e]; vexp(t_); X[e] = t_; } while (0)
#define FM(e) X[e] = vfma(S[e], C2, nm)
#define KRD(k) do { kf[(k) & 3] = ldk(KADDR(k)); SBAR(); } while (0)
#define VRD_(k, B) do { const lds_cptr vp_ = (B) + (((k) % 6) >> 1) * 4096 + (((k) % 6) & 1) * 512 + ((k) / 6) * 2048; vlo[(k) & 3] = vtr(vp_); vhi[(k) & 3] = vtr(vp_ + 1024); SBAR(); } while (0)
#define VRD(k) VRD_(k, vb)
#define VFR(k) ((bf16x8){vlo[(k) & 3][0], vlo[(k) & 3][1], vlo[(k) & 3][2], vlo[(k) & 3][3], vhi[(k) & 3][0], vhi[(k) & 3][1], vhi[(k) & 3][2], vhi[(k) & 3][3]})
#define QK(k) do { S = MFMA(kf[(k) & 3], qf[k], (k) == 0 ? f32x16{} : S); PIN(S); SBAR(); } while (0)
#define PV(k) do { o[(k) % 6] = MFMA(VFR(k), __builtin_bit_cast(bf16x8, (k) < 6 ? p0 : p1), o[(k) % 6]); PIN(o[(k) % 6]); SBAR(); } while (0)
#define ADD0(e) ps0 = vadd(ps0, X[e])
#define ADD1(e) ps1 = vadd(ps1, X[e])
#define CV(P, i, e) P[i] = vcvt(X[e], X[(e) + 1])

    int r0 = 0, r1 = TB ? 2 * BLK : BLK, r2 = TB ? 4 * BLK : 2 * BLK;
    if (nu > 0) {
        KRD(1); KRD(0); KRD(3); KRD(2);
        QK(0); dma<0, 0>(wb + (TB ? 4 : 3) * BLK, vV, srdV, 0); SBAR();
        QK(1); KRD(5); dma<1, 0>(wb + (TB ? 4 : 3) * BLK, vV, srdV, 0); SBAR();
        QK(2); KRD(4); dma<2, 0>(wb + (TB ? 4 : 3) * BLK, vV, srdV, 0); SBAR();
        QK(3); KRD(7); dma<0, 0>(wb + (TB ? 5 : 4) * BLK, vV, srdV, KBLK); SBAR();
        QK(4); KRD(6); dma<1, 0>(wb + (TB ? 5 : 4) * BLK, vV, srdV, KBLK); SBAR();
        QK(5); KRD(9); dma<2, 0>(wb + (TB ? 5 : 4) * BLK, vV, srdV, KBLK); SBAR();
        QK(6); KRD(8); if (!TB) dma<0, 0>(wb + 2 * BLK, vK, srdK, 2 * KBLK); SBAR();
        QK(7); KRD(11); if (!TB) dma<1, 0>(wb + 2 * BLK, vK, srdK, 2 * KBLK); SBAR();
        QK(8); KRD(10); if (!TB) dma<2, 0>(wb + 2 * BLK, vK, srdK, 2 * KBLK); SBAR();
        QK(9); QK(10); QK(11);
    } else {
        dma3(wb + 3 * BLK, vV, srdV, 0); dma3(wb + 4 * BLK, vV, srdV, KBLK); dma3(wb + 2 * BLK, vK, srdK, 2 * KBLK);
    }
    WAIT_VM(6); BARRIER();
    kb[0] += r1; kb[1] += r1; if (TB) vb += r2;
    if (nu > 1) { KRD(1); KRD(0); KRD(3); KRD(2); }
    if (nu > 0) {
        asm volatile("s_nop 11" : "+v"(S));
        if (diagu == 0) mask(TB ? sub : 0);
        m = rowmax() * C2;
        const float nm = -m;
        FM(0); FM(1); FM(2); FM(3); FM(4); FM(5); FM(6); FM(7); FM(8); FM(9); FM(10); FM(11); FM(12); FM(13); FM(14); FM(15);
        EXP(0); EXP(1); EXP(2); EXP(3);
    } else X = f32x16{};
    const float nm = -m;

#define ROTATE() do { const int t_ = TB ? r2 : r0; if (TB) { r2 = r1; r1 = r0; r0 = t_; } else { r0 = r1; r1 = r2; r2 = t_; } so += (TB ? 2 : 1) * KBLK; ++u; } while (0)
#define BK_WAIT() do { if (TB) { LOOPWAIT(); } else asm volatile("s_waitcnt vmcnt(6)\n\ts_cmp_lg_u32 %0, %1\n\ts_cbranch_scc1 1f\n\ts_waitcnt vmcnt(0)\n1:" :: "s"(u + 2), "s"(NU) : "scc", "memory"); } while (0)
    STAMP(4); STAMPC(8);
    int u = 0;
    unsigned so = 0;
    while (u + 1 < nu) {
        float ps0, ps1;
        const unsigned soK = so + (TB ? 4 : 3) * KBLK, soV = so + 2 * KBLK, mK = wb + r0, mV = wb + (TB ? r1 : 3 * BLK + r2);
        const int dk = TB ? r0 - r1 : r2 - r1, dv = TB ? r1 - r2 : r1 - r0;
        if (TB) {
            QK(0);  DMAL<0, 0>(mK, vK, srdK, soK); EXP(4); ps0 = vadd(X[0], X[1]); SBAR();
            QK(1);  KRD(5); EXP(5); EXP(6); ps1 = vadd(X[2], X[3]); SBAR();
            QK(2);  KRD(4); DMAL<1, 0>(mK, vK, srdK, soK); EXP(7); SBAR();
            QK(3);  KRD(7); EXP(8); CV(p0, 0, 0); ADD0(4); ADD1(5); SBAR();
            QK(4);  KRD(6); DMAL<2, 0>(mK, vK, srdK, soK); EXP(9); SBAR();
            QK(5);  KRD(9); EXP(10); CV(p0, 1, 2); CV(p0, 2, 4); ADD0(6); SBAR();
            QK(6);  KRD(8); DMAL<0, BLK>(mK, vK1, srdK, soK); EXP(11); SBAR();
            QK(7);  KRD(11); EXP(12); CV(p0, 3, 6); ADD1(7); ADD0(8); SBAR();
            QK(8);  KRD(10); DMAL<1, BLK>(mK, vK1, srdK, soK); EXP(13); SBAR();
            QK(9);  DMAL<2, BLK>(mK, vK1, srdK, soK); EXP(14); SBAR();
            LOOPWAIT(); LOOPBAR();
            VRD(1); VRD(0); VRD(3); VRD(2);
            QK(10); EXP(15); ADD1(9); CV(p1, 0, 8); ADD0(10); SBAR();
            QK(11); ADD1(11); CV(p1, 1, 10); ADD0(12); SBAR();
            PV(0);  DMAL<0, 0>(mV, vV, srdV, soV); ADD1(13); CV(p1, 2, 12); SBAR();
            PV(1);  VRD(5); ADD0(14); ADD1(15); CV(p1, 3, 14); SBAR();
            if (u + 1 == diagu) { asm volatile("s_nop 7" : "+v"(S)); mask(2 * u + 2 + sub); }
            PV(2);  VRD(4); DMAL<1, 0>(mV, vV, srdV, soV); FM(0); SBAR();
            PV(3);  VRD(7); FM(1); FM(2); FM(3); ps0 = vadd(ps0, ps1); SBAR();
            PV(4);  VRD(6); DMAL<2, 0>(mV, vV, srdV, soV); FM(4); SBAR();
            PV(5);  VRD(9); FM(5); FM(6); FM(7); l = vadd(l, ps0); SBAR();
            PV(6);  VRD(8); DMAL<0, BLK>(mV, vV1, srdV, soV); FM(8); SBAR();
            PV(7);  VRD(11); FM(9); FM(10); FM(11); SBAR();
            PV(8);  VRD(10); DMAL<1, BLK>(mV, vV1, srdV, soV); FM(12); SBAR();
            PV(9);  DMAL<2, BLK>(mV, vV1, srdV, soV); FM(13); FM(14); FM(15); kb[0] += dk; kb[1] += dk; PIN(kb[0]); PIN(kb[1]); SBAR();
        } else {
            QK(0);  EXP(4); EXP(5); ps0 = vadd(X[0], X[1]); SBAR();
            QK(1);  KRD(5); EXP(6); ps1 = vadd(X[2], X[3]); SBAR();
            QK(2);  KRD(4); EXP(7); CV(p0, 0, 0); ADD0(4); SBAR();
            QK(3);  KRD(7); EXP(8); ADD1(5); SBAR();
            QK(4);  KRD(6); EXP(9); CV(p0, 1, 2); CV(p0, 2, 4); SBAR();
            QK(5);  DMAL<0, 0>(mK, vK, srdK, soK); KRD(9); EXP(10); SBAR();
            QK(6);  KRD(8); EXP(11); ADD0(6); CV(p0, 3, 6); ADD1(7); SBAR();
            QK(7);  DMAL<1, 0>(mK, vK, srdK, soK); KRD(11); EXP(12); SBAR();
            QK(8);  KRD(10); EXP(13); ADD0(8); ADD1(9); CV(p1, 0, 8); SBAR();
            QK(9);  VRD(1); EXP(14); ADD0(10); SBAR();
            QK(10); DMAL<2, 0>(mK, vK, srdK, soK); VRD(0); EXP(15); SBAR();
            QK(11); VRD(3); ADD1(11); CV(p1, 1, 10); ADD0(12); SBAR();
            PV(0);  DMAL<0, 0>(mV, vV, srdV, soV); VRD(2); ADD1(13); SBAR();
            PV(1);  VRD(5); CV(p1, 2, 12); ADD0(14); ADD1(15); SBAR();
            if (u + 1 == diagu) { asm volatile("s_nop 7" : "+v"(S)); mask(u + 1); }
            PV(2);  DMAL<1, 0>(mV, vV, srdV, soV); VRD(4); CV(p1, 3, 14); SBAR();
            PV(3);  VRD(7); FM(0); FM(1); FM(2); ps0 = vadd(ps0, ps1); SBAR();
            PV(4);  DMAL<2, 0>(mV, vV, srdV, soV); VRD(6); FM(3); SBAR();
            PV(5);  VRD(9); FM(4); FM(5); FM(6); l = vadd(l, ps0); SBAR();
            PV(6);  VRD(8); FM(7); FM(8); FM(9); FM(10); SBAR();
            PV(7);  VRD(11); FM(11); FM(12); FM(13); SBAR();
            PV(8);  VRD(10); FM(14); FM(15); kb[0] += dk; kb[1] += dk; PIN(kb[0]); PIN(kb[1]); SBAR();
            PV(9);
        }
        BK_WAIT(); LOOPBAR();
        KRD(1); KRD(0); KRD(3); KRD(2);
        PV(10); EXP(0); EXP(1); vb += dv; PIN(vb); SBAR();
        PV(11); EXP(2); EXP(3); SBAR();
        ROTATE();
    }
#define TAIL_HEAD() \
        const bool last = u == NU - 1; \
        if (!last) { \
            dma3(wb + r0, vK, srdK, so + (TB ? 4 : 3) * KBLK); \
            if (TB) dma3(wb + r0 + BLK, vK1, srdK, so + 4 * KBLK); \
            else dma3(wb + 3 * BLK + r2, vV, srdV, so + 2 * KBLK); \
        }
#define TAIL_MID() \
        if (TB) { \
            if (last) { WAIT_VM(0); } else { WAIT_VM(6); } \
            BARRIER(); \
            if (!last) { dma3(wb + r1, vV, srdV, so + 2 * KBLK); dma3(wb + r1 + BLK, vV1, srdV, so + 2 * KBLK); } \
        }
#define TAIL_END() \
        if (!last) { BK_WAIT(); BARRIER(); }
    if (u < nu) {
        TAIL_HEAD()
#pragma unroll
        for (int e = 4; e < 16; ++e) EXP(e);
        float ps0 = vadd(X[0], X[1]), ps1 = vadd(X[2], X[3]);
#pragma unroll
        for (int e = 4; e < 16; e += 2) { ADD0(e); ADD1(e + 1); }
        CV(p0, 0, 0); CV(p0, 1, 2); CV(p0, 2, 4); CV(p0, 3, 6); CV(p1, 0, 8); CV(p1, 1, 10); CV(p1, 2, 12); CV(p1, 3, 14);
        l += ps0 + ps1;
        TAIL_MID()
        VRD(1); VRD(0); VRD(3); VRD(2);
        PV(0); PV(1); VRD(5); PV(2); VRD(4); PV(3); VRD(7); PV(4); VRD(6);
        PV(5); VRD(9); PV(6); VRD(8); PV(7); VRD(11); PV(8); VRD(10); PV(9); PV(10); PV(11);
        TAIL_END()
        ROTATE();
    }
    while (u < NU) {
        TAIL_HEAD()
        TAIL_MID()
        TAIL_END()
        ROTATE();
    }
#undef TAIL_HEAD
#undef TAIL_MID
#undef TAIL_END
    if (TB) WAIT_VM(0);
    OPIN("s_nop 15");
    int lane2;
    asm volatile("v_mbcnt_lo_u32_b32 %0, -1, 0\n\tv_mbcnt_hi_u32_b32 %0, -1, %0" : "=v"(lane2));

    const int l32e = lane2 & 31, hie = lane2 >> 5, r8e = lane2 >> 3, c8e = lane2 & 7, r16e = lane2 >> 4;
    STAMP(5);
    STAMPC(9);
    const int reg = TB ? (sub ? r2 : r1) + hsel * BLK : (wid < 3 ? wid * BLK : 3 * BLK + r0);
    l = swapsum(l);
    if (__builtin_expect(__any(!(l < LBIG)), 0)) {
        const unsigned mb = ldsb + reg;
        const int dr2 = (lane2 >> 2) & 7, dh2 = lane2 >> 5, dc2 = lane2 & 3, dg2 = (lane2 >> 4) & 1;
        const unsigned vK0 = dr2 * KROW + 64u * dh2 + 16u * (dc2 ^ dg2), vK1_ = dr2 * KROW + 64u * dh2 + 16u * (dc2 ^ (2 + dg2)), vV0 = dr2 * KROW + 64u * dh2 + 16u * dc2;
        kb[0] = lp + reg + (l32e >> 3) * 1024 + (l32e & 7) * 64 + 16 * (hie ^ ((l32e >> 2) & 3));
        kb[1] = lp + reg + (l32e >> 3) * 1024 + (l32e & 7) * 64 + 16 * ((2 + hie) ^ ((l32e >> 2) & 3));
        vb = lp + reg + (4 * hie + ((lane2 & 15) >> 2)) * 64 + 32 * ((lane2 >> 4) & 1) + 8 * (lane2 & 3);
        bf16x8 q2[12];
        {
            const unsigned vQ0 = dr2 * QROW + 64u * dh2 + 16u * (dc2 ^ dg2), vQ1 = dr2 * QROW + 64u * dh2 + 16u * (dc2 ^ (2 + dg2));
            const unsigned qs = (unsigned)(q0 + (TB ? 0 : 32 * sub)) * QROW + hsel * 384;
#pragma unroll
            for (int g = 0; g < 4; ++g) dma3(mb + g * 1024, (g & 1) ? vQ1 : vQ0, srdQ, qs + g * 8 * QROW);
            WAIT_VM(0);
#pragma unroll
            for (int ks = 0; ks < 12; ++ks) q2[ks] = ldk(KADDR(ks));
        }
        auto scores = [&](int t) {
            const int b = TB ? 2 * t + sub : t;
            asm volatile("s_waitcnt lgkmcnt(0)" ::: "memory");
#pragma unroll
            for (int g = 0; g < 4; ++g) dma3(mb + g * 1024, (g & 1) ? vK1_ : vK0, srdK, b * KBLK + g * 8 * KROW);
            WAIT_VM(0);
            S = MFMA(ldk(KADDR(0)), q2[0], f32x16{});
#pragma unroll
            for (int ks = 1; ks < 12; ++ks) S = MFMA(ldk(KADDR(ks)), q2[ks], S);
            if (t == diagu) {
                const int k0 = b * 32 + 4 * hie - (q0 + (TB ? 0 : 32 * sub) + l32e);
#pragma unroll
                for (int r = 0; r < 16; ++r) if (k0 + (r & 3) + 8 * (r >> 2) > 0) S[r] = -__builtin_inff();
            }
        };
        float mx = -__builtin_inff();
#pragma unroll 1
        for (int t = 0; t < nu; ++t) { scores(t); asm volatile("s_nop 15" : "+v"(S)); mx = max2f(mx, rowmax()); }
        m = mx * C2;
#pragma unroll
        for (int i = 0; i < 6; ++i) o[i] = f32x16{};
        l = 0.f;
#pragma unroll 1
        for (int t = 0; t < nu; ++t) {
            scores(t);
            const int b = TB ? 2 * t + sub : t;
            asm volatile("s_waitcnt lgkmcnt(0)" ::: "memory");
#pragma unroll
            for (int g = 0; g < 4; ++g) dma3(mb + g * 1024, vV0, srdV, b * KBLK + g * 8 * KROW);
            float ps = 0.f;
#pragma unroll
            for (int e = 0; e < 16; ++e) { X[e] = __builtin_amdgcn_exp2f(__builtin_fmaf(S[e], C2, -m)); ps += X[e]; }
            l += ps;
            p0 = (u32x4){cvtpk(X[0], X[1]), cvtpk(X[2], X[3]), cvtpk(X[4], X[5]), cvtpk(X[6], X[7])};
            p1 = (u32x4){cvtpk(X[8], X[9]), cvtpk(X[10], X[11]), cvtpk(X[12], X[13]), cvtpk(X[14], X[15])};
            WAIT_VM(0);
            s16x4 vlo[4], vhi[4];
#pragma unroll
            for (int k = 0; k < 12; ++k) { VRD(k); o[k % 6] = MFMA(VFR(k), __builtin_bit_cast(bf16x8, k < 6 ? p0 : p1), o[k % 6]); }
        }
        l = swapsum(l);
        asm volatile("s_waitcnt lgkmcnt(0)" ::: "memory");
    }

    const __amdgpu_buffer_rsrc_t srdO = __builtin_amdgcn_make_buffer_rsrc((void*)((char*)O + (size_t)s0 * QROW + kvh * 768), (short)0, (int)((unsigned)L * QROW - kvh * 768), 0x00020000);
    const lds_ptr lw = (lds_ptr)lds;
    const unsigned vO = r8e * QROW + c8e * 16;
#define STG(base, db, g, lo, hi_) *(__attribute__((address_space(3))) u32x2*)((base) + ((db) >> 1) * 4096 + 16 * (((4 * (db) + (g)) & 7) ^ (l32e & 7))) = (u32x2){lo, hi_}
    if constexpr (!TB) {
        if (nu > 0) {
            float il = __builtin_amdgcn_rcpf(l);
            asm volatile("s_nop 1" : "+v"(il));
            const lds_ptr wp = lw + reg + l32e * 128 + 8 * hie;
            const lds_ptr rp = lw + reg + r8e * 128 + 16 * (c8e ^ r8e);
            const unsigned ob = (unsigned)(q0 + 32 * sub) * QROW + hsel * 384;
            u32x4 w[4];
            auto scale = [&](int db) {
                float y[16];
#pragma unroll
                for (int r = 0; r < 16; ++r) y[r] = vmul(o[db][r], il);
                unsigned c[8];
#pragma unroll
                for (int r = 0; r < 8; ++r) c[r] = vcvt(y[2 * r], y[2 * r + 1]);
#pragma unroll
                for (int g = 0; g < 4; ++g) STG(wp, db, g, c[2 * g], c[2 * g + 1]);
                SBAR();
            };
            auto rd = [&](int s) {
#pragma unroll
                for (int g = 0; g < 4; ++g) w[g] = *(const __attribute__((address_space(3))) u32x4*)(rp + s * 4096 + g * 1024);
                SBAR();
            };
            auto st_ = [&](int s) {
#pragma unroll
                for (int g = 0; g < 4; ++g) __builtin_amdgcn_raw_buffer_store_b128(w[g], srdO, vO + s * 128, ob + g * 8 * QROW, 0);
                SBAR();
            };
            scale(0); scale(1); rd(0); scale(2); st_(0); scale(3); rd(1); scale(4); st_(1); scale(5); rd(2); st_(2);
        }
    } else {
        const lds_ptr xp = lw + reg + lane2 * 16;
        const lds_ptr xq = lw + (sub ? r1 : r2) + hsel * BLK + lane2 * 16;
        __attribute__((address_space(3))) f32x2* ml = (__attribute__((address_space(3))) f32x2*)(lw + LDS_MISC);
        ml[wid * 64 + lane2] = (f32x2){m, l};
        auto put = [&](const f32x16& a, int i) {
#pragma unroll
            for (int g = 0; g < 4; ++g) *(__attribute__((address_space(3))) f32x4*)(xp + (4 * i + g) * 1024) = (f32x4){a[4 * g], a[4 * g + 1], a[4 * g + 2], a[4 * g + 3]};
        };
        if (sub == 0) { asm volatile("; keep 0-2"); put(o[3], 0); put(o[4], 1); put(o[5], 2); } else { asm volatile("; keep 3-5"); put(o[0], 0); put(o[1], 1); put(o[2], 2); }
        BARRIER();
        const f32x2 pm = ml[(wid ^ 2) * 64 + lane2];
        f32x4 x[12];
#pragma unroll
        for (int i = 0; i < 12; ++i) x[i] = *(const __attribute__((address_space(3))) f32x4*)(xq + i * 1024);
        SBAR();
        const float mm = max2f(m, pm[0]);
        const float fa = __builtin_amdgcn_exp2f(m - mm), fb = __builtin_amdgcn_exp2f(pm[0] - mm);
        const float il = __builtin_amdgcn_rcpf(l * fa + pm[1] * fb);
        float wa = fa * il, wb2 = fb * il;
        asm volatile("s_nop 1" : "+v"(wa), "+v"(wb2));
        const lds_ptr wp = lw + r0 + hsel * BLK + l32e * 128 + 8 * hie;
        auto fin = [&](const f32x16& a, int i, int db) {
            float y[16];
#pragma unroll
            for (int r = 0; r < 16; ++r) y[r] = vmul(x[4 * i + (r >> 2)][r & 3], wb2);
#pragma unroll
            for (int r = 0; r < 16; ++r) y[r] = vfma_v(a[r], wa, y[r]);
            unsigned c[8];
#pragma unroll
            for (int r = 0; r < 8; ++r) c[r] = vcvt(y[2 * r], y[2 * r + 1]);
#pragma unroll
            for (int g = 0; g < 4; ++g) STG(wp, db, g, c[2 * g], c[2 * g + 1]);
            SBAR();
        };
        if (sub == 0) { asm volatile("; low"); fin(o[0], 0, 0); fin(o[1], 1, 1); fin(o[2], 2, 2); } else { asm volatile("; high"); fin(o[3], 0, 3); fin(o[4], 1, 4); fin(o[5], 2, 5); }
        BARRIER();
        const lds_ptr rp = lw + r0 + hsel * BLK + sub * 2048 + r8e * 128 + 16 * (c8e ^ r8e);
        const unsigned ob = (unsigned)(q0 + 16 * sub) * QROW + hsel * 384;
        u32x4 w[6];
#pragma unroll
        for (int i = 0; i < 6; ++i) w[i] = *(const __attribute__((address_space(3))) u32x4*)(rp + (i % 3) * 4096 + (i / 3) * 1024);
#pragma unroll
        for (int i = 0; i < 6; ++i) __builtin_amdgcn_raw_buffer_store_b128(w[i], srdO, vO + (i % 3) * 128, ob + (i / 3) * 8 * QROW, 0);
    }
#undef STG
    STAMP(6);
    asm volatile("s_waitcnt vmcnt(0)" ::: "memory");
    STAMP(7);
    st[10] = nu; st[11] = NU; st[12] = TB;
#undef EXP
#undef FM
#undef KRD
#undef VRD
#undef VFR
#undef QK
#undef PV
#undef ADD0
#undef ADD1
#undef ROTATE
#undef BK_WAIT
#undef CV
#undef KADDR
#undef VRD_
}

DI int wave_max63(int x) {
    x = max(x, __builtin_amdgcn_update_dpp(x, x, 0x111, 0xf, 0xf, false));
    x = max(x, __builtin_amdgcn_update_dpp(x, x, 0x112, 0xf, 0xf, false));
    x = max(x, __builtin_amdgcn_update_dpp(x, x, 0x114, 0xf, 0xf, false));
    x = max(x, __builtin_amdgcn_update_dpp(x, x, 0x118, 0xf, 0xf, false));
    x = max(x, __builtin_amdgcn_update_dpp(x, x, 0x142, 0xa, 0xf, false));
    x = max(x, __builtin_amdgcn_update_dpp(x, x, 0x143, 0xc, 0xf, false));
    return x;
}
constexpr int first_pure_b() { int i = TH; while (WB * ((i + 2) >> 1) < WA * TH) ++i; return i; }
constexpr int I0 = first_pure_b();

__global__ __launch_bounds__(256, 2) void attn_v5(const unsigned short* __restrict__ Q, const unsigned short* __restrict__ K,
                                                  const unsigned short* __restrict__ V, const int* __restrict__ cu,
                                                  unsigned short* __restrict__ O, int nseq, int T, unsigned long long* dbg) {
    extern __shared__ __attribute__((aligned(16))) char lds[];
    unsigned long long st[16];
    STAMP(0);
    asm volatile("" :: "s"(Q), "s"(K), "s"(V), "s"(O));
    const int lane = threadIdx.x & 63;
    const int kvh = blockIdx.x & 7, j = blockIdx.x >> 3;
    int r = j < 32 ? j : (j < 64 ? 95 - j : j);
    const int cl = min(lane, nseq - 1);
    int a, b;
    {
        u32x2 ab;
        asm volatile("global_load_dwordx2 %0, %1, %2" : "=v"(ab) : "v"(cl * 4), "s"(cu) : "memory");
        if (PFQ && j < 64) {
        const __amdgpu_buffer_rsrc_t srdQa = __builtin_amdgcn_make_buffer_rsrc((void*)((const char*)Q + kvh * 768), (short)0, (int)((unsigned)T * QROW - kvh * 768), 0x00020000);
        const int wv = rfl(threadIdx.x >> 6);
        const unsigned pm = (unsigned)(uintptr_t)lds + LDS_MISC + wv * 256;
        for (unsigned base = 0; base < (unsigned)T * 6u; base += 16384u) {
            const unsigned li = base + (j * 4 + wv) * 64 + lane, tk = li / 6u;
            const unsigned vp = tk * QROW + (li - 6u * tk) * 128u;
            asm volatile("s_mov_b32 m0, %0\n\ts_nop 0\n\tbuffer_load_dword %1, %2, 0 offen lds" :: "s"(pm), "v"(vp), "s"(srdQa) : "memory");
        }
        if ((unsigned)T * 6u <= 16384u) asm volatile("s_waitcnt vmcnt(1)" : "+v"(ab) :: "memory"); else asm volatile("s_waitcnt vmcnt(0)" : "+v"(ab) :: "memory");
        } else asm volatile("s_waitcnt vmcnt(0)" : "+v"(ab) :: "memory");
        a = ab[0]; b = ab[1];
    }
    const int n = lane < nseq ? (b - a + 31) >> 5 : 0;
    const int nmax = __builtin_amdgcn_readlane(wave_max63(n), 63);
    int tb = 1, idx = -1;
    unsigned long long msk = 0;
    for (int i = nmax - 1; i >= I0; --i) {
        msk = __ballot(n > i);
        const int c = __popcll(msk);
        if (r < c) { idx = i; break; }
        r -= c;
    }
    if (idx < 0) {
        int i = I0 - 1, jj = TH / 2 - 1;
#pragma unroll
        for (int lev = 0; lev < (I0 - TH) + TH / 2; ++lev) {
            const bool useB = i >= TH && (jj < 0 || WB * ((i + 2) >> 1) >= WA * (2 * jj + 2));
            const int thr = useB ? i : 2 * jj;
            if (idx < 0) {
                msk = __ballot(n > thr);
                const int c = __popcll(msk);
                if (r < c) { idx = useB ? i : jj; tb = useB; } else r -= c;
            }
            if (useB) --i; else --jj;
        }
    }
    if (idx < 0) { asm volatile("s_waitcnt vmcnt(0)" ::: "memory"); return; }
    const unsigned below = __builtin_amdgcn_mbcnt_hi((unsigned)(msk >> 32), __builtin_amdgcn_mbcnt_lo((unsigned)msk, 0u));
    const unsigned long long hit = __ballot(((msk >> lane) & 1) && below == (unsigned)r);
    const int sl = __builtin_ctzll(hit);
    const int s0 = __builtin_amdgcn_readlane(a, sl), L = __builtin_amdgcn_readlane(b, sl) - s0;
    STAMP(1);
    if (tb) attn_item<true>(st, Q, K, V, O, kvh, s0, L, idx, lds);
    else attn_item<false>(st, Q, K, V, O, kvh, s0, L, idx, lds);
    if (blockIdx.x < 8 && (threadIdx.x >> 6) == 0) {
        unsigned long long d = AMPL == 1 ? st[5] - st[4] : AMPL == 2 ? (st[9] - st[8]) / 24 : AMPL == 3 ? st[4] - st[0] : AMPL == 4 ? st[7] - st[5] : AMPL == 5 ? st[1] - st[0] : 0;
        const unsigned long long until = __builtin_amdgcn_s_memrealtime() + 10 * d;
        while (__builtin_amdgcn_s_memrealtime() < until) __builtin_amdgcn_s_sleep(2);
    }
}

extern "C" void kernel_launch(void* const* d_in, const int* in_sizes, int n_in, void* d_out, int out_size, void* d_ws, size_t ws_size, hipStream_t stream) {
    const int T = in_sizes[0] / (HQ * D), nseq = in_sizes[3] - 1;
    int slots = (T + 31) / 32 + nseq;
    if (slots < 64) slots = 64;
    (void)hipFuncSetAttribute((const void*)attn_v5, hipFuncAttributeMaxDynamicSharedMemorySize, LDS_BYTES);
    attn_v5<<<slots * 8, 256, LDS_BYTES, stream>>>((const unsigned short*)d_in[0], (const unsigned short*)d_in[1], (const unsigned short*)d_in[2],
                                                 (const int*)d_in[3], (unsigned short*)d_out, nseq, T, (unsigned long long*)d_ws);
}
```

```cpp
#include <hip/hip_runtime.h>
#include <hip/hip_bf16.h>
#include <stdint.h>

typedef short bf16x8 __attribute__((ext_vector_type(8)));
typedef short s16x4 __attribute__((ext_vector_type(4)));
typedef float f32x16 __attribute__((ext_vector_type(16)));
typedef float f32x4 __attribute__((ext_vector_type(4)));
typedef float f32x2 __attribute__((ext_vector_type(2)));
typedef unsigned u32x4 __attribute__((ext_vector_type(4)));
typedef unsigned u32x2 __attribute__((ext_vector_type(2)));
typedef int i32x16 __attribute__((ext_vector_type(16)));
typedef __attribute__((address_space(3))) const char* lds_cptr;
typedef __attribute__((address_space(3))) char* lds_ptr;
typedef short v4i16_t __attribute__((ext_vector_type(4)));

constexpr int HQ = 16, HKV = 8, D = 192;
constexpr unsigned QROW = HQ * D * 2, KROW = HKV * D * 2;
constexpr unsigned KBLK = 32 * KROW;
constexpr int BLK = 12288;
constexpr int LDS_MISC = 6 * BLK;
constexpr int LDS_BYTES = LDS_MISC + 2048;
constexpr float C2 = 0.07216878364870322f * 1.4426950408889634f;
constexpr float LBIG = 1073741824.f;
#define TH 8
#define PFKV 0
#define PFFIRST 0
#define PFB 0
#define DELAY2 0
#define PFQ 0
#define WA 10
#define WB 11
constexpr unsigned OOB = 0x80000000u;

#define DI __device__ __forceinline__
#define MFMA(a, b, c) __builtin_amdgcn_mfma_f32_32x32x16_bf16(a, b, c, 0, 0, 0)
#define SBAR() __builtin_amdgcn_sched_barrier(0)
#define PIN(x) asm volatile("" : "+v"(x))
typedef __bf16 bf16x2_t __attribute__((ext_vector_type(2)));
DI unsigned cvtpk(float lo, float hi) { const f32x2 v = {lo, hi}; return __builtin_bit_cast(unsigned, __builtin_convertvector(v, bf16x2_t)); }
DI float max3f(float a, float b, float c) { float r; asm("v_max3_f32 %0, %1, %2, %3" : "=v"(r) : "v"(a), "v"(b), "v"(c)); return r; }
DI float max2f(float a, float b) { float r; asm("v_max_f32_e32 %0, %1, %2" : "=v"(r) : "v"(a), "v"(b)); return r; }
DI float fmul_s(float a, float b) { float r; asm("v_mul_f32_e32 %0, %1, %2" : "=v"(r) : "v"(a), "v"(b)); return r; }
DI float fma_v(float a, float b, float c) { float r; asm("v_fma_f32 %0, %1, %2, %3" : "=v"(r) : "v"(a), "v"(b), "v"(c)); return r; }
DI void vexp(float& x) { asm volatile("v_exp_f32_e32 %0, %0" : "+v"(x)); }
DI float vadd(float a, float b) { float r; asm volatile("v_add_f32_e32 %0, %1, %2" : "=v"(r) : "v"(a), "v"(b)); return r; }
DI float vfma(float a, float b, float c) { float r; asm volatile("v_fma_f32 %0, %1, %2, %3" : "=v"(r) : "v"(a), "s"(b), "v"(c)); return r; }
DI float vmul(float a, float b) { float r; asm volatile("v_mul_f32_e32 %0, %1, %2" : "=v"(r) : "v"(a), "v"(b)); return r; }
DI float vfma_v(float a, float b, float c) { float r; asm volatile("v_fma_f32 %0, %1, %2, %3" : "=v"(r) : "v"(a), "v"(b), "v"(c)); return r; }
DI unsigned vcvt(float lo, float hi) { unsigned r; asm volatile("v_cvt_pk_bf16_f32 %0, %1, %2" : "=v"(r) : "v"(lo), "v"(hi)); return r; }
DI s16x4 vtr(lds_cptr p) { return __builtin_bit_cast(s16x4, __builtin_amdgcn_ds_read_tr16_b64_v4i16((__attribute__((address_space(3))) v4i16_t*)p)); }
DI bf16x8 ldk(lds_cptr p) { return *(const __attribute__((address_space(3))) bf16x8*)p; }
DI int rfl(int x) { return __builtin_amdgcn_readfirstlane(x); }
DI float swapmax(float x) { auto r = __builtin_amdgcn_permlane32_swap(__float_as_uint(x), __float_as_uint(x), false, false); return max2f(__uint_as_float(r[0]), __uint_as_float(r[1])); }
DI float swapsum(float x) { auto r = __builtin_amdgcn_permlane32_swap(__float_as_uint(x), __float_as_uint(x), false, false); return __uint_as_float(r[0]) + __uint_as_float(r[1]); }

template <int S, int OFF> DI void dma(unsigned mb, unsigned voff, __amdgpu_buffer_rsrc_t srd, unsigned soff) {
    asm volatile("s_add_u32 m0, %0, %5\n\ts_nop 0\n\tbuffer_load_dwordx4 %1, %2, %3 offen offset:%4 lds"
                 :: "s"(mb), "v"(voff), "s"(srd), "s"(soff), "i"(128 * S), "i"(OFF + S * (4096 - 128)) : "memory", "scc");
}
DI void dma3(unsigned mb, unsigned voff, __amdgpu_buffer_rsrc_t srd, unsigned soff) { dma<0, 0>(mb, voff, srd, soff); dma<1, 0>(mb, voff, srd, soff); dma<2, 0>(mb, voff, srd, soff); }
#define DMAL dma
#define LOOPBAR() BARRIER()
#define LOOPWAIT() WAIT_VM(6)
#define WAIT_VM(n) asm volatile("s_waitcnt vmcnt(" #n ")" ::: "memory")
#define BARRIER() asm volatile("s_waitcnt lgkmcnt(0)\n\ts_barrier" ::: "memory")
#define OPIN(nop) asm volatile(nop : "+v"(o[0]), "+v"(o[1]), "+v"(o[2]), "+v"(o[3]), "+v"(o[4]), "+v"(o[5]))

#define STAMP(i)
#define STAMPC(i)

template <bool TB>
DI void attn_item(unsigned long long* st, const unsigned short* __restrict__ Q, const unsigned short* __restrict__ K, const unsigned short* __restrict__ V,
                  unsigned short* __restrict__ O, int kvh, int s0, int L, int idx, char* lds) {
    const int tid = threadIdx.x, lane = tid & 63, wid = rfl(tid >> 6), l32 = lane & 31, hi = lane >> 5;
    const int hsel = wid & 1, sub = wid >> 1;
    const int n = (L + 31) >> 5;
    const int q0 = TB ? 32 * idx : 64 * idx;
    const int NB = TB ? idx + 1 : min(2 * idx + 2, n);
    const int NU = TB ? (NB + 1) >> 1 : NB;
    const int nu = TB ? (NB + 1 - sub) >> 1 : (2 * idx + sub < n ? 2 * idx + sub + 1 : 0);
    const int diagu = TB ? (((idx & 1) == sub) ? nu - 1 : -1) : nu - 1;
    const int qrel = q0 + (TB ? 0 : 32 * sub) + l32;

    const unsigned krec = (unsigned)min(L, 32 * NB) * KROW - kvh * 384;
    const __amdgpu_buffer_rsrc_t srdK = __builtin_amdgcn_make_buffer_rsrc((void*)((const char*)K + (size_t)s0 * KROW + kvh * 384), (short)0, (int)krec, 0x00020000);
    const __amdgpu_buffer_rsrc_t srdV = __builtin_amdgcn_make_buffer_rsrc((void*)((const char*)V + (size_t)s0 * KROW + kvh * 384), (short)0, (int)krec, 0x00020000);
    const __amdgpu_buffer_rsrc_t srdQ = __builtin_amdgcn_make_buffer_rsrc((void*)((const char*)Q + (size_t)s0 * QROW + kvh * 768), (short)0, (int)((unsigned)L * QROW - kvh * 768), 0x00020000);
    const unsigned ldsb = (unsigned)(uintptr_t)lds;
    const unsigned wb = ldsb + wid * 1024;
    const int dr = (lane >> 2) & 7, dh = lane >> 5, dc = lane & 3, dg = (lane >> 4) & 1;
    const unsigned vK = (8 * wid + dr) * KROW + 64u * dh + 16u * (dc ^ ((2 * wid + dg) & 3));
    const unsigned vV = (8 * wid + dr) * KROW + 64u * dh + 16u * dc;

    auto pf_own = [&]() {
        const int per = TB ? 8 : 16;
        const int tk = (lane * 43) >> 7;
        const unsigned vp = (PFKV && tk < per) ? (unsigned)(q0 + wid * per + tk) * KROW + (lane - 3 * tk) * 128 : OOB;
        const unsigned pm = ldsb + LDS_MISC + wid * 256;
        asm volatile("s_mov_b32 m0, %0\n\ts_nop 0\n\tbuffer_load_dword %1, %2, 0 offen lds" :: "s"(pm), "v"(vp), "s"(srdK) : "memory");
        asm volatile("s_mov_b32 m0, %0\n\ts_nop 0\n\tbuffer_load_dword %1, %2, 0 offen lds" :: "s"(pm), "v"(vp), "s"(srdV) : "memory");
    };
    if (PFFIRST && !TB) pf_own();
    dma3(wb, vK, srdK, 0);
    if (TB) dma3(wb + BLK, vK, srdK, KBLK);
    {
        const unsigned vQ0 = dr * QROW + 64u * dh + 16u * (dc ^ dg), vQ1 = dr * QROW + 64u * dh + 16u * (dc ^ (2 + dg));
        const unsigned qreg = TB ? 4 * BLK + hsel * BLK : (wid == 3 ? 2 * BLK : (3 + wid) * BLK);
        const unsigned qs = (unsigned)(q0 + (TB ? 16 * sub : 32 * sub)) * QROW + hsel * 384;
        const unsigned m = ldsb + qreg + (TB ? 2048 * sub : 0);
#pragma unroll
        for (int g = 0; g < (TB ? 2 : 4); ++g) dma3(m + g * 1024, (g & 1) ? vQ1 : vQ0, srdQ, qs + g * 8 * QROW);
    }
    if (TB) { dma3(wb + 2 * BLK, vK, srdK, 2 * KBLK); dma3(wb + 3 * BLK, vK, srdK, 3 * KBLK); } else dma3(wb + BLK, vK, srdK, KBLK);
    if (!(PFFIRST && !TB)) pf_own();
    const lds_cptr lp = (lds_cptr)lds;
    lds_cptr kb[2], vb;
#pragma unroll
    for (int i = 0; i < 2; ++i) kb[i] = lp + (l32 >> 3) * 1024 + (l32 & 7) * 64 + 16 * ((2 * i + hi) ^ ((l32 >> 2) & 3));
    vb = lp + (4 * hi + ((lane & 15) >> 2)) * 64 + 32 * ((lane >> 4) & 1) + 8 * (lane & 3);
#define KADDR(ks) (kb[(ks) & 1] + ((ks) >> 2) * 4096 + (((ks) >> 1) & 1) * 512)
    f32x16 o[6];
#pragma unroll
    for (int i = 0; i < 6; ++i) o[i] = f32x16{};
    OPIN("");

    STAMP(2);
    bf16x8 qf[12];
    if (TB) { WAIT_VM(8); BARRIER(); } else if (PFFIRST) { WAIT_VM(3); } else { WAIT_VM(5); }
    {
        const int qo = TB ? 4 * BLK + hsel * BLK : (wid == 3 ? 2 * BLK : (3 + wid) * BLK);
#pragma unroll
        for (int ks = 0; ks < 12; ++ks) qf[ks] = ldk(KADDR(ks) + qo);
    }
    BARRIER();
    STAMP(3);
    kb[0] += TB ? sub * BLK : 0; kb[1] += TB ? sub * BLK : 0;
    vb += TB ? sub * BLK : 3 * BLK;

    f32x16 S, X;
    float m = 0.f, l = 0.f;
    auto mask = [&](int kblock) {
        const int k0 = kblock * 32 + 4 * hi - qrel;
#pragma unroll
        for (int r = 0; r < 16; ++r) if (k0 + (r & 3) + 8 * (r >> 2) > 0) S[r] = -__builtin_inff();
    };
    auto rowmax = [&]() -> float {
        float a = max3f(S[0], S[1], S[2]), b = max3f(S[3], S[4], S[5]);
        a = max3f(a, S[6], S[7]); b = max3f(b, S[8], S[9]);
        a = max3f(a, S[10], S[11]); b = max3f(b, S[12], S[13]);
        a = max3f(a, S[14], S[15]);
        return swapmax(max2f(a, b));
    };
    const unsigned vK1 = vK + KBLK, vV1 = vV + KBLK;
    u32x4 p0, p1;
    bf16x8 kf[4];
    s16x4 vlo[4], vhi[4];
#define EXP(e) do { float t_ = X[e]; vexp(t_); X[e] = t_; } while (0)
#define FM(e) X[e] = vfma(S[e], C2, nm)
#define KRD(k) do { kf[(k) & 3] = ldk(KADDR(k)); SBAR(); } while (0)
#define VRD_(k, B) do { const lds_cptr vp_ = (B) + (((k) % 6) >> 1) * 4096 + (((k) % 6) & 1) * 512 + ((k) / 6) * 2048; vlo[(k) & 3] = vtr(vp_); vhi[(k) & 3] = vtr(vp_ + 1024); SBAR(); } while (0)
#define VRD(k) VRD_(k, vb)
#define VFR(k) ((bf16x8){vlo[(k) & 3][0], vlo[(k) & 3][1], vlo[(k) & 3][2], vlo[(k) & 3][3], vhi[(k) & 3][0], vhi[(k) & 3][1], vhi[(k) & 3][2], vhi[(k) & 3][3]})
#define QK(k) do { S = MFMA(kf[(k) & 3], qf[k], (k) == 0 ? f32x16{} : S); PIN(S); SBAR(); } while (0)
#define PV(k) do { o[(k) % 6] = MFMA(VFR(k), __builtin_bit_cast(bf16x8, (k) < 6 ? p0 : p1), o[(k) % 6]); PIN(o[(k) % 6]); SBAR(); } while (0)
#define ADD0(e) ps0 = vadd(ps0, X[e])
#define ADD1(e) ps1 = vadd(ps1, X[e])
#define CV(P, i, e) P[i] = vcvt(X[e], X[(e) + 1])

    int r0 = 0, r1 = TB ? 2 * BLK : BLK, r2 = TB ? 4 * BLK : 2 * BLK;
    if (nu > 0) {
        KRD(1); KRD(0); KRD(3); KRD(2);
        QK(0); dma<0, 0>(wb + (TB ? 4 : 3) * BLK, vV, srdV, 0); SBAR();
        QK(1); KRD(5); dma<1, 0>(wb + (TB ? 4 : 3) * BLK, vV, srdV, 0); SBAR();
        QK(2); KRD(4); dma<2, 0>(wb + (TB ? 4 : 3) * BLK, vV, srdV, 0); SBAR();
        QK(3); KRD(7); dma<0, 0>(wb + (TB ? 5 : 4) * BLK, vV, srdV, KBLK); SBAR();
        QK(4); KRD(6); dma<1, 0>(wb + (TB ? 5 : 4) * BLK, vV, srdV, KBLK); SBAR();
        QK(5); KRD(9); dma<2, 0>(wb + (TB ? 5 : 4) * BLK, vV, srdV, KBLK); SBAR();
        QK(6); KRD(8); if (!TB) dma<0, 0>(wb + 2 * BLK, vK, srdK, 2 * KBLK); SBAR();
        QK(7); KRD(11); if (!TB) dma<1, 0>(wb + 2 * BLK, vK, srdK, 2 * KBLK); SBAR();
        QK(8); KRD(10); if (!TB) dma<2, 0>(wb + 2 * BLK, vK, srdK, 2 * KBLK); SBAR();
        QK(9); QK(10); QK(11);
    } else {
        dma3(wb + 3 * BLK, vV, srdV, 0); dma3(wb + 4 * BLK, vV, srdV, KBLK); dma3(wb + 2 * BLK, vK, srdK, 2 * KBLK);
    }
    WAIT_VM(6); BARRIER();
    kb[0] += r1; kb[1] += r1; if (TB) vb += r2;
    if (nu > 1) { KRD(1); KRD(0); KRD(3); KRD(2); }
    if (nu > 0) {
        asm volatile("s_nop 11" : "+v"(S));
        if (diagu == 0) mask(TB ? sub : 0);
        m = rowmax() * C2;
        const float nm = -m;
        FM(0); FM(1); FM(2); FM(3); FM(4); FM(5); FM(6); FM(7); FM(8); FM(9); FM(10); FM(11); FM(12); FM(13); FM(14); FM(15);
        EXP(0); EXP(1); EXP(2); EXP(3);
    } else X = f32x16{};
    const float nm = -m;

#define ROTATE() do { const int t_ = TB ? r2 : r0; if (TB) { r2 = r1; r1 = r0; r0 = t_; } else { r0 = r1; r1 = r2; r2 = t_; } so += (TB ? 2 : 1) * KBLK; ++u; } while (0)
#define BK_WAIT() do { if (TB) { LOOPWAIT(); } else asm volatile("s_waitcnt vmcnt(6)\n\ts_cmp_lg_u32 %0, %1\n\ts_cbranch_scc1 1f\n\ts_waitcnt vmcnt(0)\n1:" :: "s"(u + 2), "s"(NU) : "scc", "memory"); } while (0)
    STAMP(4); STAMPC(8);
    int u = 0;
    unsigned so = 0;
    while (u + 1 < nu) {
        float ps0, ps1;
        const unsigned soK = so + (TB ? 4 : 3) * KBLK, soV = so + 2 * KBLK, mK = wb + r0, mV = wb + (TB ? r1 : 3 * BLK + r2);
        const int dk = TB ? r0 - r1 : r2 - r1, dv = TB ? r1 - r2 : r1 - r0;
        if (TB) {
            QK(0);  DMAL<0, 0>(mK, vK, srdK, soK); EXP(4); ps0 = vadd(X[0], X[1]); SBAR();
            QK(1);  KRD(5); EXP(5); EXP(6); ps1 = vadd(X[2], X[3]); SBAR();
            QK(2);  KRD(4); DMAL<1, 0>(mK, vK, srdK, soK); EXP(7); SBAR();
            QK(3);  KRD(7); EXP(8); CV(p0, 0, 0); ADD0(4); ADD1(5); SBAR();
            QK(4);  KRD(6); DMAL<2, 0>(mK, vK, srdK, soK); EXP(9); SBAR();
            QK(5);  KRD(9); EXP(10); CV(p0, 1, 2); CV(p0, 2, 4); ADD0(6); SBAR();
            QK(6);  KRD(8); DMAL<0, BLK>(mK, vK1, srdK, soK); EXP(11); SBAR();
            QK(7);  KRD(11); EXP(12); CV(p0, 3, 6); ADD1(7); ADD0(8); SBAR();
            QK(8);  KRD(10); DMAL<1, BLK>(mK, vK1, srdK, soK); EXP(13); SBAR();
            QK(9);  DMAL<2, BLK>(mK, vK1, srdK, soK); EXP(14); SBAR();
            LOOPWAIT(); LOOPBAR();
            VRD(1); VRD(0); VRD(3); VRD(2);
            QK(10); EXP(15); ADD1(9); CV(p1, 0, 8); ADD0(10); SBAR();
            QK(11); ADD1(11); CV(p1, 1, 10); ADD0(12); SBAR();
            PV(0);  DMAL<0, 0>(mV, vV, srdV, soV); ADD1(13); CV(p1, 2, 12); SBAR();
            PV(1);  VRD(5); ADD0(14); ADD1(15); CV(p1, 3, 14); SBAR();
            if (u + 1 == diagu) { asm volatile("s_nop 7" : "+v"(S)); mask(2 * u + 2 + sub); }
            PV(2);  VRD(4); DMAL<1, 0>(mV, vV, srdV, soV); FM(0); SBAR();
            PV(3);  VRD(7); FM(1); FM(2); FM(3); ps0 = vadd(ps0, ps1); SBAR();
            PV(4);  VRD(6); DMAL<2, 0>(mV, vV, srdV, soV); FM(4); SBAR();
            PV(5);  VRD(9); FM(5); FM(6); FM(7); l = vadd(l, ps0); SBAR();
            PV(6);  VRD(8); DMAL<0, BLK>(mV, vV1, srdV, soV); FM(8); SBAR();
            PV(7);  VRD(11); FM(9); FM(10); FM(11); SBAR();
            PV(8);  VRD(10); DMAL<1, BLK>(mV, vV1, srdV, soV); FM(12); SBAR();
            PV(9);  DMAL<2, BLK>(mV, vV1, srdV, soV); FM(13); FM(14); FM(15); kb[0] += dk; kb[1] += dk; PIN(kb[0]); PIN(kb[1]); SBAR();
        } else {
            QK(0);  EXP(4); EXP(5); ps0 = vadd(X[0], X[1]); SBAR();
            QK(1);  KRD(5); EXP(6); ps1 = vadd(X[2], X[3]); SBAR();
            QK(2);  KRD(4); EXP(7); CV(p0, 0, 0); ADD0(4); SBAR();
            QK(3);  KRD(7); EXP(8); ADD1(5); SBAR();
            QK(4);  KRD(6); EXP(9); CV(p0, 1, 2); CV(p0, 2, 4); SBAR();
            QK(5);  DMAL<0, 0>(mK, vK, srdK, soK); KRD(9); EXP(10); SBAR();
            QK(6);  KRD(8); EXP(11); ADD0(6); CV(p0, 3, 6); ADD1(7); SBAR();
            QK(7);  DMAL<1, 0>(mK, vK, srdK, soK); KRD(11); EXP(12); SBAR();
            QK(8);  KRD(10); EXP(13); ADD0(8); ADD1(9); CV(p1, 0, 8); SBAR();
            QK(9);  VRD(1); EXP(14); ADD0(10); SBAR();
            QK(10); DMAL<2, 0>(mK, vK, srdK, soK); VRD(0); EXP(15); SBAR();
            QK(11); VRD(3); ADD1(11); CV(p1, 1, 10); ADD0(12); SBAR();
            PV(0);  DMAL<0, 0>(mV, vV, srdV, soV); VRD(2); ADD1(13); SBAR();
            PV(1);  VRD(5); CV(p1, 2, 12); ADD0(14); ADD1(15); SBAR();
            if (u + 1 == diagu) { asm volatile("s_nop 7" : "+v"(S)); mask(u + 1); }
            PV(2);  DMAL<1, 0>(mV, vV, srdV, soV); VRD(4); CV(p1, 3, 14); SBAR();
            PV(3);  VRD(7); FM(0); FM(1); FM(2); ps0 = vadd(ps0, ps1); SBAR();
            PV(4);  DMAL<2, 0>(mV, vV, srdV, soV); VRD(6); FM(3); SBAR();
            PV(5);  VRD(9); FM(4); FM(5); FM(6); l = vadd(l, ps0); SBAR();
            PV(6);  VRD(8); FM(7); FM(8); FM(9); FM(10); SBAR();
            PV(7);  VRD(11); FM(11); FM(12); FM(13); SBAR();
            PV(8);  VRD(10); FM(14); FM(15); kb[0] += dk; kb[1] += dk; PIN(kb[0]); PIN(kb[1]); SBAR();
            PV(9);
        }
        BK_WAIT(); LOOPBAR();
        KRD(1); KRD(0); KRD(3); KRD(2);
        PV(10); EXP(0); EXP(1); vb += dv; PIN(vb); SBAR();
        PV(11); EXP(2); EXP(3); SBAR();
        ROTATE();
    }
    STAMPC(9);
#define TAIL_HEAD() \
        const bool last = u == NU - 1; \
        if (!last) { \
            dma3(wb + r0, vK, srdK, so + (TB ? 4 : 3) * KBLK); \
            if (TB) dma3(wb + r0 + BLK, vK1, srdK, so + 4 * KBLK); \
            else dma3(wb + 3 * BLK + r2, vV, srdV, so + 2 * KBLK); \
        }
#define TAIL_MID() \
        if (TB) { \
            if (last) { WAIT_VM(0); } else { WAIT_VM(6); } \
            BARRIER(); \
            if (!last) { dma3(wb + r1, vV, srdV, so + 2 * KBLK); dma3(wb + r1 + BLK, vV1, srdV, so + 2 * KBLK); } \
        }
#define TAIL_END() \
        if (!last) { BK_WAIT(); BARRIER(); }
    if (u < nu) {
        TAIL_HEAD()
#pragma unroll
        for (int e = 4; e < 16; ++e) EXP(e);
        float ps0 = vadd(X[0], X[1]), ps1 = vadd(X[2], X[3]);
#pragma unroll
        for (int e = 4; e < 16; e += 2) { ADD0(e); ADD1(e + 1); }
        CV(p0, 0, 0); CV(p0, 1, 2); CV(p0, 2, 4); CV(p0, 3, 6); CV(p1, 0, 8); CV(p1, 1, 10); CV(p1, 2, 12); CV(p1, 3, 14);
        l += ps0 + ps1;
        TAIL_MID()
        VRD(1); VRD(0); VRD(3); VRD(2);
        PV(0); PV(1); VRD(5); PV(2); VRD(4); PV(3); VRD(7); PV(4); VRD(6);
        PV(5); VRD(9); PV(6); VRD(8); PV(7); VRD(11); PV(8); VRD(10); PV(9); PV(10); PV(11);
        TAIL_END()
        ROTATE();
    }
    while (u < NU) {
        TAIL_HEAD()
        TAIL_MID()
        TAIL_END()
        ROTATE();
    }
#undef TAIL_HEAD
#undef TAIL_MID
#undef TAIL_END
    if (TB) WAIT_VM(0);
    OPIN("s_nop 15");
    int lane2;
    asm volatile("v_mbcnt_lo_u32_b32 %0, -1, 0\n\tv_mbcnt_hi_u32_b32 %0, -1, %0" : "=v"(lane2));

    const int l32e = lane2 & 31, hie = lane2 >> 5, r8e = lane2 >> 3, c8e = lane2 & 7, r16e = lane2 >> 4;
    STAMP(5);
    const int reg = TB ? (sub ? r2 : r1) + hsel * BLK : (wid < 3 ? wid * BLK : 3 * BLK + r0);
    l = swapsum(l);
    if (__builtin_expect(__any(!(l < LBIG)), 0)) {
        const unsigned mb = ldsb + reg;
        const int dr2 = (lane2 >> 2) & 7, dh2 = lane2 >> 5, dc2 = lane2 & 3, dg2 = (lane2 >> 4) & 1;
        const unsigned vK0 = dr2 * KROW + 64u * dh2 + 16u * (dc2 ^ dg2), vK1_ = dr2 * KROW + 64u * dh2 + 16u * (dc2 ^ (2 + dg2)), vV0 = dr2 * KROW + 64u * dh2 + 16u * dc2;
        kb[0] = lp + reg + (l32e >> 3) * 1024 + (l32e & 7) * 64 + 16 * (hie ^ ((l32e >> 2) & 3));
        kb[1] = lp + reg + (l32e >> 3) * 1024 + (l32e & 7) * 64 + 16 * ((2 + hie) ^ ((l32e >> 2) & 3));
        vb = lp + reg + (4 * hie + ((lane2 & 15) >> 2)) * 64 + 32 * ((lane2 >> 4) & 1) + 8 * (lane2 & 3);
        bf16x8 q2[12];
        {
            const unsigned vQ0 = dr2 * QROW + 64u * dh2 + 16u * (dc2 ^ dg2), vQ1 = dr2 * QROW + 64u * dh2 + 16u * (dc2 ^ (2 + dg2));
            const unsigned qs = (unsigned)(q0 + (TB ? 0 : 32 * sub)) * QROW + hsel * 384;
#pragma unroll
            for (int g = 0; g < 4; ++g) dma3(mb + g * 1024, (g & 1) ? vQ1 : vQ0, srdQ, qs + g * 8 * QROW);
            WAIT_VM(0);
#pragma unroll
            for (int ks = 0; ks < 12; ++ks) q2[ks] = ldk(KADDR(ks));
        }
        auto scores = [&](int t) {
            const int b = TB ? 2 * t + sub : t;
            asm volatile("s_waitcnt lgkmcnt(0)" ::: "memory");
#pragma unroll
            for (int g = 0; g < 4; ++g) dma3(mb + g * 1024, (g & 1) ? vK1_ : vK0, srdK, b * KBLK + g * 8 * KROW);
            WAIT_VM(0);
            S = MFMA(ldk(KADDR(0)), q2[0], f32x16{});
#pragma unroll
            for (int ks = 1; ks < 12; ++ks) S = MFMA(ldk(KADDR(ks)), q2[ks], S);
            if (t == diagu) {
                const int k0 = b * 32 + 4 * hie - (q0 + (TB ? 0 : 32 * sub) + l32e);
#pragma unroll
                for (int r = 0; r < 16; ++r) if (k0 + (r & 3) + 8 * (r >> 2) > 0) S[r] = -__builtin_inff();
            }
        };
        float mx = -__builtin_inff();
#pragma unroll 1
        for (int t = 0; t < nu; ++t) { scores(t); asm volatile("s_nop 15" : "+v"(S)); mx = max2f(mx, rowmax()); }
        m = mx * C2;
#pragma unroll
        for (int i = 0; i < 6; ++i) o[i] = f32x16{};
        l = 0.f;
#pragma unroll 1
        for (int t = 0; t < nu; ++t) {
            scores(t);
            const int b = TB ? 2 * t + sub : t;
            asm volatile("s_waitcnt lgkmcnt(0)" ::: "memory");
#pragma unroll
            for (int g = 0; g < 4; ++g) dma3(mb + g * 1024, vV0, srdV, b * KBLK + g * 8 * KROW);
            float ps = 0.f;
#pragma unroll
            for (int e = 0; e < 16; ++e) { X[e] = __builtin_amdgcn_exp2f(__builtin_fmaf(S[e], C2, -m)); ps += X[e]; }
            l += ps;
            p0 = (u32x4){cvtpk(X[0], X[1]), cvtpk(X[2], X[3]), cvtpk(X[4], X[5]), cvtpk(X[6], X[7])};
            p1 = (u32x4){cvtpk(X[8], X[9]), cvtpk(X[10], X[11]), cvtpk(X[12], X[13]), cvtpk(X[14], X[15])};
            WAIT_VM(0);
            s16x4 vlo[4], vhi[4];
#pragma unroll
            for (int k = 0; k < 12; ++k) { VRD(k); o[k % 6] = MFMA(VFR(k), __builtin_bit_cast(bf16x8, k < 6 ? p0 : p1), o[k % 6]); }
        }
        l = swapsum(l);
        asm volatile("s_waitcnt lgkmcnt(0)" ::: "memory");
    }

    const __amdgpu_buffer_rsrc_t srdO = __builtin_amdgcn_make_buffer_rsrc((void*)((char*)O + (size_t)s0 * QROW + kvh * 768), (short)0, (int)((unsigned)L * QROW - kvh * 768), 0x00020000);
    const lds_ptr lw = (lds_ptr)lds;
    const unsigned vO = r8e * QROW + c8e * 16;
#define STG(base, db, g, lo, hi_) *(__attribute__((address_space(3))) u32x2*)((base) + ((db) >> 1) * 4096 + 16 * (((4 * (db) + (g)) & 7) ^ (l32e & 7))) = (u32x2){lo, hi_}
    if constexpr (!TB) {
        if (nu > 0) {
            float il = __builtin_amdgcn_rcpf(l);
            asm volatile("s_nop 1" : "+v"(il));
            const lds_ptr wp = lw + reg + l32e * 128 + 8 * hie;
            const lds_ptr rp = lw + reg + r8e * 128 + 16 * (c8e ^ r8e);
            const unsigned ob = (unsigned)(q0 + 32 * sub) * QROW + hsel * 384;
            u32x4 w[4];
            auto scale = [&](int db) {
                float y[16];
#pragma unroll
                for (int r = 0; r < 16; ++r) y[r] = vmul(o[db][r], il);
                unsigned c[8];
#pragma unroll
                for (int r = 0; r < 8; ++r) c[r] = vcvt(y[2 * r], y[2 * r + 1]);
#pragma unroll
                for (int g = 0; g < 4; ++g) STG(wp, db, g, c[2 * g], c[2 * g + 1]);
                SBAR();
            };
            auto rd = [&](int s) {
#pragma unroll
                for (int g = 0; g < 4; ++g) w[g] = *(const __attribute__((address_space(3))) u32x4*)(rp + s * 4096 + g * 1024);
                SBAR();
            };
            auto st_ = [&](int s) {
#pragma unroll
                for (int g = 0; g < 4; ++g) __builtin_amdgcn_raw_buffer_store_b128(w[g], srdO, vO + s * 128, ob + g * 8 * QROW, 0);
                SBAR();
            };
            scale(0); scale(1); rd(0); scale(2); st_(0); scale(3); rd(1); scale(4); st_(1); scale(5); rd(2); st_(2);
        }
    } else {
        const lds_ptr xp = lw + reg + lane2 * 16;
        const lds_ptr xq = lw + (sub ? r1 : r2) + hsel * BLK + lane2 * 16;
        __attribute__((address_space(3))) f32x2* ml = (__attribute__((address_space(3))) f32x2*)(lw + LDS_MISC);
        ml[wid * 64 + lane2] = (f32x2){m, l};
        auto put = [&](const f32x16& a, int i) {
#pragma unroll
            for (int g = 0; g < 4; ++g) *(__attribute__((address_space(3))) f32x4*)(xp + (4 * i + g) * 1024) = (f32x4){a[4 * g], a[4 * g + 1], a[4 * g + 2], a[4 * g + 3]};
        };
        if (sub == 0) { asm volatile("; keep 0-2"); put(o[3], 0); put(o[4], 1); put(o[5], 2); } else { asm volatile("; keep 3-5"); put(o[0], 0); put(o[1], 1); put(o[2], 2); }
        BARRIER();
        const f32x2 pm = ml[(wid ^ 2) * 64 + lane2];
        f32x4 x[12];
#pragma unroll
        for (int i = 0; i < 12; ++i) x[i] = *(const __attribute__((address_space(3))) f32x4*)(xq + i * 1024);
        SBAR();
        const float mm = max2f(m, pm[0]);
        const float fa = __builtin_amdgcn_exp2f(m - mm), fb = __builtin_amdgcn_exp2f(pm[0] - mm);
        const float il = __builtin_amdgcn_rcpf(l * fa + pm[1] * fb);
        float wa = fa * il, wb2 = fb * il;
        asm volatile("s_nop 1" : "+v"(wa), "+v"(wb2));
        const lds_ptr wp = lw + r0 + hsel * BLK + l32e * 128 + 8 * hie;
        auto fin = [&](const f32x16& a, int i, int db) {
            float y[16];
#pragma unroll
            for (int r = 0; r < 16; ++r) y[r] = vmul(x[4 * i + (r >> 2)][r & 3], wb2);
#pragma unroll
            for (int r = 0; r < 16; ++r) y[r] = vfma_v(a[r], wa, y[r]);
            unsigned c[8];
#pragma unroll
            for (int r = 0; r < 8; ++r) c[r] = vcvt(y[2 * r], y[2 * r + 1]);
#pragma unroll
            for (int g = 0; g < 4; ++g) STG(wp, db, g, c[2 * g], c[2 * g + 1]);
            SBAR();
        };
        if (sub == 0) { asm volatile("; low"); fin(o[0], 0, 0); fin(o[1], 1, 1); fin(o[2], 2, 2); } else { asm volatile("; high"); fin(o[3], 0, 3); fin(o[4], 1, 4); fin(o[5], 2, 5); }
        BARRIER();
        const lds_ptr rp = lw + r0 + hsel * BLK + sub * 2048 + r8e * 128 + 16 * (c8e ^ r8e);
        const unsigned ob = (unsigned)(q0 + 16 * sub) * QROW + hsel * 384;
        u32x4 w[6];
#pragma unroll
        for (int i = 0; i < 6; ++i) w[i] = *(const __attribute__((address_space(3))) u32x4*)(rp + (i % 3) * 4096 + (i / 3) * 1024);
#pragma unroll
        for (int i = 0; i < 6; ++i) __builtin_amdgcn_raw_buffer_store_b128(w[i], srdO, vO + (i % 3) * 128, ob + (i / 3) * 8 * QROW, 0);
    }
#undef STG
    STAMP(6);
#undef EXP
#undef FM
#undef KRD
#undef VRD
#undef VFR
#undef QK
#undef PV
#undef ADD0
#undef ADD1
#undef ROTATE
#undef BK_WAIT
#undef CV
#undef KADDR
#undef VRD_
}

DI int wave_max63(int x) {
    asm volatile("s_nop 1\n\tv_max_i32_dpp %0, %0, %0 row_shr:1 row_mask:0xf bank_mask:0xf\n\ts_nop 1\n\tv_max_i32_dpp %0, %0, %0 row_shr:2 row_mask:0xf bank_mask:0xf\n\t"
                 "s_nop 1\n\tv_max_i32_dpp %0, %0, %0 row_shr:4 row_mask:0xf bank_mask:0xf\n\ts_nop 1\n\tv_max_i32_dpp %0, %0, %0 row_shr:8 row_mask:0xf bank_mask:0xf\n\t"
                 "s_nop 1\n\tv_max_i32_dpp %0, %0, %0 row_bcast:15 row_mask:0xa bank_mask:0xf\n\ts_nop 1\n\tv_max_i32_dpp %0, %0, %0 row_bcast:31 row_mask:0xc bank_mask:0xf\n\ts_nop 0" : "+v"(x));
    return x;
}
constexpr int first_pure_b() { int i = TH; while (WB * ((i + 2) >> 1) < WA * TH) ++i; return i; }
constexpr int I0 = first_pure_b();

__global__ __launch_bounds__(256, 2) void attn_v6(const unsigned short* __restrict__ Q, const unsigned short* __restrict__ K,
                                                  const unsigned short* __restrict__ V, const int* __restrict__ cu,
                                                  unsigned short* __restrict__ O, int nseq, int T, unsigned long long* dbg) {
    extern __shared__ __attribute__((aligned(16))) char lds[];
    unsigned long long st[16];
    STAMP(0);
    const int lane = threadIdx.x & 63;
    const int kvh = blockIdx.x & 7, j = blockIdx.x >> 3;
    const int wv = rfl(threadIdx.x >> 6);
    unsigned vp;
    {
        const unsigned li = (j * 4 + wv) * 32 + (lane & 31), tk = li / 3u;
        vp = (tk * KROW + (li - 3u * tk) * 128u) | ((PFB && lane < 32 && j < 64) ? 0u : OOB);
        asm volatile("" : "+v"(vp));
    }
    asm volatile("" :: "s"(Q), "s"(K), "s"(V), "s"(O), "s"(cu), "s"(nseq), "s"(T));
    int r = j < 32 ? j : (j < 64 ? 95 - j : j);
    int a, b;
    if (nseq <= 15) {
        const unsigned pm = (unsigned)(uintptr_t)lds + LDS_MISC + wv * 256;
        const __amdgpu_buffer_rsrc_t srdKa = __builtin_amdgcn_make_buffer_rsrc((void*)((const char*)K + kvh * 384), (short)0, (int)((unsigned)T * KROW - kvh * 384), 0x00020000);
        const __amdgpu_buffer_rsrc_t srdVa = __builtin_amdgcn_make_buffer_rsrc((void*)((const char*)V + kvh * 384), (short)0, (int)((unsigned)T * KROW - kvh * 384), 0x00020000);
        i32x16 c;
        asm volatile("s_load_dwordx16 %0, %1, 0x0\n\ts_mov_b32 m0, %2\n\ts_nop 0\n\tbuffer_load_dword %3, %4, 0 offen lds\n\tbuffer_load_dword %3, %5, 0 offen lds\n\ts_waitcnt lgkmcnt(0)"
                     : "=&s"(c) : "s"(cu), "s"(pm), "v"(vp), "s"(srdKa), "s"(srdVa) : "memory");
        asm volatile("v_mov_b32 %0, %1\n\tv_writelane_b32 %0, %2, 1\n\tv_writelane_b32 %0, %3, 2\n\tv_writelane_b32 %0, %4, 3\n\tv_writelane_b32 %0, %5, 4\n\tv_writelane_b32 %0, %6, 5\n\t"
                     "v_writelane_b32 %0, %7, 6\n\tv_writelane_b32 %0, %8, 7\n\tv_writelane_b32 %0, %9, 8\n\tv_writelane_b32 %0, %10, 9\n\tv_writelane_b32 %0, %11, 10\n\t"
                     "v_writelane_b32 %0, %12, 11\n\tv_writelane_b32 %0, %13, 12\n\tv_writelane_b32 %0, %14, 13\n\tv_writelane_b32 %0, %15, 14\n\tv_writelane_b32 %0, %16, 15\n\ts_nop 1"
                     : "=&v"(a) : "s"(c[0]), "s"(c[1]), "s"(c[2]), "s"(c[3]), "s"(c[4]), "s"(c[5]), "s"(c[6]), "s"(c[7]), "s"(c[8]), "s"(c[9]), "s"(c[10]), "s"(c[11]), "s"(c[12]), "s"(c[13]), "s"(c[14]), "s"(c[15]));
        b = __builtin_amdgcn_update_dpp(a, a, 0x101, 0xf, 0xf, false);
    } else {
        u32x2 ab;
        asm volatile("global_load_dwordx2 %0, %1, %2\n\ts_waitcnt vmcnt(0)" : "=&v"(ab) : "v"(min(lane, nseq - 1) * 4), "s"(cu) : "memory");
        a = ab[0]; b = ab[1];
    }
    const int n = lane < nseq ? (b - a + 31) >> 5 : 0;
    const int nmax = __builtin_amdgcn_readlane(wave_max63(n), 63);
    int tb = 1, idx = -1;
    unsigned long long msk = 0;
    for (int i = nmax - 1; i >= I0; --i) {
        msk = __ballot(n > i);
        const int c = __popcll(msk);
        if (r < c) { idx = i; break; }
        r -= c;
    }
    if (idx < 0) {
        int i = I0 - 1, jj = TH / 2 - 1;
#pragma unroll
        for (int lev = 0; lev < (I0 - TH) + TH / 2; ++lev) {
            const bool useB = i >= TH && (jj < 0 || WB * ((i + 2) >> 1) >= WA * (2 * jj + 2));
            const int thr = useB ? i : 2 * jj;
            if (idx < 0) {
                msk = __ballot(n > thr);
                const int c = __popcll(msk);
                if (r < c) { idx = useB ? i : jj; tb = useB; } else r -= c;
            }
            if (useB) --i; else --jj;
        }
    }
    if (idx < 0) { asm volatile("s_waitcnt vmcnt(0)" ::: "memory"); return; }
    const unsigned below = __builtin_amdgcn_mbcnt_hi((unsigned)(msk >> 32), __builtin_amdgcn_mbcnt_lo((unsigned)msk, 0u));
    const unsigned long long hit = __ballot(((msk >> lane) & 1) && below == (unsigned)r);
    const int sl = __builtin_ctzll(hit);
    const int s0 = min(max(__builtin_amdgcn_readlane(a, sl), 0), T), L = min(__builtin_amdgcn_readlane(b, sl), T) - s0;
    if (L <= 0) { asm volatile("s_waitcnt vmcnt(0)" ::: "memory"); return; }
    if (DELAY2 > 0 && j >= 32) __builtin_amdgcn_s_sleep(DELAY2);
    STAMP(1);
    if (tb) attn_item<true>(st, Q, K, V, O, kvh, s0, L, idx, lds);
    else attn_item<false>(st, Q, K, V, O, kvh, s0, L, idx, lds);
}

extern "C" void kernel_launch(void* const* d_in, const int* in_sizes, int n_in, void* d_out, int out_size, void* d_ws, size_t ws_size, hipStream_t stream) {
    const int T = in_sizes[0] / (HQ * D), nseq = in_sizes[3] - 1;
    int slots = (T + 31) / 32 + nseq;
    if (slots < 64) slots = 64;
    (void)hipFuncSetAttribute((const void*)attn_v6, hipFuncAttributeMaxDynamicSharedMemorySize, LDS_BYTES);
    attn_v6<<<slots * 8, 256, LDS_BYTES, stream>>>((const unsigned short*)d_in[0], (const unsigned short*)d_in[1], (const unsigned short*)d_in[2],
                                                 (const int*)d_in[3], (unsigned short*)d_out, nseq, T, (unsigned long long*)d_ws);
}
```

```cpp
#include <hip/hip_runtime.h>
#include <hip/hip_bf16.h>
#include <stdint.h>

typedef short bf16x8 __attribute__((ext_vector_type(8)));
typedef short s16x4 __attribute__((ext_vector_type(4)));
typedef float f32x16 __attribute__((ext_vector_type(16)));
typedef float f32x4 __attribute__((ext_vector_type(4)));
typedef float f32x2 __attribute__((ext_vector_type(2)));
typedef unsigned u32x4 __attribute__((ext_vector_type(4)));
typedef unsigned u32x2 __attribute__((ext_vector_type(2)));
typedef int i32x16 __attribute__((ext_vector_type(16)));
typedef __attribute__((address_space(3))) const char* lds_cptr;
typedef __attribute__((address_space(3))) char* lds_ptr;
typedef short v4i16_t __attribute__((ext_vector_type(4)));

constexpr int HQ = 16, HKV = 8, D = 192;
constexpr unsigned QROW = HQ * D * 2, KROW = HKV * D * 2;
constexpr unsigned KBLK = 32 * KROW;
constexpr int BLK = 12288;
constexpr int LDS_MISC = 6 * BLK;
constexpr int LDS_BYTES = LDS_MISC + 2048;
constexpr float C2 = 0.07216878364870322f * 1.4426950408889634f;
constexpr float LBIG = 1073741824.f;
#define TH 8
#define PFKV 0
#define PFFIRST 0
#define PFB 1
#define DELAY2 0
#define PFQ 0
#define WA 10
#define WB 11
constexpr unsigned LDS_NOWHERE = 0x30000u;
constexpr unsigned OOB = 0x80000000u;

#define DI __device__ __forceinline__
#define MFMA(a, b, c) __builtin_amdgcn_mfma_f32_32x32x16_bf16(a, b, c, 0, 0, 0)
#define SBAR() __builtin_amdgcn_sched_barrier(0)
#define PIN(x) asm volatile("" : "+v"(x))
typedef __bf16 bf16x2_t __attribute__((ext_vector_type(2)));
DI unsigned cvtpk(float lo, float hi) { const f32x2 v = {lo, hi}; return __builtin_bit_cast(unsigned, __builtin_convertvector(v, bf16x2_t)); }
DI float max3f(float a, float b, float c) { float r; asm("v_max3_f32 %0, %1, %2, %3" : "=v"(r) : "v"(a), "v"(b), "v"(c)); return r; }
DI float max2f(float a, float b) { float r; asm("v_max_f32_e32 %0, %1, %2" : "=v"(r) : "v"(a), "v"(b)); return r; }
DI float fmul_s(float a, float b) { float r; asm("v_mul_f32_e32 %0, %1, %2" : "=v"(r) : "v"(a), "v"(b)); return r; }
DI float fma_v(float a, float b, float c) { float r; asm("v_fma_f32 %0, %1, %2, %3" : "=v"(r) : "v"(a), "v"(b), "v"(c)); return r; }
DI void vexp(float& x) { asm volatile("v_exp_f32_e32 %0, %0" : "+v"(x)); }
DI float vadd(float a, float b) { float r; asm volatile("v_add_f32_e32 %0, %1, %2" : "=v"(r) : "v"(a), "v"(b)); return r; }
DI float vfma(float a, float b, float c) { float r; asm volatile("v_fma_f32 %0, %1, %2, %3" : "=v"(r) : "v"(a), "s"(b), "v"(c)); return r; }
DI float vmul(float a, float b) { float r; asm volatile("v_mul_f32_e32 %0, %1, %2" : "=v"(r) : "v"(a), "v"(b)); return r; }
DI float vfma_v(float a, float b, float c) { float r; asm volatile("v_fma_f32 %0, %1, %2, %3" : "=v"(r) : "v"(a), "v"(b), "v"(c)); return r; }
DI unsigned vcvt(float lo, float hi) { unsigned r; asm volatile("v_cvt_pk_bf16_f32 %0, %1, %2" : "=v"(r) : "v"(lo), "v"(hi)); return r; }
DI s16x4 vtr(lds_cptr p) { return __builtin_bit_cast(s16x4, __builtin_amdgcn_ds_read_tr16_b64_v4i16((__attribute__((address_space(3))) v4i16_t*)p)); }
DI bf16x8 ldk(lds_cptr p) { return *(const __attribute__((address_space(3))) bf16x8*)p; }
DI int rfl(int x) { return __builtin_amdgcn_readfirstlane(x); }
DI float swapmax(float x) { auto r = __builtin_amdgcn_permlane32_swap(__float_as_uint(x), __float_as_uint(x), false, false); return max2f(__uint_as_float(r[0]), __uint_as_float(r[1])); }
DI float swapsum(float x) { auto r = __builtin_amdgcn_permlane32_swap(__float_as_uint(x), __float_as_uint(x), false, false); return __uint_as_float(r[0]) + __uint_as_float(r[1]); }

template <int S, int OFF> DI void dma(unsigned mb, unsigned voff, __amdgpu_buffer_rsrc_t srd, unsigned soff) {
    asm volatile("s_add_u32 m0, %0, %5\n\ts_nop 0\n\tbuffer_load_dwordx4 %1, %2, %3 offen offset:%4 lds"
                 :: "s"(mb), "v"(voff), "s"(srd), "s"(soff), "i"(128 * S), "i"(OFF + S * (4096 - 128)) : "memory", "scc");
}
DI void dma3(unsigned mb, unsigned voff, __amdgpu_buffer_rsrc_t srd, unsigned soff) { dma<0, 0>(mb, voff, srd, soff); dma<1, 0>(mb, voff, srd, soff); dma<2, 0>(mb, voff, srd, soff); }
#define DMAL dma
#define LOOPBAR() BARRIER()
#define LOOPWAIT() WAIT_VM(6)
#define WAIT_VM(n) asm volatile("s_waitcnt vmcnt(" #n ")" ::: "memory")
#define BARRIER() asm volatile("s_waitcnt lgkmcnt(0)\n\ts_barrier" ::: "memory")
#define OPIN(nop) asm volatile(nop : "+v"(o[0]), "+v"(o[1]), "+v"(o[2]), "+v"(o[3]), "+v"(o[4]), "+v"(o[5]))

#define STAMP(i)
#define STAMPC(i)

struct LaneAddr { unsigned vK, vV, vQ0, vQ1; };

template <bool TB>
DI void attn_item(unsigned long long* st, const unsigned short* __restrict__ Q, const unsigned short* __restrict__ K, const unsigned short* __restrict__ V,
                  unsigned short* __restrict__ O, int kvh, int s0, int L, int idx, char* lds, int wid, const LaneAddr& la) {
    const int tid = threadIdx.x, lane = tid & 63, l32 = lane & 31, hi = lane >> 5;
    const int hsel = wid & 1, sub = wid >> 1;
    const int n = (L + 31) >> 5;
    const int q0 = TB ? 32 * idx : 64 * idx;
    const int NB = TB ? idx + 1 : min(2 * idx + 2, n);
    const int NU = TB ? (NB + 1) >> 1 : NB;
    const int nu = TB ? (NB + 1 - sub) >> 1 : (2 * idx + sub < n ? 2 * idx + sub + 1 : 0);
    const int diagu = TB ? (((idx & 1) == sub) ? nu - 1 : -1) : nu - 1;

    const unsigned krec = (unsigned)min(L, 32 * NB) * KROW - kvh * 384;
    const __amdgpu_buffer_rsrc_t srdK = __builtin_amdgcn_make_buffer_rsrc((void*)((const char*)K + (size_t)s0 * KROW + kvh * 384), (short)0, (int)krec, 0x00020000);
    const __amdgpu_buffer_rsrc_t srdQ = __builtin_amdgcn_make_buffer_rsrc((void*)((const char*)Q + (size_t)s0 * QROW + kvh * 768), (short)0, (int)((unsigned)L * QROW - kvh * 768), 0x00020000);
    const unsigned ldsb = (unsigned)(uintptr_t)lds;
    const unsigned wb = ldsb + wid * 1024;
    const unsigned vK = la.vK, vV = la.vV;

    dma3(wb, vK, srdK, 0);
    if (TB) dma3(wb + BLK, vK, srdK, KBLK);
    {
        const unsigned qreg = TB ? 4 * BLK + hsel * BLK : (wid == 3 ? 2 * BLK : (3 + wid) * BLK);
        const unsigned qs = (unsigned)(q0 + (TB ? 16 * sub : 32 * sub)) * QROW + hsel * 384;
        const unsigned m = ldsb + qreg + (TB ? 2048 * sub : 0);
#pragma unroll
        for (int g = 0; g < (TB ? 2 : 4); ++g) dma3(m + g * 1024, (g & 1) ? la.vQ1 : la.vQ0, srdQ, qs + g * 8 * QROW);
    }
    SBAR();
    const __amdgpu_buffer_rsrc_t srdV = __builtin_amdgcn_make_buffer_rsrc((void*)((const char*)V + (size_t)s0 * KROW + kvh * 384), (short)0, (int)krec, 0x00020000);
    const int qrel = q0 + (TB ? 0 : 32 * sub) + l32;
    const lds_cptr lp = (lds_cptr)lds;
    lds_cptr kb[2], vb, vbo;
#pragma unroll
    for (int i = 0; i < 2; ++i) kb[i] = lp + (l32 >> 3) * 1024 + (l32 & 7) * 64 + 16 * ((2 * i + hi) ^ ((l32 >> 2) & 3));
    vb = lp + (4 * hi + ((lane & 15) >> 2)) * 64 + 32 * ((lane >> 4) & 1) + 8 * (lane & 3);
#define KADDR(ks) (kb[(ks) & 1] + ((ks) >> 2) * 4096 + (((ks) >> 1) & 1) * 512)
    f32x16 o[6];
#pragma unroll
    for (int i = 0; i < 6; ++i) o[i] = f32x16{};
    OPIN("");

    STAMP(2);
    bf16x8 qf[12];
    WAIT_VM(0);
    if (TB) BARRIER();
    {
        const int qo = TB ? 4 * BLK + hsel * BLK : (wid == 3 ? 2 * BLK : (3 + wid) * BLK);
#pragma unroll
        for (int ks = 0; ks < 12; ++ks) {
            qf[ks] = ldk(KADDR(ks) + qo);
            if (TB) {
                if (ks == 1) dma<0, 0>(wb + 2 * BLK, vK, srdK, 2 * KBLK); if (ks == 3) dma<1, 0>(wb + 2 * BLK, vK, srdK, 2 * KBLK); if (ks == 5) dma<2, 0>(wb + 2 * BLK, vK, srdK, 2 * KBLK);
                if (ks == 7) dma<0, 0>(wb + 3 * BLK, vK, srdK, 3 * KBLK); if (ks == 9) dma<1, 0>(wb + 3 * BLK, vK, srdK, 3 * KBLK); if (ks == 11) dma<2, 0>(wb + 3 * BLK, vK, srdK, 3 * KBLK);
            } else {
                if (ks == 3) dma<0, 0>(wb + BLK, vK, srdK, KBLK); if (ks == 7) dma<1, 0>(wb + BLK, vK, srdK, KBLK); if (ks == 11) dma<2, 0>(wb + BLK, vK, srdK, KBLK);
            }
        }
    }
    BARRIER();
    STAMP(3);
    kb[0] += TB ? sub * BLK : 0; kb[1] += TB ? sub * BLK : 0;
    vb += TB ? sub * BLK : 3 * BLK;
    vbo = vb + (TB ? 512 - sub * 512 : 512);
    vb += TB ? sub * 512 : 0;

    f32x16 S, X;
    float m = 0.f, l = 0.f;
    auto mask = [&](int kblock) {
        const int k0 = kblock * 32 + 4 * hi - qrel;
#pragma unroll
        for (int r = 0; r < 16; ++r) if (k0 + (r & 3) + 8 * (r >> 2) > 0) S[r] = -__builtin_inff();
    };
    auto rowmax = [&]() -> float {
        float a = max3f(S[0], S[1], S[2]), b = max3f(S[3], S[4], S[5]);
        a = max3f(a, S[6], S[7]); b = max3f(b, S[8], S[9]);
        a = max3f(a, S[10], S[11]); b = max3f(b, S[12], S[13]);
        a = max3f(a, S[14], S[15]);
        return swapmax(max2f(a, b));
    };
    const unsigned vK1 = vK + KBLK, vV1 = vV + KBLK;
    u32x4 p0, p1;
    bf16x8 kf[6];
    s16x4 vlo[6], vhi[6];
#define EXP(e) do { float t_ = X[e]; vexp(t_); X[e] = t_; } while (0)
#define FM(e) X[e] = vfma(S[e], C2, nm)
#define KRD(k) do { kf[(k) % 6] = ldk(KADDR(k)); SBAR(); } while (0)
#define VRD(k) do { const lds_cptr vp_ = ((k) & 1 ? vbo : vb) + (((k) % 6) >> 1) * 4096 + ((k) / 6) * 2048; vlo[(k) % 6] = vtr(vp_); vhi[(k) % 6] = vtr(vp_ + 1024); SBAR(); } while (0)
#define VFR(k) ((bf16x8){vlo[(k) % 6][0], vlo[(k) % 6][1], vlo[(k) % 6][2], vlo[(k) % 6][3], vhi[(k) % 6][0], vhi[(k) % 6][1], vhi[(k) % 6][2], vhi[(k) % 6][3]})
#define QK(k) do { S = MFMA(kf[(k) % 6], qf[k], (k) == 0 ? f32x16{} : S); PIN(S); SBAR(); } while (0)
#define PV(k) do { o[(k) % 6] = MFMA(VFR(k), __builtin_bit_cast(bf16x8, (k) < 6 ? p0 : p1), o[(k) % 6]); PIN(o[(k) % 6]); SBAR(); } while (0)
#define ADD0(e) ps0 = vadd(ps0, X[e])
#define ADD1(e) ps1 = vadd(ps1, X[e])
#define CV(P, i, e) P[i] = vcvt(X[e], X[(e) + 1])

    int r0 = 0, r1 = TB ? 2 * BLK : BLK, r2 = TB ? 4 * BLK : 2 * BLK;
    const unsigned mV1 = (TB || 1 < NB) ? wb + (TB ? 5 : 4) * BLK : LDS_NOWHERE, mK2 = 2 < NB ? wb + 2 * BLK : LDS_NOWHERE;
    if (nu > 0) {
        KRD(1); KRD(0); KRD(3); KRD(2); KRD(5); KRD(4);
        QK(0); dma<0, 0>(wb + (TB ? 4 : 3) * BLK, vV, srdV, 0); SBAR();
        QK(1); KRD(7); dma<1, 0>(wb + (TB ? 4 : 3) * BLK, vV, srdV, 0); SBAR();
        QK(2); KRD(6); dma<2, 0>(wb + (TB ? 4 : 3) * BLK, vV, srdV, 0); SBAR();
        QK(3); KRD(9); dma<0, 0>(mV1, vV, srdV, KBLK); SBAR();
        QK(4); KRD(8); dma<1, 0>(mV1, vV, srdV, KBLK); SBAR();
        QK(5); KRD(11); dma<2, 0>(mV1, vV, srdV, KBLK); SBAR();
        QK(6); KRD(10); if (!TB) dma<0, 0>(mK2, vK, srdK, 2 * KBLK); SBAR();
        QK(7); if (!TB) dma<1, 0>(mK2, vK, srdK, 2 * KBLK); SBAR();
        QK(8); if (!TB) dma<2, 0>(mK2, vK, srdK, 2 * KBLK); SBAR();
        QK(9); QK(10); QK(11);
    } else {
        dma3(wb + 3 * BLK, vV, srdV, 0); dma3(mV1, vV, srdV, KBLK); dma3(mK2, vK, srdK, 2 * KBLK);
    }
    WAIT_VM(6); BARRIER();
    kb[0] += r1; kb[1] += r1; if (TB) { vb += r2; vbo += r2; }
    if (nu > 1) { KRD(1); KRD(0); KRD(3); KRD(2); KRD(5); KRD(4); }
    if (TB) { dma<0, 0>(wb + r0, vK, srdK, 4 * KBLK); dma<1, 0>(wb + r0, vK, srdK, 4 * KBLK); }
    if (nu > 0) {
        asm volatile("s_nop 11" : "+v"(S));
        if (diagu == 0) mask(TB ? sub : 0);
        m = rowmax() * C2;
        const float nm = -m;
        FM(0); FM(1); FM(2); FM(3); FM(4); FM(5); FM(6); FM(7); FM(8); FM(9); FM(10); FM(11); FM(12); FM(13); FM(14); FM(15);
        EXP(0); EXP(1); EXP(2); EXP(3);
    } else X = f32x16{};
    const float nm = -m;

#define ROTATE() do { const int t_ = TB ? r2 : r0; if (TB) { r2 = r1; r1 = r0; r0 = t_; } else { r0 = r1; r1 = r2; r2 = t_; } so += (TB ? 2 : 1) * KBLK; ++u; } while (0)
#define BK_WAIT() LOOPWAIT()
    STAMP(4); STAMPC(8);
    unsigned barw = 0, barn = 0; (void)barw; (void)barn;
    int u = 0;
    unsigned so = 0;
    while (u + 1 < nu) {
        float ps0, ps1;
        const unsigned soK = so + (TB ? 4 : 3) * KBLK, soV = so + 2 * KBLK;
        const unsigned mK = (TB || u + 3 < NU) ? wb + r0 : LDS_NOWHERE, mV = TB ? wb + r1 : (u + 2 < NU ? wb + 3 * BLK + r2 : LDS_NOWHERE);
        const int dk = TB ? r0 - r1 : r2 - r1, dv = TB ? r1 - r2 : r1 - r0;
        if (TB) {
            QK(0);  EXP(4); EXP(5); ps0 = vadd(X[0], X[1]); SBAR();
            QK(1);  KRD(7); DMAL<2, 0>(mK, vK, srdK, soK); EXP(6); SBAR();
            QK(2);  KRD(6); EXP(7); EXP(8); ps1 = vadd(X[2], X[3]); SBAR();
            QK(3);  KRD(9); DMAL<0, BLK>(mK, vK1, srdK, soK); EXP(9); SBAR();
            QK(4);  KRD(8); EXP(10); CV(p0, 0, 0); ADD0(4); ADD1(5); SBAR();
            QK(5);  KRD(11); DMAL<1, BLK>(mK, vK1, srdK, soK); EXP(11); SBAR();
            QK(6);  KRD(10); EXP(12); CV(p0, 1, 2); CV(p0, 2, 4); SBAR();
            QK(7);  DMAL<2, BLK>(mK, vK1, srdK, soK); EXP(13); ADD0(6); SBAR();
            QK(8);
            LOOPWAIT(); LOOPBAR();
            VRD(1); VRD(0); VRD(3); VRD(2);
            QK(9);  DMAL<0, 0>(mV, vV, srdV, soV); VRD(5); EXP(14); SBAR();
            QK(10); VRD(4); EXP(15); CV(p0, 3, 6); ADD1(7); ADD0(8); SBAR();
            QK(11); DMAL<1, 0>(mV, vV, srdV, soV); ADD1(9); CV(p1, 0, 8); ADD0(10); SBAR();
            PV(0);  ADD1(11); CV(p1, 1, 10); ADD0(12); ADD1(13); CV(p1, 2, 12); SBAR();
            PV(1);  VRD(7); DMAL<2, 0>(mV, vV, srdV, soV); ADD0(14); SBAR();
            if (__builtin_expect(u + 1 == diagu, 0)) { asm volatile("s_nop 7" : "+v"(S)); mask(2 * u + 2 + sub); }
            PV(2);  VRD(6); ADD1(15); CV(p1, 3, 14); FM(0); FM(1); SBAR();
            PV(3);  VRD(9); DMAL<0, BLK>(mV, vV1, srdV, soV); FM(2); SBAR();
            PV(4);  VRD(8); FM(3); FM(4); FM(5); ps0 = vadd(ps0, ps1); SBAR();
            PV(5);  VRD(11); DMAL<1, BLK>(mV, vV1, srdV, soV); FM(6); SBAR();
            PV(6);  VRD(10); FM(7); FM(8); FM(9); l = vadd(l, ps0); SBAR();
            PV(7);  DMAL<2, BLK>(mV, vV1, srdV, soV); FM(10); FM(11); kb[0] += dk; kb[1] += dk; PIN(kb[0]); PIN(kb[1]); SBAR();
            PV(8);
        } else {
            QK(0);  EXP(4); EXP(5); ps0 = vadd(X[0], X[1]); SBAR();
            QK(1);  KRD(7); EXP(6); EXP(7); ps1 = vadd(X[2], X[3]); SBAR();
            QK(2);  KRD(6); EXP(8); CV(p0, 0, 0); ADD0(4); ADD1(5); SBAR();
            QK(3);  KRD(9); DMAL<0, 0>(mK, vK, srdK, soK); EXP(9); SBAR();
            QK(4);  KRD(8); EXP(10); CV(p0, 1, 2); CV(p0, 2, 4); SBAR();
            QK(5);  KRD(11); DMAL<1, 0>(mK, vK, srdK, soK); EXP(11); SBAR();
            QK(6);  KRD(10); EXP(12); ADD0(6); CV(p0, 3, 6); ADD1(7); SBAR();
            QK(7);  VRD(1); DMAL<2, 0>(mK, vK, srdK, soK); EXP(13); SBAR();
            QK(8);  VRD(0); EXP(14); ADD0(8); ADD1(9); SBAR();
            QK(9);  VRD(3); EXP(15); CV(p1, 0, 8); SBAR();
            QK(10); VRD(2); ADD0(10); ADD1(11); CV(p1, 1, 10); SBAR();
            QK(11); VRD(5); ADD0(12); ADD1(13); CV(p1, 2, 12); SBAR();
            PV(0);  VRD(4); DMAL<0, 0>(mV, vV, srdV, soV); ADD0(14); SBAR();
            PV(1);  VRD(7); ADD1(15); CV(p1, 3, 14); SBAR();
            if (__builtin_expect(u + 1 == diagu, 0)) { asm volatile("s_nop 7" : "+v"(S)); mask(u + 1); }
            PV(2);  VRD(6); DMAL<1, 0>(mV, vV, srdV, soV); FM(0); SBAR();
            PV(3);  VRD(9); FM(1); FM(2); FM(3); ps0 = vadd(ps0, ps1); SBAR();
            PV(4);  VRD(8); DMAL<2, 0>(mV, vV, srdV, soV); FM(4); SBAR();
            PV(5);  VRD(11); FM(5); FM(6); FM(7); l = vadd(l, ps0); SBAR();
            PV(6);  VRD(10); FM(8); FM(9); FM(10); SBAR();
            PV(7);  FM(11); FM(12); FM(13); kb[0] += dk; kb[1] += dk; PIN(kb[0]); PIN(kb[1]); SBAR();
            PV(8);
        }
        BK_WAIT(); LOOPBAR();
        KRD(1); KRD(0); KRD(3); KRD(2);
        if (TB) {
            PV(9);  DMAL<0, 0>(wb + r2, vK, srdK, soK + 2 * KBLK); KRD(5); FM(12); FM(13); SBAR();
            PV(10); KRD(4); FM(14); FM(15); EXP(0); vb += dv; vbo += dv; PIN(vb); PIN(vbo); SBAR();
            PV(11); DMAL<1, 0>(wb + r2, vK, srdK, soK + 2 * KBLK); EXP(1); EXP(2); EXP(3); SBAR();
        } else {
            PV(9);  KRD(5); FM(14); FM(15); EXP(0); SBAR();
            PV(10); KRD(4); EXP(1); EXP(2); vb += dv; vbo += dv; PIN(vb); PIN(vbo); SBAR();
            PV(11); EXP(3); SBAR();
        }
        ROTATE();
    }
    STAMPC(9);
#define TAIL_HEAD() \
        const bool last = u == NU - 1; \
        if (!last) { \
            if (TB) { dma<2, 0>(wb + r0, vK, srdK, so + 4 * KBLK); dma3(wb + r0 + BLK, vK1, srdK, so + 4 * KBLK); } \
            else { dma3(u + 3 < NU ? wb + r0 : LDS_NOWHERE, vK, srdK, so + 3 * KBLK); dma3(u + 2 < NU ? wb + 3 * BLK + r2 : LDS_NOWHERE, vV, srdV, so + 2 * KBLK); } \
        }
#define TAIL_MID() \
        if (TB) { if (last) { WAIT_VM(0); } else { WAIT_VM(6); } BARRIER(); }
#define TAIL_END() \
        if (!last) { BK_WAIT(); BARRIER(); if (TB) { dma<0, 0>(wb + r2, vK, srdK, so + 6 * KBLK); dma<1, 0>(wb + r2, vK, srdK, so + 6 * KBLK); } }
    if (u < nu) {
        TAIL_HEAD()
#pragma unroll
        for (int e = 4; e < 8; ++e) EXP(e);
        float ps0 = vadd(X[0], X[1]), ps1 = vadd(X[2], X[3]);
        CV(p0, 0, 0); CV(p0, 1, 2); EXP(8); EXP(9); CV(p0, 2, 4); CV(p0, 3, 6);
        TAIL_MID()
        VRD(1); VRD(0); VRD(3); VRD(2); VRD(5); VRD(4);
        EXP(10); EXP(11); EXP(12); EXP(13); SBAR();
        const bool ld = TB && !last;
        PV(0);  if (ld) dma<0, 0>(wb + r1, vV, srdV, so + 2 * KBLK); EXP(14); EXP(15); SBAR();
        PV(1);  VRD(7); ADD0(4); ADD1(5); SBAR();
        PV(2);  VRD(6); if (ld) dma<1, 0>(wb + r1, vV, srdV, so + 2 * KBLK); CV(p1, 0, 8); CV(p1, 1, 10); SBAR();
        PV(3);  VRD(9); CV(p1, 2, 12); CV(p1, 3, 14); SBAR();
        PV(4);  VRD(8); if (ld) dma<2, 0>(wb + r1, vV, srdV, so + 2 * KBLK); ADD0(6); ADD1(7); SBAR();
        PV(5);  VRD(11); ADD0(8); ADD1(9); ADD0(10); SBAR();
        PV(6);  VRD(10); if (ld) dma<0, BLK>(wb + r1, vV1, srdV, so + 2 * KBLK); ADD1(11); ADD0(12); SBAR();
        PV(7);  if (ld) dma<1, BLK>(wb + r1, vV1, srdV, so + 2 * KBLK); ADD1(13); SBAR();
        PV(8);  if (ld) dma<2, BLK>(wb + r1, vV1, srdV, so + 2 * KBLK); ADD0(14); ADD1(15); SBAR();
        PV(9); PV(10); PV(11);
        l += ps0 + ps1;
        TAIL_END()
        ROTATE();
    }
    while (u < NU) {
        TAIL_HEAD()
        TAIL_MID()
        if (TB && !last) { dma3(wb + r1, vV, srdV, so + 2 * KBLK); dma3(wb + r1 + BLK, vV1, srdV, so + 2 * KBLK); }
        TAIL_END()
        ROTATE();
    }
#undef TAIL_HEAD
#undef TAIL_MID
#undef TAIL_END
    if (TB) WAIT_VM(0);
    OPIN("s_nop 15");
    int lane2;
    asm volatile("v_mbcnt_lo_u32_b32 %0, -1, 0\n\tv_mbcnt_hi_u32_b32 %0, -1, %0" : "=v"(lane2));

    const int l32e = lane2 & 31, hie = lane2 >> 5, r8e = lane2 >> 3, c8e = lane2 & 7, r16e = lane2 >> 4;
    STAMP(5);
    const int reg = TB ? (sub ? r2 : r1) + hsel * BLK : (wid < 3 ? wid * BLK : 3 * BLK + r0);
    l = swapsum(l);
    if (__builtin_expect(__any(!(l < LBIG)), 0)) {
        const unsigned mb = ldsb + reg;
        const int dr2 = (lane2 >> 2) & 7, dh2 = lane2 >> 5, dc2 = lane2 & 3, dg2 = (lane2 >> 4) & 1;
        const unsigned vK0 = dr2 * KROW + 64u * dh2 + 16u * (dc2 ^ dg2), vK1_ = dr2 * KROW + 64u * dh2 + 16u * (dc2 ^ (2 + dg2)), vV0 = dr2 * KROW + 64u * dh2 + 16u * dc2;
        kb[0] = lp + reg + (l32e >> 3) * 1024 + (l32e & 7) * 64 + 16 * (hie ^ ((l32e >> 2) & 3));
        kb[1] = lp + reg + (l32e >> 3) * 1024 + (l32e & 7) * 64 + 16 * ((2 + hie) ^ ((l32e >> 2) & 3));
        vb = lp + reg + (4 * hie + ((lane2 & 15) >> 2)) * 64 + 32 * ((lane2 >> 4) & 1) + 8 * (lane2 & 3);
        vbo = vb + (TB ? 512 - sub * 512 : 512); vb += TB ? sub * 512 : 0;
        bf16x8 q2[12];
        {
            const unsigned vQ0 = dr2 * QROW + 64u * dh2 + 16u * (dc2 ^ dg2), vQ1 = dr2 * QROW + 64u * dh2 + 16u * (dc2 ^ (2 + dg2));
            const unsigned qs = (unsigned)(q0 + (TB ? 0 : 32 * sub)) * QROW + hsel * 384;
#pragma unroll
            for (int g = 0; g < 4; ++g) dma3(mb + g * 1024, (g & 1) ? vQ1 : vQ0, srdQ, qs + g * 8 * QROW);
            WAIT_VM(0);
#pragma unroll
            for (int ks = 0; ks < 12; ++ks) q2[ks] = ldk(KADDR(ks));
        }
        auto scores = [&](int t) {
            const int b = TB ? 2 * t + sub : t;
            asm volatile("s_waitcnt lgkmcnt(0)" ::: "memory");
#pragma unroll
            for (int g = 0; g < 4; ++g) dma3(mb + g * 1024, (g & 1) ? vK1_ : vK0, srdK, b * KBLK + g * 8 * KROW);
            WAIT_VM(0);
            S = MFMA(ldk(KADDR(0)), q2[0], f32x16{});
#pragma unroll
            for (int ks = 1; ks < 12; ++ks) S = MFMA(ldk(KADDR(ks)), q2[ks], S);
            if (t == diagu) {
                const int k0 = b * 32 + 4 * hie - (q0 + (TB ? 0 : 32 * sub) + l32e);
#pragma unroll
                for (int r = 0; r < 16; ++r) if (k0 + (r & 3) + 8 * (r >> 2) > 0) S[r] = -__builtin_inff();
            }
        };
        float mx = -__builtin_inff();
#pragma unroll 1
        for (int t = 0; t < nu; ++t) { scores(t); asm volatile("s_nop 15" : "+v"(S)); mx = max2f(mx, rowmax()); }
        m = mx * C2;
#pragma unroll
        for (int i = 0; i < 6; ++i) o[i] = f32x16{};
        l = 0.f;
#pragma unroll 1
        for (int t = 0; t < nu; ++t) {
            scores(t);
            const int b = TB ? 2 * t + sub : t;
            asm volatile("s_waitcnt lgkmcnt(0)" ::: "memory");
#pragma unroll
            for (int g = 0; g < 4; ++g) dma3(mb + g * 1024, vV0, srdV, b * KBLK + g * 8 * KROW);
            float ps = 0.f;
#pragma unroll
            for (int e = 0; e < 16; ++e) { X[e] = __builtin_amdgcn_exp2f(__builtin_fmaf(S[e], C2, -m)); ps += X[e]; }
            l += ps;
            p0 = (u32x4){cvtpk(X[0], X[1]), cvtpk(X[2], X[3]), cvtpk(X[4], X[5]), cvtpk(X[6], X[7])};
            p1 = (u32x4){cvtpk(X[8], X[9]), cvtpk(X[10], X[11]), cvtpk(X[12], X[13]), cvtpk(X[14], X[15])};
            WAIT_VM(0);
            #pragma unroll
            for (int k = 0; k < 12; ++k) { VRD(k); o[k % 6] = MFMA(VFR(k), __builtin_bit_cast(bf16x8, k < 6 ? p0 : p1), o[k % 6]); }
        }
        l = swapsum(l);
        asm volatile("s_waitcnt lgkmcnt(0)" ::: "memory");
    }

    const __amdgpu_buffer_rsrc_t srdO = __builtin_amdgcn_make_buffer_rsrc((void*)((char*)O + (size_t)s0 * QROW + kvh * 768), (short)0, (int)((unsigned)L * QROW - kvh * 768), 0x00020000);
    const lds_ptr lw = (lds_ptr)lds;
    const unsigned vO = r8e * QROW + c8e * 16;
#define STG(base, db, g, lo, hi_) *(__attribute__((address_space(3))) u32x2*)((base) + ((db) >> 1) * 4096 + 16 * (((4 * (db) + (g)) & 7) ^ (l32e & 7))) = (u32x2){lo, hi_}
    if constexpr (!TB) {
        if (nu > 0) {
            float il = __builtin_amdgcn_rcpf(l);
            asm volatile("s_nop 1" : "+v"(il));
            const lds_ptr wp = lw + reg + l32e * 128 + 8 * hie;
            const lds_ptr rp = lw + reg + r8e * 128 + 16 * (c8e ^ r8e);
            const unsigned ob = (unsigned)(q0 + 32 * sub) * QROW + hsel * 384;
            u32x4 w[4];
            auto scale = [&](int db) {
                float y[16];
#pragma unroll
                for (int r = 0; r < 16; ++r) y[r] = vmul(o[db][r], il);
                unsigned c[8];
#pragma unroll
                for (int r = 0; r < 8; ++r) c[r] = vcvt(y[2 * r], y[2 * r + 1]);
#pragma unroll
                for (int g = 0; g < 4; ++g) STG(wp, db, g, c[2 * g], c[2 * g + 1]);
                SBAR();
            };
            auto rd = [&](int s) {
#pragma unroll
                for (int g = 0; g < 4; ++g) w[g] = *(const __attribute__((address_space(3))) u32x4*)(rp + s * 4096 + g * 1024);
                SBAR();
            };
            auto st_ = [&](int s) {
#pragma unroll
                for (int g = 0; g < 4; ++g) __builtin_amdgcn_raw_buffer_store_b128(w[g], srdO, vO + s * 128, ob + g * 8 * QROW, 0);
                SBAR();
            };
            scale(0); scale(1); rd(0); scale(2); st_(0); scale(3); rd(1); scale(4); st_(1); scale(5); rd(2); st_(2);
        }
    } else {
        const lds_ptr xp = lw + reg + lane2 * 16;
        const lds_ptr xq = lw + (sub ? r1 : r2) + hsel * BLK + lane2 * 16;
        __attribute__((address_space(3))) f32x2* ml = (__attribute__((address_space(3))) f32x2*)(lw + LDS_MISC);
        ml[wid * 64 + lane2] = (f32x2){m, l};
#pragma unroll
        for (int i = 0; i < 3; ++i)
#pragma unroll
            for (int g = 0; g < 4; ++g) *(__attribute__((address_space(3))) f32x4*)(xp + (4 * i + g) * 1024) = (f32x4){o[2 * i + 1][4 * g], o[2 * i + 1][4 * g + 1], o[2 * i + 1][4 * g + 2], o[2 * i + 1][4 * g + 3]};
        BARRIER();
        const f32x2 pm = ml[(wid ^ 2) * 64 + lane2];
        f32x4 x[12];
#pragma unroll
        for (int i = 0; i < 12; ++i) x[i] = *(const __attribute__((address_space(3))) f32x4*)(xq + i * 1024);
        SBAR();
        const float mm = max2f(m, pm[0]);
        const float fa = __builtin_amdgcn_exp2f(m - mm), fb = __builtin_amdgcn_exp2f(pm[0] - mm);
        const float il = __builtin_amdgcn_rcpf(l * fa + pm[1] * fb);
        float wa = fa * il, wb2 = fb * il;
        asm volatile("s_nop 1" : "+v"(wa), "+v"(wb2));
        const lds_ptr wp = lw + r0 + hsel * BLK + l32e * 128 + 8 * hie + 16 * ((4 * sub) ^ (l32e & 4));
#pragma unroll
        for (int i = 0; i < 3; ++i) {
            float y[16];
#pragma unroll
            for (int r = 0; r < 16; ++r) y[r] = vmul(x[4 * i + (r >> 2)][r & 3], wb2);
#pragma unroll
            for (int r = 0; r < 16; ++r) y[r] = vfma_v(o[2 * i][r], wa, y[r]);
            unsigned c[8];
#pragma unroll
            for (int r = 0; r < 8; ++r) c[r] = vcvt(y[2 * r], y[2 * r + 1]);
#pragma unroll
            for (int g = 0; g < 4; ++g) *(__attribute__((address_space(3))) u32x2*)(wp + i * 4096 + 16 * (g ^ (l32e & 3))) = (u32x2){c[2 * g], c[2 * g + 1]};
            SBAR();
        }
        BARRIER();
        const lds_ptr rp = lw + r0 + hsel * BLK + sub * 2048 + r8e * 128 + 16 * (c8e ^ r8e);
        const unsigned ob = (unsigned)(q0 + 16 * sub) * QROW + hsel * 384;
        u32x4 w[6];
#pragma unroll
        for (int i = 0; i < 6; ++i) w[i] = *(const __attribute__((address_space(3))) u32x4*)(rp + (i % 3) * 4096 + (i / 3) * 1024);
#pragma unroll
        for (int i = 0; i < 6; ++i) __builtin_amdgcn_raw_buffer_store_b128(w[i], srdO, vO + (i % 3) * 128, ob + (i / 3) * 8 * QROW, 0);
    }
#undef STG
    STAMP(6);
#undef EXP
#undef FM
#undef KRD
#undef VRD
#undef VFR
#undef QK
#undef PV
#undef ADD0
#undef ADD1
#undef ROTATE
#undef BK_WAIT
#undef CV
#undef KADDR
}

DI int wave_max63(int x) {
    asm volatile("s_nop 1\n\tv_max_i32_dpp %0, %0, %0 row_shr:1 row_mask:0xf bank_mask:0xf\n\ts_nop 1\n\tv_max_i32_dpp %0, %0, %0 row_shr:2 row_mask:0xf bank_mask:0xf\n\t"
                 "s_nop 1\n\tv_max_i32_dpp %0, %0, %0 row_shr:4 row_mask:0xf bank_mask:0xf\n\ts_nop 1\n\tv_max_i32_dpp %0, %0, %0 row_shr:8 row_mask:0xf bank_mask:0xf\n\t"
                 "s_nop 1\n\tv_max_i32_dpp %0, %0, %0 row_bcast:15 row_mask:0xa bank_mask:0xf\n\ts_nop 1\n\tv_max_i32_dpp %0, %0, %0 row_bcast:31 row_mask:0xc bank_mask:0xf\n\ts_nop 0" : "+v"(x));
    return x;
}
constexpr int first_pure_b() { int i = TH; while (WB * ((i + 2) >> 1) < WA * TH) ++i; return i; }
constexpr int I0 = first_pure_b();
constexpr int NMIX = (I0 - TH) + TH / 2;
static_assert(NMIX <= 12 && I0 < 60, "level table (16 levels; thresholds are inline constants)");
constexpr int level(int k) {
    if (k < 4) return (I0 + 3 - k) | 0x10000;
    k -= 4;
    if (k >= NMIX) return (int)0xffff0040;
    int i = I0 - 1, jj = TH / 2 - 1, out = 0;
    for (int lev = 0; lev <= k; ++lev) {
        const bool useB = i >= TH && (jj < 0 || WB * ((i + 2) >> 1) >= WA * (2 * jj + 2));
        if (useB) { out = i | 0x10000; --i; } else { out = 2 * jj; --jj; }
    }
    return out;
}

__global__ __launch_bounds__(256, 2) void attn_v10(const unsigned short* __restrict__ Q, const unsigned short* __restrict__ K,
                                                  const unsigned short* __restrict__ V, const int* __restrict__ cu,
                                                  unsigned short* __restrict__ O, int nseq, int T, unsigned long long* dbg) {
    extern __shared__ __attribute__((aligned(16))) char lds[];
    unsigned long long st[16];
    STAMP(0);
    const int lane = threadIdx.x & 63;
    const int kvh = blockIdx.x & 7, j = blockIdx.x >> 3;
    const int wv = rfl(threadIdx.x >> 6);
    unsigned vp;
    LaneAddr la;
    {
        const unsigned li = (j * 4 + wv) * 32 + (lane & 31), tk = li / 3u;
        vp = (tk * KROW + (li - 3u * tk) * 128u) | ((PFB && lane < 32 && j < 64) ? 0u : OOB);
        const int dr = (lane >> 2) & 7, dh = lane >> 5, dc = lane & 3, dg = (lane >> 4) & 1;
        la.vK = (8 * wv + dr) * KROW + 64u * dh + 16u * (dc ^ ((2 * wv + dg) & 3));
        la.vV = (8 * wv + dr) * KROW + 64u * dh + 16u * dc;
        la.vQ0 = dr * QROW + 64u * dh + 16u * (dc ^ dg);
        la.vQ1 = dr * QROW + 64u * dh + 16u * (dc ^ (2 + dg));
        asm volatile("" : "+v"(vp), "+v"(la.vK), "+v"(la.vV), "+v"(la.vQ0), "+v"(la.vQ1));
    }
    asm volatile("" :: "s"(Q), "s"(K), "s"(V), "s"(O), "s"(cu), "s"(nseq), "s"(T));
    int r = j < 32 ? j : (j < 64 ? 95 - j : j);
    int a, b;
    if (nseq <= 15) {
        const unsigned pm = (unsigned)(uintptr_t)lds + LDS_MISC + wv * 256;
        const __amdgpu_buffer_rsrc_t srdKa = __builtin_amdgcn_make_buffer_rsrc((void*)((const char*)K + kvh * 384), (short)0, (int)((unsigned)T * KROW - kvh * 384), 0x00020000);
        const __amdgpu_buffer_rsrc_t srdVa = __builtin_amdgcn_make_buffer_rsrc((void*)((const char*)V + kvh * 384), (short)0, (int)((unsigned)T * KROW - kvh * 384), 0x00020000);
        i32x16 c;
        asm volatile("s_load_dwordx16 %0, %1, 0x0\n\ts_mov_b32 m0, %2\n\ts_nop 0\n\tbuffer_load_dword %3, %4, 0 offen lds\n\tbuffer_load_dword %3, %5, 0 offen lds\n\ts_waitcnt lgkmcnt(0)"
                     : "=&s"(c) : "s"(cu), "s"(pm), "v"(vp), "s"(srdKa), "s"(srdVa) : "memory");
        asm volatile("v_mov_b32 %0, %1\n\tv_writelane_b32 %0, %2, 1\n\tv_writelane_b32 %0, %3, 2\n\tv_writelane_b32 %0, %4, 3\n\tv_writelane_b32 %0, %5, 4\n\tv_writelane_b32 %0, %6, 5\n\t"
                     "v_writelane_b32 %0, %7, 6\n\tv_writelane_b32 %0, %8, 7\n\tv_writelane_b32 %0, %9, 8\n\tv_writelane_b32 %0, %10, 9\n\tv_writelane_b32 %0, %11, 10\n\t"
                     "v_writelane_b32 %0, %12, 11\n\tv_writelane_b32 %0, %13, 12\n\tv_writelane_b32 %0, %14, 13\n\tv_writelane_b32 %0, %15, 14\n\tv_writelane_b32 %0, %16, 15\n\ts_nop 1"
                     : "=&v"(a) : "s"(c[0]), "s"(c[1]), "s"(c[2]), "s"(c[3]), "s"(c[4]), "s"(c[5]), "s"(c[6]), "s"(c[7]), "s"(c[8]), "s"(c[9]), "s"(c[10]), "s"(c[11]), "s"(c[12]), "s"(c[13]), "s"(c[14]), "s"(c[15]));
        b = __builtin_amdgcn_update_dpp(a, a, 0x101, 0xf, 0xf, false);
    } else {
        u32x2 ab;
        asm volatile("global_load_dwordx2 %0, %1, %2\n\ts_waitcnt vmcnt(0)" : "=&v"(ab) : "v"(min(lane, nseq - 1) * 4), "s"(cu) : "memory");
        a = ab[0]; b = ab[1];
    }
    const int n = lane < nseq ? (b - a + 31) >> 5 : 0;
    int code = -1;
    unsigned long long msk = 0;
    if (__builtin_expect(__ballot(n > I0 + 4) != 0, 0)) {
        const int nmax = __builtin_amdgcn_readlane(wave_max63(n), 63);
        for (int i = nmax - 1; i >= I0 + 4; --i) {
            msk = __ballot(n > i);
            const int c = __popcll(msk);
            if (r < c) { code = i | 0x10000; goto found; }
            r -= c;
        }
    }
    {
        int cnt;
        asm volatile("v_cmp_lt_i32_e64 %0, (%c5&65535), %4\n\ts_bcnt1_i32_b64 %2, %0\n\ts_mov_b32 %3, %c5\n\ts_cmp_lt_u32 %1, %2\n\ts_cbranch_scc1 1f\n\ts_sub_u32 %1, %1, %2\n\t"
                     "v_cmp_lt_i32_e64 %0, (%c6&65535), %4\n\ts_bcnt1_i32_b64 %2, %0\n\ts_mov_b32 %3, %c6\n\ts_cmp_lt_u32 %1, %2\n\ts_cbranch_scc1 1f\n\ts_sub_u32 %1, %1, %2\n\t"
                     "v_cmp_lt_i32_e64 %0, (%c7&65535), %4\n\ts_bcnt1_i32_b64 %2, %0\n\ts_mov_b32 %3, %c7\n\ts_cmp_lt_u32 %1, %2\n\ts_cbranch_scc1 1f\n\ts_sub_u32 %1, %1, %2\n\t"
                     "v_cmp_lt_i32_e64 %0, (%c8&65535), %4\n\ts_bcnt1_i32_b64 %2, %0\n\ts_mov_b32 %3, %c8\n\ts_cmp_lt_u32 %1, %2\n\ts_cbranch_scc1 1f\n\ts_sub_u32 %1, %1, %2\n\t"
                     "v_cmp_lt_i32_e64 %0, (%c9&65535), %4\n\ts_bcnt1_i32_b64 %2, %0\n\ts_mov_b32 %3, %c9\n\ts_cmp_lt_u32 %1, %2\n\ts_cbranch_scc1 1f\n\ts_sub_u32 %1, %1, %2\n\t"
                     "v_cmp_lt_i32_e64 %0, (%c10&65535), %4\n\ts_bcnt1_i32_b64 %2, %0\n\ts_mov_b32 %3, %c10\n\ts_cmp_lt_u32 %1, %2\n\ts_cbranch_scc1 1f\n\ts_sub_u32 %1, %1, %2\n\t"
                     "v_cmp_lt_i32_e64 %0, (%c11&65535), %4\n\ts_bcnt1_i32_b64 %2, %0\n\ts_mov_b32 %3, %c11\n\ts_cmp_lt_u32 %1, %2\n\ts_cbranch_scc1 1f\n\ts_sub_u32 %1, %1, %2\n\t"
                     "v_cmp_lt_i32_e64 %0, (%c12&65535), %4\n\ts_bcnt1_i32_b64 %2, %0\n\ts_mov_b32 %3, %c12\n\ts_cmp_lt_u32 %1, %2\n\ts_cbranch_scc1 1f\n\ts_sub_u32 %1, %1, %2\n\t"
                     "v_cmp_lt_i32_e64 %0, (%c13&65535), %4\n\ts_bcnt1_i32_b64 %2, %0\n\ts_mov_b32 %3, %c13\n\ts_cmp_lt_u32 %1, %2\n\ts_cbranch_scc1 1f\n\ts_sub_u32 %1, %1, %2\n\t"
                     "v_cmp_lt_i32_e64 %0, (%c14&65535), %4\n\ts_bcnt1_i32_b64 %2, %0\n\ts_mov_b32 %3, %c14\n\ts_cmp_lt_u32 %1, %2\n\ts_cbranch_scc1 1f\n\ts_sub_u32 %1, %1, %2\n\t"
                     "v_cmp_lt_i32_e64 %0, (%c15&65535), %4\n\ts_bcnt1_i32_b64 %2, %0\n\ts_mov_b32 %3, %c15\n\ts_cmp_lt_u32 %1, %2\n\ts_cbranch_scc1 1f\n\ts_sub_u32 %1, %1, %2\n\t"
                     "v_cmp_lt_i32_e64 %0, (%c16&65535), %4\n\ts_bcnt1_i32_b64 %2, %0\n\ts_mov_b32 %3, %c16\n\ts_cmp_lt_u32 %1, %2\n\ts_cbranch_scc1 1f\n\ts_sub_u32 %1, %1, %2\n\t"
                     "v_cmp_lt_i32_e64 %0, (%c17&65535), %4\n\ts_bcnt1_i32_b64 %2, %0\n\ts_mov_b32 %3, %c17\n\ts_cmp_lt_u32 %1, %2\n\ts_cbranch_scc1 1f\n\ts_sub_u32 %1, %1, %2\n\t"
                     "v_cmp_lt_i32_e64 %0, (%c18&65535), %4\n\ts_bcnt1_i32_b64 %2, %0\n\ts_mov_b32 %3, %c18\n\ts_cmp_lt_u32 %1, %2\n\ts_cbranch_scc1 1f\n\ts_sub_u32 %1, %1, %2\n\t"
                     "v_cmp_lt_i32_e64 %0, (%c19&65535), %4\n\ts_bcnt1_i32_b64 %2, %0\n\ts_mov_b32 %3, %c19\n\ts_cmp_lt_u32 %1, %2\n\ts_cbranch_scc1 1f\n\ts_sub_u32 %1, %1, %2\n\t"
                     "v_cmp_lt_i32_e64 %0, (%c20&65535), %4\n\ts_bcnt1_i32_b64 %2, %0\n\ts_mov_b32 %3, %c20\n\ts_cmp_lt_u32 %1, %2\n\ts_cbranch_scc1 1f\n\ts_sub_u32 %1, %1, %2\n\t"
                     "s_mov_b32 %3, -1\n1:"
                     : "=&s"(msk), "+s"(r), "=&s"(cnt), "=&s"(code) : "v"(n), "i"(level(0)), "i"(level(1)), "i"(level(2)), "i"(level(3)), "i"(level(4)), "i"(level(5)), "i"(level(6)), "i"(level(7)), "i"(level(8)), "i"(level(9)), "i"(level(10)), "i"(level(11)), "i"(level(12)), "i"(level(13)), "i"(level(14)), "i"(level(15)) : "scc");
    }
found:
    if (__builtin_expect(code < 0, 0)) { asm volatile("s_waitcnt vmcnt(0)" ::: "memory"); return; }
    const int tb = code >> 16, idx = (code & 0xffff) >> (1 - tb);
    const unsigned below = __builtin_amdgcn_mbcnt_hi((unsigned)(msk >> 32), __builtin_amdgcn_mbcnt_lo((unsigned)msk, 0u));
    const unsigned long long hit = __ballot(below == (unsigned)r) & msk;
    const int sl = __builtin_ctzll(hit);
    const int s0 = min(max(__builtin_amdgcn_readlane(a, sl), 0), T), L = min(__builtin_amdgcn_readlane(b, sl), T) - s0;
    if (__builtin_expect(L <= 0, 0)) { asm volatile("s_waitcnt vmcnt(0)" ::: "memory"); return; }
    if (DELAY2 > 0 && j >= 32) __builtin_amdgcn_s_sleep(DELAY2);
    STAMP(1);
    if (tb) attn_item<true>(st, Q, K, V, O, kvh, s0, L, idx, lds, wv, la);
    else attn_item<false>(st, Q, K, V, O, kvh, s0, L, idx, lds, wv, la);
}

extern "C" void kernel_launch(void* const* d_in, const int* in_sizes, int n_in, void* d_out, int out_size, void* d_ws, size_t ws_size, hipStream_t stream) {
    const int T = in_sizes[0] / (HQ * D), nseq = in_sizes[3] - 1;
    int slots = (T + 31) / 32 + nseq;
    if (slots < 64) slots = 64;
    (void)hipFuncSetAttribute((const void*)attn_v10, hipFuncAttributeMaxDynamicSharedMemorySize, LDS_BYTES);
    attn_v10<<<slots * 8, 256, LDS_BYTES, stream>>>((const unsigned short*)d_in[0], (const unsigned short*)d_in[1], (const unsigned short*)d_in[2],
                                                 (const int*)d_in[3], (unsigned short*)d_out, nseq, T, (unsigned long long*)d_ws);
}
```

```cpp
#include <hip/hip_runtime.h>
#include <hip/hip_bf16.h>
#include <stdint.h>

typedef short bf16x8 __attribute__((ext_vector_type(8)));
typedef short s16x4 __attribute__((ext_vector_type(4)));
typedef float f32x16 __attribute__((ext_vector_type(16)));
typedef float f32x4 __attribute__((ext_vector_type(4)));
typedef float f32x2 __attribute__((ext_vector_type(2)));
typedef unsigned u32x4 __attribute__((ext_vector_type(4)));
typedef unsigned u32x2 __attribute__((ext_vector_type(2)));
typedef int i32x16 __attribute__((ext_vector_type(16)));
typedef __attribute__((address_space(3))) const char* lds_cptr;
typedef __attribute__((address_space(3))) char* lds_ptr;
typedef short v4i16_t __attribute__((ext_vector_type(4)));

constexpr int HQ = 16, HKV = 8, D = 192;
constexpr unsigned QROW = HQ * D * 2, KROW = HKV * D * 2;
constexpr unsigned KBLK = 32 * KROW;
constexpr int BLK = 12288;
constexpr int LDS_MISC = 6 * BLK;
constexpr int LDS_BYTES = LDS_MISC + 2048;
constexpr float C2 = 0.07216878364870322f * 1.4426950408889634f;
constexpr float LBIG = 1073741824.f;
#define TH 8
#define PFKV 0
#define PFFIRST 0
#define PFB 1
#define DELAY2 0
#define PFQ 0
#define WA 10
#define WB 11
constexpr unsigned LDS_NOWHERE = 0x30000u;
constexpr unsigned OOB = 0x80000000u;

#define DI __device__ __forceinline__
#define MFMA(a, b, c) __builtin_amdgcn_mfma_f32_32x32x16_bf16(a, b, c, 0, 0, 0)
#define SBAR() __builtin_amdgcn_sched_barrier(0)
#define PIN(x) asm volatile("" : "+v"(x))
typedef __bf16 bf16x2_t __attribute__((ext_vector_type(2)));
DI unsigned cvtpk(float lo, float hi) { const f32x2 v = {lo, hi}; return __builtin_bit_cast(unsigned, __builtin_convertvector(v, bf16x2_t)); }
DI float max3f(float a, float b, float c) { float r; asm("v_max3_f32 %0, %1, %2, %3" : "=v"(r) : "v"(a), "v"(b), "v"(c)); return r; }
DI float max2f(float a, float b) { float r; asm("v_max_f32_e32 %0, %1, %2" : "=v"(r) : "v"(a), "v"(b)); return r; }
DI float fmul_s(float a, float b) { float r; asm("v_mul_f32_e32 %0, %1, %2" : "=v"(r) : "v"(a), "v"(b)); return r; }
DI float fma_v(float a, float b, float c) { float r; asm("v_fma_f32 %0, %1, %2, %3" : "=v"(r) : "v"(a), "v"(b), "v"(c)); return r; }
DI void vexp(float& x) { asm volatile("v_exp_f32_e32 %0, %0" : "+v"(x)); }
DI float vadd(float a, float b) { float r; asm volatile("v_add_f32_e32 %0, %1, %2" : "=v"(r) : "v"(a), "v"(b)); return r; }
DI float vfma(float a, float b, float c) { float r; asm volatile("v_fma_f32 %0, %1, %2, %3" : "=v"(r) : "v"(a), "s"(b), "v"(c)); return r; }
DI float vmul(float a, float b) { float r; asm volatile("v_mul_f32_e32 %0, %1, %2" : "=v"(r) : "v"(a), "v"(b)); return r; }
DI float vfma_v(float a, float b, float c) { float r; asm volatile("v_fma_f32 %0, %1, %2, %3" : "=v"(r) : "v"(a), "v"(b), "v"(c)); return r; }
DI unsigned vcvt(float lo, float hi) { unsigned r; asm volatile("v_cvt_pk_bf16_f32 %0, %1, %2" : "=v"(r) : "v"(lo), "v"(hi)); return r; }
DI s16x4 vtr(lds_cptr p) { return __builtin_bit_cast(s16x4, __builtin_amdgcn_ds_read_tr16_b64_v4i16((__attribute__((address_space(3))) v4i16_t*)p)); }
DI bf16x8 ldk(lds_cptr p) { return *(const __attribute__((address_space(3))) bf16x8*)p; }
DI int rfl(int x) { return __builtin_amdgcn_readfirstlane(x); }
DI float swapmax(float x) { auto r = __builtin_amdgcn_permlane32_swap(__float_as_uint(x), __float_as_uint(x), false, false); return max2f(__uint_as_float(r[0]), __uint_as_float(r[1])); }
DI float swapsum(float x) { auto r = __builtin_amdgcn_permlane32_swap(__float_as_uint(x), __float_as_uint(x), false, false); return __uint_as_float(r[0]) + __uint_as_float(r[1]); }

template <int S, int OFF> DI void dma(unsigned mb, unsigned voff, __amdgpu_buffer_rsrc_t srd, unsigned soff) {
    asm volatile("s_add_u32 m0, %0, %5\n\ts_nop 0\n\tbuffer_load_dwordx4 %1, %2, %3 offen offset:%4 lds"
                 :: "s"(mb), "v"(voff), "s"(srd), "s"(soff), "i"(128 * S), "i"(OFF + S * (4096 - 128)) : "memory", "scc");
}
DI void dma3(unsigned mb, unsigned voff, __amdgpu_buffer_rsrc_t srd, unsigned soff) { dma<0, 0>(mb, voff, srd, soff); dma<1, 0>(mb, voff, srd, soff); dma<2, 0>(mb, voff, srd, soff); }
#define DMAL dma
#define LOOPBAR() BARRIER()
#define LOOPWAIT() WAIT_VM(6)
#define WAIT_VM(n) asm volatile("s_waitcnt vmcnt(" #n ")" ::: "memory")
#define BARRIER() asm volatile("s_waitcnt lgkmcnt(0)\n\ts_barrier" ::: "memory")
#define OPIN(nop) asm volatile(nop : "+v"(o[0]), "+v"(o[1]), "+v"(o[2]), "+v"(o[3]), "+v"(o[4]), "+v"(o[5]))

#define STAMP(i)
#define STAMPC(i)

struct LaneAddr { unsigned vK, vV, vQ0, vQ1; };

template <bool TB>
DI void attn_item(unsigned long long* st, const unsigned short* __restrict__ Q, const unsigned short* __restrict__ K, const unsigned short* __restrict__ V,
                  unsigned short* __restrict__ O, int kvh, int s0, int L, int idx, char* lds, int wid, const LaneAddr& la) {
    const int tid = threadIdx.x, lane = tid & 63, l32 = lane & 31, hi = lane >> 5;
    const int hsel = wid & 1, sub = wid >> 1;
    const int n = (L + 31) >> 5;
    const int q0 = TB ? 32 * idx : 64 * idx;
    const int NB = TB ? idx + 1 : min(2 * idx + 2, n);
    const int NU = TB ? (NB + 1) >> 1 : NB;
    const int nu = TB ? (NB + 1 - sub) >> 1 : (2 * idx + sub < n ? 2 * idx + sub + 1 : 0);
    const int diagu = TB ? (((idx & 1) == sub) ? nu - 1 : -1) : nu - 1;

    const unsigned krec = (unsigned)min(L, 32 * NB) * KROW - kvh * 384;
    const __amdgpu_buffer_rsrc_t srdK = __builtin_amdgcn_make_buffer_rsrc((void*)((const char*)K + (size_t)s0 * KROW + kvh * 384), (short)0, (int)krec, 0x00020000);
    const __amdgpu_buffer_rsrc_t srdQ = __builtin_amdgcn_make_buffer_rsrc((void*)((const char*)Q + (size_t)s0 * QROW + kvh * 768), (short)0, (int)((unsigned)L * QROW - kvh * 768), 0x00020000);
    const unsigned ldsb = (unsigned)(uintptr_t)lds;
    const unsigned wb = ldsb + wid * 1024;
    const unsigned vK = la.vK, vV = la.vV;

    dma3(wb, vK, srdK, 0);
    if (TB) dma3(wb + BLK, vK, srdK, KBLK);
    {
        const unsigned qreg = TB ? 4 * BLK + hsel * BLK : (wid == 3 ? 2 * BLK : (3 + wid) * BLK);
        const unsigned qs = (unsigned)(q0 + (TB ? 16 * sub : 32 * sub)) * QROW + hsel * 384;
        const unsigned m = ldsb + qreg + (TB ? 2048 * sub : 0);
#pragma unroll
        for (int g = 0; g < (TB ? 2 : 4); ++g) dma3(m + g * 1024, (g & 1) ? la.vQ1 : la.vQ0, srdQ, qs + g * 8 * QROW);
    }
    SBAR();
    const __amdgpu_buffer_rsrc_t srdV = __builtin_amdgcn_make_buffer_rsrc((void*)((const char*)V + (size_t)s0 * KROW + kvh * 384), (short)0, (int)krec, 0x00020000);
    if (PFKV) {
        const int per = TB ? 8 : 16, tk = (lane * 43) >> 7;
        const unsigned vp = tk < per ? (unsigned)(q0 + wid * per + tk) * KROW + (lane - 3 * tk) * 128 : OOB;
        const unsigned pm = ldsb + LDS_MISC + wid * 256;
        asm volatile("s_mov_b32 m0, %0\n\ts_nop 0\n\tbuffer_load_dword %1, %2, 0 offen lds" :: "s"(pm), "v"(vp), "s"(srdK) : "memory");
        asm volatile("s_mov_b32 m0, %0\n\ts_nop 0\n\tbuffer_load_dword %1, %2, 0 offen lds" :: "s"(pm), "v"(vp), "s"(srdV) : "memory");
    }
    const int qrel = q0 + (TB ? 0 : 32 * sub) + l32;
    const lds_cptr lp = (lds_cptr)lds;
    lds_cptr kb[2], vb, vbo;
#pragma unroll
    for (int i = 0; i < 2; ++i) kb[i] = lp + (l32 >> 3) * 1024 + (l32 & 7) * 64 + 16 * ((2 * i + hi) ^ ((l32 >> 2) & 3));
    vb = lp + (4 * hi + ((lane & 15) >> 2)) * 64 + 32 * ((lane >> 4) & 1) + 8 * (lane & 3);
#define KADDR(ks) (kb[(ks) & 1] + ((ks) >> 2) * 4096 + (((ks) >> 1) & 1) * 512)
    f32x16 o[6];
#pragma unroll
    for (int i = 0; i < 6; ++i) o[i] = f32x16{};
    OPIN("");

    STAMP(2);
    bf16x8 qf[12];
    if (PFKV) { WAIT_VM(2); } else { WAIT_VM(0); }
    if (TB) BARRIER();
    {
        const int qo = TB ? 4 * BLK + hsel * BLK : (wid == 3 ? 2 * BLK : (3 + wid) * BLK);
#pragma unroll
        for (int ks = 0; ks < 12; ++ks) {
            qf[ks] = ldk(KADDR(ks) + qo);
            if (TB) {
                if (ks == 1) dma<0, 0>(wb + 2 * BLK, vK, srdK, 2 * KBLK); if (ks == 3) dma<1, 0>(wb + 2 * BLK, vK, srdK, 2 * KBLK); if (ks == 5) dma<2, 0>(wb + 2 * BLK, vK, srdK, 2 * KBLK);
                if (ks == 7) dma<0, 0>(wb + 3 * BLK, vK, srdK, 3 * KBLK); if (ks == 9) dma<1, 0>(wb + 3 * BLK, vK, srdK, 3 * KBLK); if (ks == 11) dma<2, 0>(wb + 3 * BLK, vK, srdK, 3 * KBLK);
            } else {
                if (ks == 3) dma<0, 0>(wb + BLK, vK, srdK, KBLK); if (ks == 7) dma<1, 0>(wb + BLK, vK, srdK, KBLK); if (ks == 11) dma<2, 0>(wb + BLK, vK, srdK, KBLK);
            }
        }
    }
    BARRIER();
    STAMP(3);
    kb[0] += TB ? sub * BLK : 0; kb[1] += TB ? sub * BLK : 0;
    vb += TB ? sub * BLK : 3 * BLK;
    vbo = vb + (TB ? 512 - sub * 512 : 512);
    vb += TB ? sub * 512 : 0;

    f32x16 S, X;
    float m = 0.f, l = 0.f;
    auto mask = [&](int kblock) {
        const int d = qrel - kblock * 32 - 4 * hi;
        const float ninf = -__builtin_inff();
        unsigned long long k[16];
#define MK(r) asm volatile("v_cmp_le_i32_e64 %0, %2, %1" : "=s"(k[r]) : "v"(d), "i"(((r) & 3) + 8 * ((r) >> 2)))
#define MS(r) do { float t_ = S[r]; asm volatile("v_cndmask_b32_e64 %0, %1, %0, %2" : "+v"(t_) : "v"(ninf), "s"(k[r])); S[r] = t_; } while (0)
        MK(0); MK(1); MK(2); MK(3); MK(4); MK(5); MK(6); MK(7); MK(8); MK(9); MK(10); MK(11); MK(12); MK(13); MK(14); MK(15);
        MS(0); MS(1); MS(2); MS(3); MS(4); MS(5); MS(6); MS(7); MS(8); MS(9); MS(10); MS(11); MS(12); MS(13); MS(14); MS(15);
#undef MK
#undef MS
    };
    auto rowmax = [&]() -> float {
        const float a = max3f(S[0], S[1], S[2]), b = max3f(S[3], S[4], S[5]), c = max3f(S[6], S[7], S[8]), d = max3f(S[9], S[10], S[11]), e = max3f(S[12], S[13], S[14]);
        return swapmax(max2f(max3f(a, b, S[15]), max3f(c, d, e)));
    };
    const unsigned vK1 = vK + KBLK, vV1 = vV + KBLK;
    u32x4 p0, p1;
    bf16x8 kf[6];
    s16x4 vlo[6], vhi[6];
#define EXP(e) do { float t_ = X[e]; vexp(t_); X[e] = t_; } while (0)
#define FM(e) X[e] = vfma(S[e], C2, nm)
#define KRD(k) do { kf[(k) % 6] = ldk(KADDR(k)); SBAR(); } while (0)
#define VRD(k) do { const lds_cptr vp_ = ((k) & 1 ? vbo : vb) + (((k) % 6) >> 1) * 4096 + ((k) / 6) * 2048; vlo[(k) % 6] = vtr(vp_); vhi[(k) % 6] = vtr(vp_ + 1024); SBAR(); } while (0)
#define VFR(k) ((bf16x8){vlo[(k) % 6][0], vlo[(k) % 6][1], vlo[(k) % 6][2], vlo[(k) % 6][3], vhi[(k) % 6][0], vhi[(k) % 6][1], vhi[(k) % 6][2], vhi[(k) % 6][3]})
#define QK(k) do { S = MFMA(kf[(k) % 6], qf[k], (k) == 0 ? f32x16{} : S); PIN(S); SBAR(); } while (0)
#define PV(k) do { o[(k) % 6] = MFMA(VFR(k), __builtin_bit_cast(bf16x8, (k) < 6 ? p0 : p1), o[(k) % 6]); PIN(o[(k) % 6]); SBAR(); } while (0)
#define ADD0(e) ps0 = vadd(ps0, X[e])
#define ADD1(e) ps1 = vadd(ps1, X[e])
#define CV(P, i, e) P[i] = vcvt(X[e], X[(e) + 1])

    int r0 = 0, r1 = TB ? 2 * BLK : BLK, r2 = TB ? 4 * BLK : 2 * BLK;
    const unsigned mV1 = (TB || 1 < NB) ? wb + (TB ? 5 : 4) * BLK : LDS_NOWHERE, mK2 = 2 < NB ? wb + 2 * BLK : LDS_NOWHERE;
    if (nu > 0) {
        KRD(1); KRD(0); KRD(3); KRD(2); KRD(5); KRD(4);
        if (TB) {
            QK(0); dma<0, 0>(wb + 4 * BLK, vV, srdV, 0); SBAR();
            QK(1); KRD(7); SBAR();
            QK(2); KRD(6); dma<1, 0>(wb + 4 * BLK, vV, srdV, 0); SBAR();
            QK(3); KRD(9); SBAR();
            QK(4); KRD(8); dma<2, 0>(wb + 4 * BLK, vV, srdV, 0); SBAR();
            QK(5); KRD(11); SBAR();
            QK(6); KRD(10); dma<0, 0>(mV1, vV, srdV, KBLK); SBAR();
            QK(7);
            QK(8); dma<1, 0>(mV1, vV, srdV, KBLK); SBAR();
            QK(9);
            QK(10); dma<2, 0>(mV1, vV, srdV, KBLK); SBAR();
            QK(11);
        } else {
            QK(0); dma<0, 0>(wb + 3 * BLK, vV, srdV, 0); SBAR();
            QK(1); KRD(7); dma<1, 0>(wb + 3 * BLK, vV, srdV, 0); SBAR();
            QK(2); KRD(6); dma<2, 0>(wb + 3 * BLK, vV, srdV, 0); SBAR();
            QK(3); KRD(9); SBAR();
            QK(4); KRD(8); dma<0, 0>(mV1, vV, srdV, KBLK); SBAR();
            QK(5); KRD(11); dma<1, 0>(mV1, vV, srdV, KBLK); SBAR();
            QK(6); KRD(10); dma<2, 0>(mV1, vV, srdV, KBLK); SBAR();
            QK(7);
            QK(8); dma<0, 0>(mK2, vK, srdK, 2 * KBLK); SBAR();
            QK(9); dma<1, 0>(mK2, vK, srdK, 2 * KBLK); SBAR();
            QK(10); dma<2, 0>(mK2, vK, srdK, 2 * KBLK); SBAR();
            QK(11);
        }
    } else {
        dma3(wb + 3 * BLK, vV, srdV, 0); dma3(mV1, vV, srdV, KBLK); dma3(mK2, vK, srdK, 2 * KBLK);
    }
    WAIT_VM(6); BARRIER();
    kb[0] += r1; kb[1] += r1; if (TB) { vb += r2; vbo += r2; }
    if (nu > 1) { KRD(1); KRD(0); KRD(3); KRD(2); KRD(5); KRD(4); }
    if (TB) { dma<0, 0>(wb + r0, vK, srdK, 4 * KBLK); dma<1, 0>(wb + r0, vK, srdK, 4 * KBLK); }
    if (nu > 0) {
        asm volatile("s_nop 11" : "+v"(S));
        if (diagu == 0) mask(TB ? sub : 0);
        m = rowmax() * C2;
        const float nm = -m;
        FM(0); FM(1); FM(2); FM(3); FM(4); FM(5); FM(6); FM(7); FM(8); FM(9); FM(10); FM(11); FM(12); FM(13); FM(14); FM(15);
        EXP(0); EXP(1); EXP(2); EXP(3);
    } else X = f32x16{};
    const float nm = -m;

#define ROTATE() do { const int t_ = TB ? r2 : r0; if (TB) { r2 = r1; r1 = r0; r0 = t_; } else { r0 = r1; r1 = r2; r2 = t_; } so += (TB ? 2 : 1) * KBLK; ++u; } while (0)
#define BK_WAIT() LOOPWAIT()
    STAMP(4); STAMPC(8);
    unsigned barw = 0, barn = 0; (void)barw; (void)barn;
    int u = 0;
    unsigned so = 0;
    while (u + 1 < nu) {
        float ps0, ps1;
        const unsigned soK = so + (TB ? 4 : 3) * KBLK, soV = so + 2 * KBLK;
        const unsigned mK = (TB || u + 3 < NU) ? wb + r0 : LDS_NOWHERE, mV = TB ? wb + r1 : (u + 2 < NU ? wb + 3 * BLK + r2 : LDS_NOWHERE);
        const int dk = TB ? r0 - r1 : r2 - r1, dv = TB ? r1 - r2 : r1 - r0;
        if (TB) {
            QK(0);  EXP(4); EXP(5); ps0 = vadd(X[0], X[1]); SBAR();
            QK(1);  KRD(7); DMAL<2, 0>(mK, vK, srdK, soK); EXP(6); SBAR();
            QK(2);  KRD(6); EXP(7); EXP(8); ps1 = vadd(X[2], X[3]); SBAR();
            QK(3);  KRD(9); DMAL<0, BLK>(mK, vK1, srdK, soK); EXP(9); SBAR();
            QK(4);  KRD(8); EXP(10); CV(p0, 0, 0); ADD0(4); ADD1(5); SBAR();
            QK(5);  KRD(11); DMAL<1, BLK>(mK, vK1, srdK, soK); EXP(11); SBAR();
            QK(6);  KRD(10); EXP(12); CV(p0, 1, 2); CV(p0, 2, 4); SBAR();
            QK(7);  DMAL<2, BLK>(mK, vK1, srdK, soK); EXP(13); ADD0(6); SBAR();
            QK(8);
            LOOPWAIT(); LOOPBAR();
            VRD(1); VRD(0); VRD(3); VRD(2);
            QK(9);  DMAL<0, 0>(mV, vV, srdV, soV); VRD(5); EXP(14); SBAR();
            QK(10); VRD(4); EXP(15); CV(p0, 3, 6); ADD1(7); ADD0(8); SBAR();
            QK(11); DMAL<1, 0>(mV, vV, srdV, soV); ADD1(9); CV(p1, 0, 8); ADD0(10); SBAR();
            PV(0);  ADD1(11); CV(p1, 1, 10); ADD0(12); ADD1(13); CV(p1, 2, 12); SBAR();
            PV(1);  VRD(7); DMAL<2, 0>(mV, vV, srdV, soV); ADD0(14); SBAR();
            if (__builtin_expect(u + 1 == diagu, 0)) { asm volatile("s_nop 7" : "+v"(S)); mask(2 * u + 2 + sub); }
            PV(2);  VRD(6); ADD1(15); CV(p1, 3, 14); FM(0); FM(1); SBAR();
            PV(3);  VRD(9); DMAL<0, BLK>(mV, vV1, srdV, soV); FM(2); SBAR();
            PV(4);  VRD(8); FM(3); FM(4); FM(5); ps0 = vadd(ps0, ps1); SBAR();
            PV(5);  VRD(11); DMAL<1, BLK>(mV, vV1, srdV, soV); FM(6); SBAR();
            PV(6);  VRD(10); FM(7); FM(8); FM(9); l = vadd(l, ps0); SBAR();
            PV(7);  DMAL<2, BLK>(mV, vV1, srdV, soV); FM(10); FM(11); kb[0] += dk; kb[1] += dk; PIN(kb[0]); PIN(kb[1]); SBAR();
            PV(8);
        } else {
            QK(0);  EXP(4); EXP(5); ps0 = vadd(X[0], X[1]); SBAR();
            QK(1);  KRD(7); EXP(6); EXP(7); ps1 = vadd(X[2], X[3]); SBAR();
            QK(2);  KRD(6); EXP(8); CV(p0, 0, 0); ADD0(4); ADD1(5); SBAR();
            QK(3);  KRD(9); DMAL<0, 0>(mK, vK, srdK, soK); EXP(9); SBAR();
            QK(4);  KRD(8); EXP(10); CV(p0, 1, 2); CV(p0, 2, 4); SBAR();
            QK(5);  KRD(11); DMAL<1, 0>(mK, vK, srdK, soK); EXP(11); SBAR();
            QK(6);  KRD(10); EXP(12); ADD0(6); CV(p0, 3, 6); ADD1(7); SBAR();
            QK(7);  VRD(1); DMAL<2, 0>(mK, vK, srdK, soK); EXP(13); SBAR();
            QK(8);  VRD(0); EXP(14); ADD0(8); ADD1(9); SBAR();
            QK(9);  VRD(3); EXP(15); CV(p1, 0, 8); SBAR();
            QK(10); VRD(2); ADD0(10); ADD1(11); CV(p1, 1, 10); SBAR();
            QK(11); VRD(5); ADD0(12); ADD1(13); CV(p1, 2, 12); SBAR();
            PV(0);  VRD(4); DMAL<0, 0>(mV, vV, srdV, soV); ADD0(14); SBAR();
            PV(1);  VRD(7); ADD1(15); CV(p1, 3, 14); SBAR();
            if (__builtin_expect(u + 1 == diagu, 0)) { asm volatile("s_nop 7" : "+v"(S)); mask(u + 1); }
            PV(2);  VRD(6); DMAL<1, 0>(mV, vV, srdV, soV); FM(0); SBAR();
            PV(3);  VRD(9); FM(1); FM(2); FM(3); ps0 = vadd(ps0, ps1); SBAR();
            PV(4);  VRD(8); DMAL<2, 0>(mV, vV, srdV, soV); FM(4); SBAR();
            PV(5);  VRD(11); FM(5); FM(6); FM(7); l = vadd(l, ps0); SBAR();
            PV(6);  VRD(10); FM(8); FM(9); FM(10); SBAR();
            PV(7);  FM(11); FM(12); FM(13); kb[0] += dk; kb[1] += dk; PIN(kb[0]); PIN(kb[1]); SBAR();
            PV(8);
        }
        BK_WAIT(); LOOPBAR();
        KRD(1); KRD(0); KRD(3); KRD(2);
        if (TB) {
            PV(9);  DMAL<0, 0>(wb + r2, vK, srdK, soK + 2 * KBLK); KRD(5); FM(12); FM(13); SBAR();
            PV(10); KRD(4); FM(14); FM(15); EXP(0); vb += dv; vbo += dv; PIN(vb); PIN(vbo); SBAR();
            PV(11); DMAL<1, 0>(wb + r2, vK, srdK, soK + 2 * KBLK); EXP(1); EXP(2); EXP(3); SBAR();
        } else {
            PV(9);  KRD(5); FM(14); FM(15); EXP(0); SBAR();
            PV(10); KRD(4); EXP(1); EXP(2); vb += dv; vbo += dv; PIN(vb); PIN(vbo); SBAR();
            PV(11); EXP(3); SBAR();
        }
        ROTATE();
    }
    STAMPC(9);
#define TAIL_HEAD() \
        const bool last = u == NU - 1; \
        if (!last) { \
            if (TB) { dma<2, 0>(wb + r0, vK, srdK, so + 4 * KBLK); dma3(wb + r0 + BLK, vK1, srdK, so + 4 * KBLK); } \
            else { dma3(u + 3 < NU ? wb + r0 : LDS_NOWHERE, vK, srdK, so + 3 * KBLK); dma3(u + 2 < NU ? wb + 3 * BLK + r2 : LDS_NOWHERE, vV, srdV, so + 2 * KBLK); } \
        }
#define TAIL_MID() \
        if (TB) { if (last) { WAIT_VM(0); } else { WAIT_VM(6); } BARRIER(); }
#define TAIL_END() \
        if (!last) { BK_WAIT(); BARRIER(); if (TB) { dma<0, 0>(wb + r2, vK, srdK, so + 6 * KBLK); dma<1, 0>(wb + r2, vK, srdK, so + 6 * KBLK); } }
    if (u < nu) {
        TAIL_HEAD()
#pragma unroll
        for (int e = 4; e < 8; ++e) EXP(e);
        float ps0 = vadd(X[0], X[1]), ps1 = vadd(X[2], X[3]);
        CV(p0, 0, 0); CV(p0, 1, 2); EXP(8); EXP(9); CV(p0, 2, 4); CV(p0, 3, 6);
        TAIL_MID()
        VRD(1); VRD(0); VRD(3); VRD(2); VRD(5); VRD(4);
        EXP(10); EXP(11); EXP(12); EXP(13); SBAR();
        const unsigned mVt = last ? LDS_NOWHERE : wb + r1;
        PV(0);  if (TB) dma<0, 0>(mVt, vV, srdV, so + 2 * KBLK); EXP(14); EXP(15); SBAR();
        PV(1);  VRD(7); ADD0(4); ADD1(5); SBAR();
        PV(2);  VRD(6); if (TB) dma<1, 0>(mVt, vV, srdV, so + 2 * KBLK); CV(p1, 0, 8); CV(p1, 1, 10); SBAR();
        PV(3);  VRD(9); CV(p1, 2, 12); CV(p1, 3, 14); SBAR();
        PV(4);  VRD(8); if (TB) dma<2, 0>(mVt, vV, srdV, so + 2 * KBLK); ADD0(6); ADD1(7); SBAR();
        PV(5);  VRD(11); ADD0(8); ADD1(9); ADD0(10); SBAR();
        PV(6);  VRD(10); if (TB) dma<0, BLK>(mVt, vV1, srdV, so + 2 * KBLK); ADD1(11); ADD0(12); SBAR();
        PV(7);  if (TB) dma<1, BLK>(mVt, vV1, srdV, so + 2 * KBLK); ADD1(13); SBAR();
        PV(8);  if (TB) dma<2, BLK>(mVt, vV1, srdV, so + 2 * KBLK); ADD0(14); ADD1(15); SBAR();
        PV(9); PV(10); PV(11);
        l += ps0 + ps1;
        TAIL_END()
        ROTATE();
    }
    while (u < NU) {
        TAIL_HEAD()
        TAIL_MID()
        if (TB && !last) { dma3(wb + r1, vV, srdV, so + 2 * KBLK); dma3(wb + r1 + BLK, vV1, srdV, so + 2 * KBLK); }
        TAIL_END()
        ROTATE();
    }
#undef TAIL_HEAD
#undef TAIL_MID
#undef TAIL_END
    OPIN("s_nop 15");
    int lane2;
    asm volatile("v_mbcnt_lo_u32_b32 %0, -1, 0\n\tv_mbcnt_hi_u32_b32 %0, -1, %0" : "=v"(lane2));

    const int l32e = lane2 & 31, hie = lane2 >> 5, r8e = lane2 >> 3, c8e = lane2 & 7, r16e = lane2 >> 4;
    STAMP(5);
    const int reg = TB ? (sub ? r2 : r1) + hsel * BLK : (wid < 3 ? wid * BLK : 3 * BLK + r0);
    l = swapsum(l);
    if (__builtin_expect(__any(!(l < LBIG)), 0)) {
        const unsigned mb = ldsb + reg;
        const int dr2 = (lane2 >> 2) & 7, dh2 = lane2 >> 5, dc2 = lane2 & 3, dg2 = (lane2 >> 4) & 1;
        const unsigned vK0 = dr2 * KROW + 64u * dh2 + 16u * (dc2 ^ dg2), vK1_ = dr2 * KROW + 64u * dh2 + 16u * (dc2 ^ (2 + dg2)), vV0 = dr2 * KROW + 64u * dh2 + 16u * dc2;
        kb[0] = lp + reg + (l32e >> 3) * 1024 + (l32e & 7) * 64 + 16 * (hie ^ ((l32e >> 2) & 3));
        kb[1] = lp + reg + (l32e >> 3) * 1024 + (l32e & 7) * 64 + 16 * ((2 + hie) ^ ((l32e >> 2) & 3));
        vb = lp + reg + (4 * hie + ((lane2 & 15) >> 2)) * 64 + 32 * ((lane2 >> 4) & 1) + 8 * (lane2 & 3);
        vbo = vb + (TB ? 512 - sub * 512 : 512); vb += TB ? sub * 512 : 0;
        bf16x8 q2[12];
        {
            const unsigned vQ0 = dr2 * QROW + 64u * dh2 + 16u * (dc2 ^ dg2), vQ1 = dr2 * QROW + 64u * dh2 + 16u * (dc2 ^ (2 + dg2));
            const unsigned qs = (unsigned)(q0 + (TB ? 0 : 32 * sub)) * QROW + hsel * 384;
#pragma unroll
            for (int g = 0; g < 4; ++g) dma3(mb + g * 1024, (g & 1) ? vQ1 : vQ0, srdQ, qs + g * 8 * QROW);
            WAIT_VM(0);
#pragma unroll
            for (int ks = 0; ks < 12; ++ks) q2[ks] = ldk(KADDR(ks));
        }
        auto scores = [&](int t) {
            const int b = TB ? 2 * t + sub : t;
            asm volatile("s_waitcnt lgkmcnt(0)" ::: "memory");
#pragma unroll
            for (int g = 0; g < 4; ++g) dma3(mb + g * 1024, (g & 1) ? vK1_ : vK0, srdK, b * KBLK + g * 8 * KROW);
            WAIT_VM(0);
            S = MFMA(ldk(KADDR(0)), q2[0], f32x16{});
#pragma unroll
            for (int ks = 1; ks < 12; ++ks) S = MFMA(ldk(KADDR(ks)), q2[ks], S);
            if (t == diagu) {
                const int k0 = b * 32 + 4 * hie - (q0 + (TB ? 0 : 32 * sub) + l32e);
#pragma unroll
                for (int r = 0; r < 16; ++r) if (k0 + (r & 3) + 8 * (r >> 2) > 0) S[r] = -__builtin_inff();
            }
        };
        float mx = -__builtin_inff();
#pragma unroll 1
        for (int t = 0; t < nu; ++t) { scores(t); asm volatile("s_nop 15" : "+v"(S)); mx = max2f(mx, rowmax()); }
        m = mx * C2;
#pragma unroll
        for (int i = 0; i < 6; ++i) o[i] = f32x16{};
        l = 0.f;
#pragma unroll 1
        for (int t = 0; t < nu; ++t) {
            scores(t);
            const int b = TB ? 2 * t + sub : t;
            asm volatile("s_waitcnt lgkmcnt(0)" ::: "memory");
#pragma unroll
            for (int g = 0; g < 4; ++g) dma3(mb + g * 1024, vV0, srdV, b * KBLK + g * 8 * KROW);
            float ps = 0.f;
#pragma unroll
            for (int e = 0; e < 16; ++e) { X[e] = __builtin_amdgcn_exp2f(__builtin_fmaf(S[e], C2, -m)); ps += X[e]; }
            l += ps;
            p0 = (u32x4){cvtpk(X[0], X[1]), cvtpk(X[2], X[3]), cvtpk(X[4], X[5]), cvtpk(X[6], X[7])};
            p1 = (u32x4){cvtpk(X[8], X[9]), cvtpk(X[10], X[11]), cvtpk(X[12], X[13]), cvtpk(X[14], X[15])};
            WAIT_VM(0);
            #pragma unroll
            for (int k = 0; k < 12; ++k) { VRD(k); o[k % 6] = MFMA(VFR(k), __builtin_bit_cast(bf16x8, k < 6 ? p0 : p1), o[k % 6]); }
        }
        l = swapsum(l);
        asm volatile("s_waitcnt lgkmcnt(0)" ::: "memory");
    }

    const __amdgpu_buffer_rsrc_t srdO = __builtin_amdgcn_make_buffer_rsrc((void*)((char*)O + (size_t)s0 * QROW + kvh * 768), (short)0, (int)((unsigned)L * QROW - kvh * 768), 0x00020000);
    const lds_ptr lw = (lds_ptr)lds;
    const unsigned vO = r8e * QROW + c8e * 16;
#define STG(base, db, g, lo, hi_) *(__attribute__((address_space(3))) u32x2*)((base) + ((db) >> 1) * 4096 + 16 * (((4 * (db) + (g)) & 7) ^ (l32e & 7))) = (u32x2){lo, hi_}
    if constexpr (!TB) {
        if (nu > 0) {
            float il = __builtin_amdgcn_rcpf(l);
            asm volatile("s_nop 1" : "+v"(il));
            const lds_ptr wp = lw + reg + l32e * 128 + 8 * hie;
            const lds_ptr rp = lw + reg + r8e * 128 + 16 * (c8e ^ r8e);
            const unsigned ob = (unsigned)(q0 + 32 * sub) * QROW + hsel * 384;
            u32x4 w[4];
            auto scale = [&](int db) {
                float y[16];
#pragma unroll
                for (int r = 0; r < 16; ++r) y[r] = vmul(o[db][r], il);
                unsigned c[8];
#pragma unroll
                for (int r = 0; r < 8; ++r) c[r] = vcvt(y[2 * r], y[2 * r + 1]);
#pragma unroll
                for (int g = 0; g < 4; ++g) STG(wp, db, g, c[2 * g], c[2 * g + 1]);
                SBAR();
            };
            auto rd = [&](int s) {
#pragma unroll
                for (int g = 0; g < 4; ++g) w[g] = *(const __attribute__((address_space(3))) u32x4*)(rp + s * 4096 + g * 1024);
                SBAR();
            };
            auto st_ = [&](int s) {
#pragma unroll
                for (int g = 0; g < 4; ++g) __builtin_amdgcn_raw_buffer_store_b128(w[g], srdO, vO + s * 128, ob + g * 8 * QROW, 0);
                SBAR();
            };
            scale(0); scale(1); rd(0); scale(2); st_(0); scale(3); rd(1); scale(4); st_(1); scale(5); rd(2); st_(2);
        }
    } else {
        const lds_ptr xp = lw + reg + lane2 * 16;
        const lds_ptr xq = lw + (sub ? r1 : r2) + hsel * BLK + lane2 * 16;
        __attribute__((address_space(3))) f32x2* ml = (__attribute__((address_space(3))) f32x2*)(lw + LDS_MISC);
        ml[wid * 64 + lane2] = (f32x2){m, l};
#pragma unroll
        for (int i = 0; i < 3; ++i)
#pragma unroll
            for (int g = 0; g < 4; ++g) *(__attribute__((address_space(3))) f32x4*)(xp + (4 * i + g) * 1024) = (f32x4){o[2 * i + 1][4 * g], o[2 * i + 1][4 * g + 1], o[2 * i + 1][4 * g + 2], o[2 * i + 1][4 * g + 3]};
        BARRIER();
        const f32x2 pm = ml[(wid ^ 2) * 64 + lane2];
        f32x4 x[12];
#pragma unroll
        for (int i = 0; i < 12; ++i) x[i] = *(const __attribute__((address_space(3))) f32x4*)(xq + i * 1024);
        SBAR();
        const float mm = max2f(m, pm[0]);
        const float fa = __builtin_amdgcn_exp2f(m - mm), fb = __builtin_amdgcn_exp2f(pm[0] - mm);
        const float il = __builtin_amdgcn_rcpf(l * fa + pm[1] * fb);
        float wa = fa * il, wb2 = fb * il;
        asm volatile("s_nop 1" : "+v"(wa), "+v"(wb2));
        const lds_ptr wp = lw + r0 + hsel * BLK + l32e * 128 + 8 * hie + 16 * ((4 * sub) ^ (l32e & 4));
#pragma unroll
        for (int i = 0; i < 3; ++i) {
            float y[16];
#pragma unroll
            for (int r = 0; r < 16; ++r) y[r] = vmul(x[4 * i + (r >> 2)][r & 3], wb2);
#pragma unroll
            for (int r = 0; r < 16; ++r) y[r] = vfma_v(o[2 * i][r], wa, y[r]);
            unsigned c[8];
#pragma unroll
            for (int r = 0; r < 8; ++r) c[r] = vcvt(y[2 * r], y[2 * r + 1]);
#pragma unroll
            for (int g = 0; g < 4; ++g) *(__attribute__((address_space(3))) u32x2*)(wp + i * 4096 + 16 * (g ^ (l32e & 3))) = (u32x2){c[2 * g], c[2 * g + 1]};
            SBAR();
        }
        BARRIER();
        const lds_ptr rp = lw + r0 + hsel * BLK + sub * 2048 + r8e * 128 + 16 * (c8e ^ r8e);
        const unsigned ob = (unsigned)(q0 + 16 * sub) * QROW + hsel * 384;
        u32x4 w[6];
#pragma unroll
        for (int i = 0; i < 6; ++i) w[i] = *(const __attribute__((address_space(3))) u32x4*)(rp + (i % 3) * 4096 + (i / 3) * 1024);
#pragma unroll
        for (int i = 0; i < 6; ++i) __builtin_amdgcn_raw_buffer_store_b128(w[i], srdO, vO + (i % 3) * 128, ob + (i / 3) * 8 * QROW, 0);
    }
#undef STG
    STAMP(6);
#undef EXP
#undef FM
#undef KRD
#undef VRD
#undef VFR
#undef QK
#undef PV
#undef ADD0
#undef ADD1
#undef ROTATE
#undef BK_WAIT
#undef CV
#undef KADDR
}

DI int wave_max63(int x) {
    asm volatile("s_nop 1\n\tv_max_i32_dpp %0, %0, %0 row_shr:1 row_mask:0xf bank_mask:0xf\n\ts_nop 1\n\tv_max_i32_dpp %0, %0, %0 row_shr:2 row_mask:0xf bank_mask:0xf\n\t"
                 "s_nop 1\n\tv_max_i32_dpp %0, %0, %0 row_shr:4 row_mask:0xf bank_mask:0xf\n\ts_nop 1\n\tv_max_i32_dpp %0, %0, %0 row_shr:8 row_mask:0xf bank_mask:0xf\n\t"
                 "s_nop 1\n\tv_max_i32_dpp %0, %0, %0 row_bcast:15 row_mask:0xa bank_mask:0xf\n\ts_nop 1\n\tv_max_i32_dpp %0, %0, %0 row_bcast:31 row_mask:0xc bank_mask:0xf\n\ts_nop 0" : "+v"(x));
    return x;
}
constexpr int first_pure_b() { int i = TH; while (WB * ((i + 2) >> 1) < WA * TH) ++i; return i; }
constexpr int I0 = first_pure_b();
constexpr int NMIX = (I0 - TH) + TH / 2;
static_assert(NMIX <= 12 && I0 < 60, "level table (16 levels; thresholds are inline constants)");
constexpr int level(int k) {
    if (k < 4) return (I0 + 3 - k) | 0x10000;
    k -= 4;
    if (k >= NMIX) return (int)0xffff0040;
    int i = I0 - 1, jj = TH / 2 - 1, out = 0;
    for (int lev = 0; lev <= k; ++lev) {
        const bool useB = i >= TH && (jj < 0 || WB * ((i + 2) >> 1) >= WA * (2 * jj + 2));
        if (useB) { out = i | 0x10000; --i; } else { out = 2 * jj; --jj; }
    }
    return out;
}

__global__ __launch_bounds__(256, 2) void attn_v11(const unsigned short* __restrict__ Q, const unsigned short* __restrict__ K,
                                                  const unsigned short* __restrict__ V, const int* __restrict__ cu,
                                                  unsigned short* __restrict__ O, int nseq, int T, unsigned long long* dbg) {
    extern __shared__ __attribute__((aligned(16))) char lds[];
    unsigned long long st[16];
    STAMP(0);
    const int lane = threadIdx.x & 63;
    const int kvh = blockIdx.x & 7, j = blockIdx.x >> 3;
    const int wv = rfl(threadIdx.x >> 6);
    unsigned vp, vq;
    LaneAddr la;
    {
        const unsigned li = (j * 4 + wv) * 32 + (lane & 31), tk = li / 3u;
        vp = (tk * KROW + (li - 3u * tk) * 128u) | ((PFB && lane < 32 && j < 64) ? 0u : OOB);
        const unsigned lq = (j * 4 + wv) * 64 + lane, tq = lq / 6u;
        vq = (tq * QROW + (lq - 6u * tq) * 128u) | ((PFQ && j < 64) ? 0u : OOB);
        const int dr = (lane >> 2) & 7, dh = lane >> 5, dc = lane & 3, dg = (lane >> 4) & 1;
        la.vK = (8 * wv + dr) * KROW + 64u * dh + 16u * (dc ^ ((2 * wv + dg) & 3));
        la.vV = (8 * wv + dr) * KROW + 64u * dh + 16u * dc;
        la.vQ0 = dr * QROW + 64u * dh + 16u * (dc ^ dg);
        la.vQ1 = dr * QROW + 64u * dh + 16u * (dc ^ (2 + dg));
        asm volatile("" : "+v"(vp), "+v"(vq), "+v"(la.vK), "+v"(la.vV), "+v"(la.vQ0), "+v"(la.vQ1));
    }
    asm volatile("" :: "s"(Q), "s"(K), "s"(V), "s"(O), "s"(cu), "s"(nseq), "s"(T));
    int r = j < 32 ? j : (j < 64 ? 95 - j : j);
    int a, b;
    if (nseq <= 15) {
        const unsigned pm = (unsigned)(uintptr_t)lds + LDS_MISC + wv * 256;
        const __amdgpu_buffer_rsrc_t srdKa = __builtin_amdgcn_make_buffer_rsrc((void*)((const char*)K + kvh * 384), (short)0, (int)((unsigned)T * KROW - kvh * 384), 0x00020000);
        const __amdgpu_buffer_rsrc_t srdVa = __builtin_amdgcn_make_buffer_rsrc((void*)((const char*)V + kvh * 384), (short)0, (int)((unsigned)T * KROW - kvh * 384), 0x00020000);
        const __amdgpu_buffer_rsrc_t srdQa = __builtin_amdgcn_make_buffer_rsrc((void*)((const char*)Q + kvh * 768), (short)0, (int)((unsigned)T * QROW - kvh * 768), 0x00020000);
        i32x16 c;
        if (PFQ) asm volatile("s_load_dwordx16 %0, %1, 0x0\n\ts_mov_b32 m0, %2\n\ts_nop 0\n\tbuffer_load_dword %6, %7, 0 offen lds\n\tbuffer_load_dword %3, %4, 0 offen lds\n\tbuffer_load_dword %3, %5, 0 offen lds\n\ts_waitcnt lgkmcnt(0)"
                     : "=&s"(c) : "s"(cu), "s"(pm), "v"(vp), "s"(srdKa), "s"(srdVa), "v"(vq), "s"(srdQa) : "memory");
        else asm volatile("s_load_dwordx16 %0, %1, 0x0\n\ts_mov_b32 m0, %2\n\ts_nop 0\n\tbuffer_load_dword %3, %4, 0 offen lds\n\tbuffer_load_dword %3, %5, 0 offen lds\n\ts_waitcnt lgkmcnt(0)"
                     : "=&s"(c) : "s"(cu), "s"(pm), "v"(vp), "s"(srdKa), "s"(srdVa) : "memory");
        asm volatile("v_mov_b32 %0, %1\n\tv_writelane_b32 %0, %2, 1\n\tv_writelane_b32 %0, %3, 2\n\tv_writelane_b32 %0, %4, 3\n\tv_writelane_b32 %0, %5, 4\n\tv_writelane_b32 %0, %6, 5\n\t"
                     "v_writelane_b32 %0, %7, 6\n\tv_writelane_b32 %0, %8, 7\n\tv_writelane_b32 %0, %9, 8\n\tv_writelane_b32 %0, %10, 9\n\tv_writelane_b32 %0, %11, 10\n\t"
                     "v_writelane_b32 %0, %12, 11\n\tv_writelane_b32 %0, %13, 12\n\tv_writelane_b32 %0, %14, 13\n\tv_writelane_b32 %0, %15, 14\n\tv_writelane_b32 %0, %16, 15\n\ts_nop 1"
                     : "=&v"(a) : "s"(c[0]), "s"(c[1]), "s"(c[2]), "s"(c[3]), "s"(c[4]), "s"(c[5]), "s"(c[6]), "s"(c[7]), "s"(c[8]), "s"(c[9]), "s"(c[10]), "s"(c[11]), "s"(c[12]), "s"(c[13]), "s"(c[14]), "s"(c[15]));
        b = __builtin_amdgcn_update_dpp(a, a, 0x101, 0xf, 0xf, false);
    } else {
        u32x2 ab;
        asm volatile("global_load_dwordx2 %0, %1, %2\n\ts_waitcnt vmcnt(0)" : "=&v"(ab) : "v"(min(lane, nseq - 1) * 4), "s"(cu) : "memory");
        a = ab[0]; b = ab[1];
    }
    const int n = lane < nseq ? (b - a + 31) >> 5 : 0;
    int code = -1;
    unsigned long long msk = 0;
    if (__builtin_expect(__ballot(n > I0 + 4) != 0, 0)) {
        const int nmax = __builtin_amdgcn_readlane(wave_max63(n), 63);
        for (int i = nmax - 1; i >= I0 + 4; --i) {
            msk = __ballot(n > i);
            const int c = __popcll(msk);
            if (r < c) { code = i | 0x10000; goto found; }
            r -= c;
        }
    }
    {
        int cnt;
        asm volatile("v_cmp_lt_i32_e64 %0, (%c5&65535), %4\n\ts_bcnt1_i32_b64 %2, %0\n\ts_mov_b32 %3, %c5\n\ts_cmp_lt_u32 %1, %2\n\ts_cbranch_scc1 1f\n\ts_sub_u32 %1, %1, %2\n\t"
                     "v_cmp_lt_i32_e64 %0, (%c6&65535), %4\n\ts_bcnt1_i32_b64 %2, %0\n\ts_mov_b32 %3, %c6\n\ts_cmp_lt_u32 %1, %2\n\ts_cbranch_scc1 1f\n\ts_sub_u32 %1, %1, %2\n\t"
                     "v_cmp_lt_i32_e64 %0, (%c7&65535), %4\n\ts_bcnt1_i32_b64 %2, %0\n\ts_mov_b32 %3, %c7\n\ts_cmp_lt_u32 %1, %2\n\ts_cbranch_scc1 1f\n\ts_sub_u32 %1, %1, %2\n\t"
                     "v_cmp_lt_i32_e64 %0, (%c8&65535), %4\n\ts_bcnt1_i32_b64 %2, %0\n\ts_mov_b32 %3, %c8\n\ts_cmp_lt_u32 %1, %2\n\ts_cbranch_scc1 1f\n\ts_sub_u32 %1, %1, %2\n\t"
                     "v_cmp_lt_i32_e64 %0, (%c9&65535), %4\n\ts_bcnt1_i32_b64 %2, %0\n\ts_mov_b32 %3, %c9\n\ts_cmp_lt_u32 %1, %2\n\ts_cbranch_scc1 1f\n\ts_sub_u32 %1, %1, %2\n\t"
                     "v_cmp_lt_i32_e64 %0, (%c10&65535), %4\n\ts_bcnt1_i32_b64 %2, %0\n\ts_mov_b32 %3, %c10\n\ts_cmp_lt_u32 %1, %2\n\ts_cbranch_scc1 1f\n\ts_sub_u32 %1, %1, %2\n\t"
                     "v_cmp_lt_i32_e64 %0, (%c11&65535), %4\n\ts_bcnt1_i32_b64 %2, %0\n\ts_mov_b32 %3, %c11\n\ts_cmp_lt_u32 %1, %2\n\ts_cbranch_scc1 1f\n\ts_sub_u32 %1, %1, %2\n\t"
                     "v_cmp_lt_i32_e64 %0, (%c12&65535), %4\n\ts_bcnt1_i32_b64 %2, %0\n\ts_mov_b32 %3, %c12\n\ts_cmp_lt_u32 %1, %2\n\ts_cbranch_scc1 1f\n\ts_sub_u32 %1, %1, %2\n\t"
                     "v_cmp_lt_i32_e64 %0, (%c13&65535), %4\n\ts_bcnt1_i32_b64 %2, %0\n\ts_mov_b32 %3, %c13\n\ts_cmp_lt_u32 %1, %2\n\ts_cbranch_scc1 1f\n\ts_sub_u32 %1, %1, %2\n\t"
                     "v_cmp_lt_i32_e64 %0, (%c14&65535), %4\n\ts_bcnt1_i32_b64 %2, %0\n\ts_mov_b32 %3, %c14\n\ts_cmp_lt_u32 %1, %2\n\ts_cbranch_scc1 1f\n\ts_sub_u32 %1, %1, %2\n\t"
                     "v_cmp_lt_i32_e64 %0, (%c15&65535), %4\n\ts_bcnt1_i32_b64 %2, %0\n\ts_mov_b32 %3, %c15\n\ts_cmp_lt_u32 %1, %2\n\ts_cbranch_scc1 1f\n\ts_sub_u32 %1, %1, %2\n\t"
                     "v_cmp_lt_i32_e64 %0, (%c16&65535), %4\n\ts_bcnt1_i32_b64 %2, %0\n\ts_mov_b32 %3, %c16\n\ts_cmp_lt_u32 %1, %2\n\ts_cbranch_scc1 1f\n\ts_sub_u32 %1, %1, %2\n\t"
                     "v_cmp_lt_i32_e64 %0, (%c17&65535), %4\n\ts_bcnt1_i32_b64 %2, %0\n\ts_mov_b32 %3, %c17\n\ts_cmp_lt_u32 %1, %2\n\ts_cbranch_scc1 1f\n\ts_sub_u32 %1, %1, %2\n\t"
                     "v_cmp_lt_i32_e64 %0, (%c18&65535), %4\n\ts_bcnt1_i32_b64 %2, %0\n\ts_mov_b32 %3, %c18\n\ts_cmp_lt_u32 %1, %2\n\ts_cbranch_scc1 1f\n\ts_sub_u32 %1, %1, %2\n\t"
                     "v_cmp_lt_i32_e64 %0, (%c19&65535), %4\n\ts_bcnt1_i32_b64 %2, %0\n\ts_mov_b32 %3, %c19\n\ts_cmp_lt_u32 %1, %2\n\ts_cbranch_scc1 1f\n\ts_sub_u32 %1, %1, %2\n\t"
                     "v_cmp_lt_i32_e64 %0, (%c20&65535), %4\n\ts_bcnt1_i32_b64 %2, %0\n\ts_mov_b32 %3, %c20\n\ts_cmp_lt_u32 %1, %2\n\ts_cbranch_scc1 1f\n\ts_sub_u32 %1, %1, %2\n\t"
                     "s_mov_b32 %3, -1\n1:"
                     : "=&s"(msk), "+s"(r), "=&s"(cnt), "=&s"(code) : "v"(n), "i"(level(0)), "i"(level(1)), "i"(level(2)), "i"(level(3)), "i"(level(4)), "i"(level(5)), "i"(level(6)), "i"(level(7)), "i"(level(8)), "i"(level(9)), "i"(level(10)), "i"(level(11)), "i"(level(12)), "i"(level(13)), "i"(level(14)), "i"(level(15)) : "scc");
    }
found:
    if (__builtin_expect(code < 0, 0)) { asm volatile("s_waitcnt vmcnt(0)" ::: "memory"); return; }
    const int tb = code >> 16, idx = (code & 0xffff) >> (1 - tb);
    const unsigned below = __builtin_amdgcn_mbcnt_hi((unsigned)(msk >> 32), __builtin_amdgcn_mbcnt_lo((unsigned)msk, 0u));
    const unsigned long long hit = __ballot(below == (unsigned)r) & msk;
    const int sl = __builtin_ctzll(hit);
    const int s0 = min(max(__builtin_amdgcn_readlane(a, sl), 0), T), L = min(__builtin_amdgcn_readlane(b, sl), T) - s0;
    if (__builtin_expect(L <= 0, 0)) { asm volatile("s_waitcnt vmcnt(0)" ::: "memory"); return; }
    if (DELAY2 > 0 && j >= 32) __builtin_amdgcn_s_sleep(DELAY2);
    STAMP(1);
    if (tb) attn_item<true>(st, Q, K, V, O, kvh, s0, L, idx, lds, wv, la);
    else attn_item<false>(st, Q, K, V, O, kvh, s0, L, idx, lds, wv, la);
}

extern "C" void kernel_launch(void* const* d_in, const int* in_sizes, int n_in, void* d_out, int out_size, void* d_ws, size_t ws_size, hipStream_t stream) {
    const int T = in_sizes[0] / (HQ * D), nseq = in_sizes[3] - 1;
    int slots = (T + 31) / 32 + nseq;
    if (slots < 64) slots = 64;
    (void)hipFuncSetAttribute((const void*)attn_v11, hipFuncAttributeMaxDynamicSharedMemorySize, LDS_BYTES);
    attn_v11<<<slots * 8, 256, LDS_BYTES, stream>>>((const unsigned short*)d_in[0], (const unsigned short*)d_in[1], (const unsigned short*)d_in[2],
                                                 (const int*)d_in[3], (unsigned short*)d_out, nseq, T, (unsigned long long*)d_ws);
}
```

```cpp
#define STAMPS
#define AMPL 0
#define JLO 0
#define JHI 0
#include <hip/hip_runtime.h>
#include <hip/hip_bf16.h>
#include <stdint.h>

typedef short bf16x8 __attribute__((ext_vector_type(8)));
typedef short s16x4 __attribute__((ext_vector_type(4)));
typedef float f32x16 __attribute__((ext_vector_type(16)));
typedef float f32x4 __attribute__((ext_vector_type(4)));
typedef float f32x2 __attribute__((ext_vector_type(2)));
typedef unsigned u32x4 __attribute__((ext_vector_type(4)));
typedef unsigned u32x2 __attribute__((ext_vector_type(2)));
typedef int i32x16 __attribute__((ext_vector_type(16)));
typedef __attribute__((address_space(3))) const char* lds_cptr;
typedef __attribute__((address_space(3))) char* lds_ptr;
typedef short v4i16_t __attribute__((ext_vector_type(4)));

constexpr int HQ = 16, HKV = 8, D = 192;
constexpr unsigned QROW = HQ * D * 2, KROW = HKV * D * 2;
constexpr unsigned KBLK = 32 * KROW;
constexpr int BLK = 12288;
constexpr int LDS_MISC = 6 * BLK;
constexpr int LDS_BYTES = LDS_MISC + 2048;
constexpr float C2 = 0.07216878364870322f * 1.4426950408889634f;
constexpr float LBIG = 1073741824.f;
#define TH 8
#define PFKV 0
#define PFFIRST 0
#define PFB 1
#define DELAY2 0
#define PFQ 0
#define WA 10
#define WB 11
constexpr unsigned LDS_NOWHERE = 0x30000u;
constexpr unsigned OOB = 0x80000000u;

#define DI __device__ __forceinline__
#define MFMA(a, b, c) __builtin_amdgcn_mfma_f32_32x32x16_bf16(a, b, c, 0, 0, 0)
#define SBAR() __builtin_amdgcn_sched_barrier(0)
#define PIN(x) asm volatile("" : "+v"(x))
typedef __bf16 bf16x2_t __attribute__((ext_vector_type(2)));
DI unsigned cvtpk(float lo, float hi) { const f32x2 v = {lo, hi}; return __builtin_bit_cast(unsigned, __builtin_convertvector(v, bf16x2_t)); }
DI float max3f(float a, float b, float c) { float r; asm("v_max3_f32 %0, %1, %2, %3" : "=v"(r) : "v"(a), "v"(b), "v"(c)); return r; }
DI float max2f(float a, float b) { float r; asm("v_max_f32_e32 %0, %1, %2" : "=v"(r) : "v"(a), "v"(b)); return r; }
DI float fmul_s(float a, float b) { float r; asm("v_mul_f32_e32 %0, %1, %2" : "=v"(r) : "v"(a), "v"(b)); return r; }
DI float fma_v(float a, float b, float c) { float r; asm("v_fma_f32 %0, %1, %2, %3" : "=v"(r) : "v"(a), "v"(b), "v"(c)); return r; }
DI void vexp(float& x) { asm volatile("v_exp_f32_e32 %0, %0" : "+v"(x)); }
DI float vadd(float a, float b) { float r; asm volatile("v_add_f32_e32 %0, %1, %2" : "=v"(r) : "v"(a), "v"(b)); return r; }
DI float vfma(float a, float b, float c) { float r; asm volatile("v_fma_f32 %0, %1, %2, %3" : "=v"(r) : "v"(a), "s"(b), "v"(c)); return r; }
DI float vmul(float a, float b) { float r; asm volatile("v_mul_f32_e32 %0, %1, %2" : "=v"(r) : "v"(a), "v"(b)); return r; }
DI float vfma_v(float a, float b, float c) { float r; asm volatile("v_fma_f32 %0, %1, %2, %3" : "=v"(r) : "v"(a), "v"(b), "v"(c)); return r; }
DI unsigned vcvt(float lo, float hi) { unsigned r; asm volatile("v_cvt_pk_bf16_f32 %0, %1, %2" : "=v"(r) : "v"(lo), "v"(hi)); return r; }
DI s16x4 vtr(lds_cptr p) { return __builtin_bit_cast(s16x4, __builtin_amdgcn_ds_read_tr16_b64_v4i16((__attribute__((address_space(3))) v4i16_t*)p)); }
DI bf16x8 ldk(lds_cptr p) { return *(const __attribute__((address_space(3))) bf16x8*)p; }
DI int rfl(int x) { return __builtin_amdgcn_readfirstlane(x); }
DI float swapmax(float x) { auto r = __builtin_amdgcn_permlane32_swap(__float_as_uint(x), __float_as_uint(x), false, false); return max2f(__uint_as_float(r[0]), __uint_as_float(r[1])); }
DI float swapsum(float x) { auto r = __builtin_amdgcn_permlane32_swap(__float_as_uint(x), __float_as_uint(x), false, false); return __uint_as_float(r[0]) + __uint_as_float(r[1]); }

template <int S, int OFF> DI void dma(unsigned mb, unsigned voff, __amdgpu_buffer_rsrc_t srd, unsigned soff) {
    asm volatile("s_add_u32 m0, %0, %5\n\ts_nop 0\n\tbuffer_load_dwordx4 %1, %2, %3 offen offset:%4 lds"
                 :: "s"(mb), "v"(voff), "s"(srd), "s"(soff), "i"(128 * S), "i"(OFF + S * (4096 - 128)) : "memory", "scc");
}
DI void dma3(unsigned mb, unsigned voff, __amdgpu_buffer_rsrc_t srd, unsigned soff) { dma<0, 0>(mb, voff, srd, soff); dma<1, 0>(mb, voff, srd, soff); dma<2, 0>(mb, voff, srd, soff); }
#define DMAL dma
#define LOOPBAR() BARRIER()
#define LOOPWAIT() WAIT_VM(6)
#define WAIT_VM(n) asm volatile("s_waitcnt vmcnt(" #n ")" ::: "memory")
#define BARRIER() asm volatile("s_waitcnt lgkmcnt(0)\n\ts_barrier" ::: "memory")
#define OPIN(nop) asm volatile(nop : "+v"(o[0]), "+v"(o[1]), "+v"(o[2]), "+v"(o[3]), "+v"(o[4]), "+v"(o[5]))

#define STAMP(i) do { SBAR(); unsigned long long t_; asm volatile("s_memrealtime %0\n\ts_waitcnt lgkmcnt(0)" : "=s"(t_) :: "memory"); st[i] = t_; SBAR(); } while (0)
#define STAMPC(i) do { SBAR(); unsigned long long t_; asm volatile("s_memtime %0\n\ts_waitcnt lgkmcnt(0)" : "=s"(t_) :: "memory"); st[i] = t_; SBAR(); } while (0)

struct LaneAddr { unsigned vK, vV, vQ0, vQ1; };

template <bool TB>
DI void attn_item(unsigned long long* st, const unsigned short* __restrict__ Q, const unsigned short* __restrict__ K, const unsigned short* __restrict__ V,
                  unsigned short* __restrict__ O, int kvh, int s0, int L, int idx, char* lds, int wid, const LaneAddr& la) {
    const int tid = threadIdx.x, lane = tid & 63, l32 = lane & 31, hi = lane >> 5;
    const int hsel = wid & 1, sub = wid >> 1;
    const int n = (L + 31) >> 5;
    const int q0 = TB ? 32 * idx : 64 * idx;
    const int NB = TB ? idx + 1 : min(2 * idx + 2, n);
    const int NU = TB ? (NB + 1) >> 1 : NB;
    const int nu = TB ? (NB + 1 - sub) >> 1 : (2 * idx + sub < n ? 2 * idx + sub + 1 : 0);
    const int diagu = TB ? (((idx & 1) == sub) ? nu - 1 : -1) : nu - 1;

    const unsigned krec = (unsigned)min(L, 32 * NB) * KROW - kvh * 384;
    const __amdgpu_buffer_rsrc_t srdK = __builtin_amdgcn_make_buffer_rsrc((void*)((const char*)K + (size_t)s0 * KROW + kvh * 384), (short)0, (int)krec, 0x00020000);
    const __amdgpu_buffer_rsrc_t srdQ = __builtin_amdgcn_make_buffer_rsrc((void*)((const char*)Q + (size_t)s0 * QROW + kvh * 768), (short)0, (int)((unsigned)L * QROW - kvh * 768), 0x00020000);
    const unsigned ldsb = (unsigned)(uintptr_t)lds;
    const unsigned wb = ldsb + wid * 1024;
    const unsigned vK = la.vK, vV = la.vV;

    dma3(wb, vK, srdK, 0);
    if (TB) dma3(wb + BLK, vK, srdK, KBLK);
    {
        const unsigned qreg = TB ? 4 * BLK + hsel * BLK : (wid == 3 ? 2 * BLK : (3 + wid) * BLK);
        const unsigned qs = (unsigned)(q0 + (TB ? 16 * sub : 32 * sub)) * QROW + hsel * 384;
        const unsigned m = ldsb + qreg + (TB ? 2048 * sub : 0);
#pragma unroll
        for (int g = 0; g < (TB ? 2 : 4); ++g) dma3(m + g * 1024, (g & 1) ? la.vQ1 : la.vQ0, srdQ, qs + g * 8 * QROW);
    }
    SBAR();
    const __amdgpu_buffer_rsrc_t srdV = __builtin_amdgcn_make_buffer_rsrc((void*)((const char*)V + (size_t)s0 * KROW + kvh * 384), (short)0, (int)krec, 0x00020000);
    if (PFKV) {
        const int per = TB ? 8 : 16, tk = (lane * 43) >> 7;
        const unsigned vp = tk < per ? (unsigned)(q0 + wid * per + tk) * KROW + (lane - 3 * tk) * 128 : OOB;
        const unsigned pm = ldsb + LDS_MISC + wid * 256;
        asm volatile("s_mov_b32 m0, %0\n\ts_nop 0\n\tbuffer_load_dword %1, %2, 0 offen lds" :: "s"(pm), "v"(vp), "s"(srdK) : "memory");
        asm volatile("s_mov_b32 m0, %0\n\ts_nop 0\n\tbuffer_load_dword %1, %2, 0 offen lds" :: "s"(pm), "v"(vp), "s"(srdV) : "memory");
    }
    const int qrel = q0 + (TB ? 0 : 32 * sub) + l32;
    const lds_cptr lp = (lds_cptr)lds;
    lds_cptr kb[2], vb, vbo;
#pragma unroll
    for (int i = 0; i < 2; ++i) kb[i] = lp + (l32 >> 3) * 1024 + (l32 & 7) * 64 + 16 * ((2 * i + hi) ^ ((l32 >> 2) & 3));
    vb = lp + (4 * hi + ((lane & 15) >> 2)) * 64 + 32 * ((lane >> 4) & 1) + 8 * (lane & 3);
#define KADDR(ks) (kb[(ks) & 1] + ((ks) >> 2) * 4096 + (((ks) >> 1) & 1) * 512)
    f32x16 o[6];
#pragma unroll
    for (int i = 0; i < 6; ++i) o[i] = f32x16{};
    OPIN("");

    STAMP(2);
    bf16x8 qf[12];
    if (PFKV) { WAIT_VM(2); } else { WAIT_VM(0); }
    if (TB) BARRIER();
    {
        const int qo = TB ? 4 * BLK + hsel * BLK : (wid == 3 ? 2 * BLK : (3 + wid) * BLK);
#pragma unroll
        for (int ks = 0; ks < 12; ++ks) {
            qf[ks] = ldk(KADDR(ks) + qo);
            if (TB) {
                if (ks == 1) dma<0, 0>(wb + 2 * BLK, vK, srdK, 2 * KBLK); if (ks == 3) dma<1, 0>(wb + 2 * BLK, vK, srdK, 2 * KBLK); if (ks == 5) dma<2, 0>(wb + 2 * BLK, vK, srdK, 2 * KBLK);
                if (ks == 7) dma<0, 0>(wb + 3 * BLK, vK, srdK, 3 * KBLK); if (ks == 9) dma<1, 0>(wb + 3 * BLK, vK, srdK, 3 * KBLK); if (ks == 11) dma<2, 0>(wb + 3 * BLK, vK, srdK, 3 * KBLK);
            } else {
                if (ks == 3) dma<0, 0>(wb + BLK, vK, srdK, KBLK); if (ks == 7) dma<1, 0>(wb + BLK, vK, srdK, KBLK); if (ks == 11) dma<2, 0>(wb + BLK, vK, srdK, KBLK);
            }
        }
    }
    BARRIER();
    STAMP(3);
    kb[0] += TB ? sub * BLK : 0; kb[1] += TB ? sub * BLK : 0;
    vb += TB ? sub * BLK : 3 * BLK;
    vbo = vb + (TB ? 512 - sub * 512 : 512);
    vb += TB ? sub * 512 : 0;

    f32x16 S, X;
    float m = 0.f, l = 0.f;
    auto mask = [&](int kblock) {
        const int d = qrel - kblock * 32 - 4 * hi;
        const float ninf = -__builtin_inff();
        unsigned long long k[16];
#define MK(r) asm volatile("v_cmp_le_i32_e64 %0, %2, %1" : "=s"(k[r]) : "v"(d), "i"(((r) & 3) + 8 * ((r) >> 2)))
#define MS(r) do { float t_ = S[r]; asm volatile("v_cndmask_b32_e64 %0, %1, %0, %2" : "+v"(t_) : "v"(ninf), "s"(k[r])); S[r] = t_; } while (0)
        MK(0); MK(1); MK(2); MK(3); MK(4); MK(5); MK(6); MK(7); MK(8); MK(9); MK(10); MK(11); MK(12); MK(13); MK(14); MK(15);
        MS(0); MS(1); MS(2); MS(3); MS(4); MS(5); MS(6); MS(7); MS(8); MS(9); MS(10); MS(11); MS(12); MS(13); MS(14); MS(15);
#undef MK
#undef MS
    };
    auto rowmax = [&]() -> float {
        const float a = max3f(S[0], S[1], S[2]), b = max3f(S[3], S[4], S[5]), c = max3f(S[6], S[7], S[8]), d = max3f(S[9], S[10], S[11]), e = max3f(S[12], S[13], S[14]);
        return swapmax(max2f(max3f(a, b, S[15]), max3f(c, d, e)));
    };
    const unsigned vK1 = vK + KBLK, vV1 = vV + KBLK;
    u32x4 p0, p1;
    bf16x8 kf[6];
    s16x4 vlo[6], vhi[6];
#define EXP(e) do { float t_ = X[e]; vexp(t_); X[e] = t_; } while (0)
#define FM(e) X[e] = vfma(S[e], C2, nm)
#define KRD(k) do { kf[(k) % 6] = ldk(KADDR(k)); SBAR(); } while (0)
#define VRD(k) do { const lds_cptr vp_ = ((k) & 1 ? vbo : vb) + (((k) % 6) >> 1) * 4096 + ((k) / 6) * 2048; vlo[(k) % 6] = vtr(vp_); vhi[(k) % 6] = vtr(vp_ + 1024); SBAR(); } while (0)
#define VFR(k) ((bf16x8){vlo[(k) % 6][0], vlo[(k) % 6][1], vlo[(k) % 6][2], vlo[(k) % 6][3], vhi[(k) % 6][0], vhi[(k) % 6][1], vhi[(k) % 6][2], vhi[(k) % 6][3]})
#define QK(k) do { S = MFMA(kf[(k) % 6], qf[k], (k) == 0 ? f32x16{} : S); PIN(S); SBAR(); } while (0)
#define PV(k) do { o[(k) % 6] = MFMA(VFR(k), __builtin_bit_cast(bf16x8, (k) < 6 ? p0 : p1), o[(k) % 6]); PIN(o[(k) % 6]); SBAR(); } while (0)
#define ADD0(e) ps0 = vadd(ps0, X[e])
#define ADD1(e) ps1 = vadd(ps1, X[e])
#define CV(P, i, e) P[i] = vcvt(X[e], X[(e) + 1])

    int r0 = 0, r1 = TB ? 2 * BLK : BLK, r2 = TB ? 4 * BLK : 2 * BLK;
    const unsigned mV1 = (TB || 1 < NB) ? wb + (TB ? 5 : 4) * BLK : LDS_NOWHERE, mK2 = 2 < NB ? wb + 2 * BLK : LDS_NOWHERE;
    if (nu > 0) {
        KRD(1); KRD(0); KRD(3); KRD(2); KRD(5); KRD(4);
        if (TB) {
            QK(0); dma<0, 0>(wb + 4 * BLK, vV, srdV, 0); SBAR();
            QK(1); KRD(7); SBAR();
            QK(2); KRD(6); dma<1, 0>(wb + 4 * BLK, vV, srdV, 0); SBAR();
            QK(3); KRD(9); SBAR();
            QK(4); KRD(8); dma<2, 0>(wb + 4 * BLK, vV, srdV, 0); SBAR();
            QK(5); KRD(11); SBAR();
            QK(6); KRD(10); dma<0, 0>(mV1, vV, srdV, KBLK); SBAR();
            QK(7);
            QK(8); dma<1, 0>(mV1, vV, srdV, KBLK); SBAR();
            QK(9);
            QK(10); dma<2, 0>(mV1, vV, srdV, KBLK); SBAR();
            QK(11);
        } else {
            QK(0); dma<0, 0>(wb + 3 * BLK, vV, srdV, 0); SBAR();
            QK(1); KRD(7); dma<1, 0>(wb + 3 * BLK, vV, srdV, 0); SBAR();
            QK(2); KRD(6); dma<2, 0>(wb + 3 * BLK, vV, srdV, 0); SBAR();
            QK(3); KRD(9); SBAR();
            QK(4); KRD(8); dma<0, 0>(mV1, vV, srdV, KBLK); SBAR();
            QK(5); KRD(11); dma<1, 0>(mV1, vV, srdV, KBLK); SBAR();
            QK(6); KRD(10); dma<2, 0>(mV1, vV, srdV, KBLK); SBAR();
            QK(7);
            QK(8); dma<0, 0>(mK2, vK, srdK, 2 * KBLK); SBAR();
            QK(9); dma<1, 0>(mK2, vK, srdK, 2 * KBLK); SBAR();
            QK(10); dma<2, 0>(mK2, vK, srdK, 2 * KBLK); SBAR();
            QK(11);
        }
    } else {
        dma3(wb + 3 * BLK, vV, srdV, 0); dma3(mV1, vV, srdV, KBLK); dma3(mK2, vK, srdK, 2 * KBLK);
    }
    WAIT_VM(6); BARRIER();
    kb[0] += r1; kb[1] += r1; if (TB) { vb += r2; vbo += r2; }
    if (nu > 1) { KRD(1); KRD(0); KRD(3); KRD(2); KRD(5); KRD(4); }
    if (TB) { dma<0, 0>(wb + r0, vK, srdK, 4 * KBLK); dma<1, 0>(wb + r0, vK, srdK, 4 * KBLK); }
    if (nu > 0) {
        asm volatile("s_nop 11" : "+v"(S));
        if (diagu == 0) mask(TB ? sub : 0);
        m = rowmax() * C2;
        const float nm = -m;
        FM(0); FM(1); FM(2); FM(3); FM(4); FM(5); FM(6); FM(7); FM(8); FM(9); FM(10); FM(11); FM(12); FM(13); FM(14); FM(15);
        EXP(0); EXP(1); EXP(2); EXP(3);
    } else X = f32x16{};
    const float nm = -m;

#define ROTATE() do { const int t_ = TB ? r2 : r0; if (TB) { r2 = r1; r1 = r0; r0 = t_; } else { r0 = r1; r1 = r2; r2 = t_; } so += (TB ? 2 : 1) * KBLK; ++u; } while (0)
#define BK_WAIT() LOOPWAIT()
    STAMP(4); STAMPC(8);
    unsigned barw = 0, barn = 0; (void)barw; (void)barn;
    int u = 0;
    unsigned so = 0;
    while (u + 1 < nu) {
        float ps0, ps1;
        const unsigned soK = so + (TB ? 4 : 3) * KBLK, soV = so + 2 * KBLK;
        const unsigned mK = (TB || u + 3 < NU) ? wb + r0 : LDS_NOWHERE, mV = TB ? wb + r1 : (u + 2 < NU ? wb + 3 * BLK + r2 : LDS_NOWHERE);
        const int dk = TB ? r0 - r1 : r2 - r1, dv = TB ? r1 - r2 : r1 - r0;
        if (TB) {
            QK(0);  EXP(4); EXP(5); ps0 = vadd(X[0], X[1]); SBAR();
            QK(1);  KRD(7); DMAL<2, 0>(mK, vK, srdK, soK); EXP(6); SBAR();
            QK(2);  KRD(6); EXP(7); EXP(8); ps1 = vadd(X[2], X[3]); SBAR();
            QK(3);  KRD(9); DMAL<0, BLK>(mK, vK1, srdK, soK); EXP(9); SBAR();
            QK(4);  KRD(8); EXP(10); CV(p0, 0, 0); ADD0(4); ADD1(5); SBAR();
            QK(5);  KRD(11); DMAL<1, BLK>(mK, vK1, srdK, soK); EXP(11); SBAR();
            QK(6);  KRD(10); EXP(12); CV(p0, 1, 2); CV(p0, 2, 4); SBAR();
            QK(7);  DMAL<2, BLK>(mK, vK1, srdK, soK); EXP(13); ADD0(6); SBAR();
            QK(8);
            LOOPWAIT(); LOOPBAR();
            VRD(1); VRD(0); VRD(3); VRD(2);
            QK(9);  DMAL<0, 0>(mV, vV, srdV, soV); VRD(5); EXP(14); SBAR();
            QK(10); VRD(4); EXP(15); CV(p0, 3, 6); ADD1(7); ADD0(8); SBAR();
            QK(11); DMAL<1, 0>(mV, vV, srdV, soV); ADD1(9); CV(p1, 0, 8); ADD0(10); SBAR();
            PV(0);  ADD1(11); CV(p1, 1, 10); ADD0(12); ADD1(13); CV(p1, 2, 12); SBAR();
            PV(1);  VRD(7); DMAL<2, 0>(mV, vV, srdV, soV); ADD0(14); SBAR();
            if (__builtin_expect(u + 1 == diagu, 0)) { asm volatile("s_nop 7" : "+v"(S)); mask(2 * u + 2 + sub); }
            PV(2);  VRD(6); ADD1(15); CV(p1, 3, 14); FM(0); FM(1); SBAR();
            PV(3);  VRD(9); DMAL<0, BLK>(mV, vV1, srdV, soV); FM(2); SBAR();
            PV(4);  VRD(8); FM(3); FM(4); FM(5); ps0 = vadd(ps0, ps1); SBAR();
            PV(5);  VRD(11); DMAL<1, BLK>(mV, vV1, srdV, soV); FM(6); SBAR();
            PV(6);  VRD(10); FM(7); FM(8); FM(9); l = vadd(l, ps0); SBAR();
            PV(7);  DMAL<2, BLK>(mV, vV1, srdV, soV); FM(10); FM(11); kb[0] += dk; kb[1] += dk; PIN(kb[0]); PIN(kb[1]); SBAR();
            PV(8);
        } else {
            QK(0);  EXP(4); EXP(5); ps0 = vadd(X[0], X[1]); SBAR();
            QK(1);  KRD(7); EXP(6); EXP(7); ps1 = vadd(X[2], X[3]); SBAR();
            QK(2);  KRD(6); EXP(8); CV(p0, 0, 0); ADD0(4); ADD1(5); SBAR();
            QK(3);  KRD(9); DMAL<0, 0>(mK, vK, srdK, soK); EXP(9); SBAR();
            QK(4);  KRD(8); EXP(10); CV(p0, 1, 2); CV(p0, 2, 4); SBAR();
            QK(5);  KRD(11); DMAL<1, 0>(mK, vK, srdK, soK); EXP(11); SBAR();
            QK(6);  KRD(10); EXP(12); ADD0(6); CV(p0, 3, 6); ADD1(7); SBAR();
            QK(7);  VRD(1); DMAL<2, 0>(mK, vK, srdK, soK); EXP(13); SBAR();
            QK(8);  VRD(0); EXP(14); ADD0(8); ADD1(9); SBAR();
            QK(9);  VRD(3); EXP(15); CV(p1, 0, 8); SBAR();
            QK(10); VRD(2); ADD0(10); ADD1(11); CV(p1, 1, 10); SBAR();
            QK(11); VRD(5); ADD0(12); ADD1(13); CV(p1, 2, 12); SBAR();
            PV(0);  VRD(4); DMAL<0, 0>(mV, vV, srdV, soV); ADD0(14); SBAR();
            PV(1);  VRD(7); ADD1(15); CV(p1, 3, 14); SBAR();
            if (__builtin_expect(u + 1 == diagu, 0)) { asm volatile("s_nop 7" : "+v"(S)); mask(u + 1); }
            PV(2);  VRD(6); DMAL<1, 0>(mV, vV, srdV, soV); FM(0); SBAR();
            PV(3);  VRD(9); FM(1); FM(2); FM(3); ps0 = vadd(ps0, ps1); SBAR();
            PV(4);  VRD(8); DMAL<2, 0>(mV, vV, srdV, soV); FM(4); SBAR();
            PV(5);  VRD(11); FM(5); FM(6); FM(7); l = vadd(l, ps0); SBAR();
            PV(6);  VRD(10); FM(8); FM(9); FM(10); SBAR();
            PV(7);  FM(11); FM(12); FM(13); kb[0] += dk; kb[1] += dk; PIN(kb[0]); PIN(kb[1]); SBAR();
            PV(8);
        }
        BK_WAIT(); LOOPBAR();
        KRD(1); KRD(0); KRD(3); KRD(2);
        if (TB) {
            PV(9);  DMAL<0, 0>(wb + r2, vK, srdK, soK + 2 * KBLK); KRD(5); FM(12); FM(13); SBAR();
            PV(10); KRD(4); FM(14); FM(15); EXP(0); vb += dv; vbo += dv; PIN(vb); PIN(vbo); SBAR();
            PV(11); DMAL<1, 0>(wb + r2, vK, srdK, soK + 2 * KBLK); EXP(1); EXP(2); EXP(3); SBAR();
        } else {
            PV(9);  KRD(5); FM(14); FM(15); EXP(0); SBAR();
            PV(10); KRD(4); EXP(1); EXP(2); vb += dv; vbo += dv; PIN(vb); PIN(vbo); SBAR();
            PV(11); EXP(3); SBAR();
        }
        ROTATE();
    }
    STAMPC(9);
#define TAIL_HEAD() \
        const bool last = u == NU - 1; \
        if (!last) { \
            if (TB) { dma<2, 0>(wb + r0, vK, srdK, so + 4 * KBLK); dma3(wb + r0 + BLK, vK1, srdK, so + 4 * KBLK); } \
            else { dma3(u + 3 < NU ? wb + r0 : LDS_NOWHERE, vK, srdK, so + 3 * KBLK); dma3(u + 2 < NU ? wb + 3 * BLK + r2 : LDS_NOWHERE, vV, srdV, so + 2 * KBLK); } \
        }
#define TAIL_MID() \
        if (TB) { if (last) { WAIT_VM(0); } else { WAIT_VM(6); } BARRIER(); }
#define TAIL_END() \
        if (!last) { BK_WAIT(); BARRIER(); if (TB) { dma<0, 0>(wb + r2, vK, srdK, so + 6 * KBLK); dma<1, 0>(wb + r2, vK, srdK, so + 6 * KBLK); } }
    if (u < nu) {
        TAIL_HEAD()
#pragma unroll
        for (int e = 4; e < 8; ++e) EXP(e);
        float ps0 = vadd(X[0], X[1]), ps1 = vadd(X[2], X[3]);
        CV(p0, 0, 0); CV(p0, 1, 2); EXP(8); EXP(9); CV(p0, 2, 4); CV(p0, 3, 6);
        TAIL_MID()
        VRD(1); VRD(0); VRD(3); VRD(2); VRD(5); VRD(4);
        EXP(10); EXP(11); EXP(12); EXP(13); SBAR();
        const unsigned mVt = last ? LDS_NOWHERE : wb + r1;
        PV(0);  if (TB) dma<0, 0>(mVt, vV, srdV, so + 2 * KBLK); EXP(14); EXP(15); SBAR();
        PV(1);  VRD(7); ADD0(4); ADD1(5); SBAR();
        PV(2);  VRD(6); if (TB) dma<1, 0>(mVt, vV, srdV, so + 2 * KBLK); CV(p1, 0, 8); CV(p1, 1, 10); SBAR();
        PV(3);  VRD(9); CV(p1, 2, 12); CV(p1, 3, 14); SBAR();
        PV(4);  VRD(8); if (TB) dma<2, 0>(mVt, vV, srdV, so + 2 * KBLK); ADD0(6); ADD1(7); SBAR();
        PV(5);  VRD(11); ADD0(8); ADD1(9); ADD0(10); SBAR();
        PV(6);  VRD(10); if (TB) dma<0, BLK>(mVt, vV1, srdV, so + 2 * KBLK); ADD1(11); ADD0(12); SBAR();
        PV(7);  if (TB) dma<1, BLK>(mVt, vV1, srdV, so + 2 * KBLK); ADD1(13); SBAR();
        PV(8);  if (TB) dma<2, BLK>(mVt, vV1, srdV, so + 2 * KBLK); ADD0(14); ADD1(15); SBAR();
        PV(9); PV(10); PV(11);
        l += ps0 + ps1;
        TAIL_END()
        ROTATE();
    }
    while (u < NU) {
        TAIL_HEAD()
        TAIL_MID()
        if (TB && !last) { dma3(wb + r1, vV, srdV, so + 2 * KBLK); dma3(wb + r1 + BLK, vV1, srdV, so + 2 * KBLK); }
        TAIL_END()
        ROTATE();
    }
#undef TAIL_HEAD
#undef TAIL_MID
#undef TAIL_END
    OPIN("s_nop 15");
    int lane2;
    asm volatile("v_mbcnt_lo_u32_b32 %0, -1, 0\n\tv_mbcnt_hi_u32_b32 %0, -1, %0" : "=v"(lane2));

    const int l32e = lane2 & 31, hie = lane2 >> 5, r8e = lane2 >> 3, c8e = lane2 & 7, r16e = lane2 >> 4;
    STAMP(5);
    const int reg = TB ? (sub ? r2 : r1) + hsel * BLK : (wid < 3 ? wid * BLK : 3 * BLK + r0);
    l = swapsum(l);
    if (__builtin_expect(__any(!(l < LBIG)), 0)) {
        const unsigned mb = ldsb + reg;
        const int dr2 = (lane2 >> 2) & 7, dh2 = lane2 >> 5, dc2 = lane2 & 3, dg2 = (lane2 >> 4) & 1;
        const unsigned vK0 = dr2 * KROW + 64u * dh2 + 16u * (dc2 ^ dg2), vK1_ = dr2 * KROW + 64u * dh2 + 16u * (dc2 ^ (2 + dg2)), vV0 = dr2 * KROW + 64u * dh2 + 16u * dc2;
        kb[0] = lp + reg + (l32e >> 3) * 1024 + (l32e & 7) * 64 + 16 * (hie ^ ((l32e >> 2) & 3));
        kb[1] = lp + reg + (l32e >> 3) * 1024 + (l32e & 7) * 64 + 16 * ((2 + hie) ^ ((l32e >> 2) & 3));
        vb = lp + reg + (4 * hie + ((lane2 & 15) >> 2)) * 64 + 32 * ((lane2 >> 4) & 1) + 8 * (lane2 & 3);
        vbo = vb + (TB ? 512 - sub * 512 : 512); vb += TB ? sub * 512 : 0;
        bf16x8 q2[12];
        {
            const unsigned vQ0 = dr2 * QROW + 64u * dh2 + 16u * (dc2 ^ dg2), vQ1 = dr2 * QROW + 64u * dh2 + 16u * (dc2 ^ (2 + dg2));
            const unsigned qs = (unsigned)(q0 + (TB ? 0 : 32 * sub)) * QROW + hsel * 384;
#pragma unroll
            for (int g = 0; g < 4; ++g) dma3(mb + g * 1024, (g & 1) ? vQ1 : vQ0, srdQ, qs + g * 8 * QROW);
            WAIT_VM(0);
#pragma unroll
            for (int ks = 0; ks < 12; ++ks) q2[ks] = ldk(KADDR(ks));
        }
        auto scores = [&](int t) {
            const int b = TB ? 2 * t + sub : t;
            asm volatile("s_waitcnt lgkmcnt(0)" ::: "memory");
#pragma unroll
            for (int g = 0; g < 4; ++g) dma3(mb + g * 1024, (g & 1) ? vK1_ : vK0, srdK, b * KBLK + g * 8 * KROW);
            WAIT_VM(0);
            S = MFMA(ldk(KADDR(0)), q2[0], f32x16{});
#pragma unroll
            for (int ks = 1; ks < 12; ++ks) S = MFMA(ldk(KADDR(ks)), q2[ks], S);
            if (t == diagu) {
                const int k0 = b * 32 + 4 * hie - (q0 + (TB ? 0 : 32 * sub) + l32e);
#pragma unroll
                for (int r = 0; r < 16; ++r) if (k0 + (r & 3) + 8 * (r >> 2) > 0) S[r] = -__builtin_inff();
            }
        };
        float mx = -__builtin_inff();
#pragma unroll 1
        for (int t = 0; t < nu; ++t) { scores(t); asm volatile("s_nop 15" : "+v"(S)); mx = max2f(mx, rowmax()); }
        m = mx * C2;
#pragma unroll
        for (int i = 0; i < 6; ++i) o[i] = f32x16{};
        l = 0.f;
#pragma unroll 1
        for (int t = 0; t < nu; ++t) {
            scores(t);
            const int b = TB ? 2 * t + sub : t;
            asm volatile("s_waitcnt lgkmcnt(0)" ::: "memory");
#pragma unroll
            for (int g = 0; g < 4; ++g) dma3(mb + g * 1024, vV0, srdV, b * KBLK + g * 8 * KROW);
            float ps = 0.f;
#pragma unroll
            for (int e = 0; e < 16; ++e) { X[e] = __builtin_amdgcn_exp2f(__builtin_fmaf(S[e], C2, -m)); ps += X[e]; }
            l += ps;
            p0 = (u32x4){cvtpk(X[0], X[1]), cvtpk(X[2], X[3]), cvtpk(X[4], X[5]), cvtpk(X[6], X[7])};
            p1 = (u32x4){cvtpk(X[8], X[9]), cvtpk(X[10], X[11]), cvtpk(X[12], X[13]), cvtpk(X[14], X[15])};
            WAIT_VM(0);
            #pragma unroll
            for (int k = 0; k < 12; ++k) { VRD(k); o[k % 6] = MFMA(VFR(k), __builtin_bit_cast(bf16x8, k < 6 ? p0 : p1), o[k % 6]); }
        }
        l = swapsum(l);
        asm volatile("s_waitcnt lgkmcnt(0)" ::: "memory");
    }

    const __amdgpu_buffer_rsrc_t srdO = __builtin_amdgcn_make_buffer_rsrc((void*)((char*)O + (size_t)s0 * QROW + kvh * 768), (short)0, (int)((unsigned)L * QROW - kvh * 768), 0x00020000);
    const lds_ptr lw = (lds_ptr)lds;
    const unsigned vO = r8e * QROW + c8e * 16;
#define STG(base, db, g, lo, hi_) *(__attribute__((address_space(3))) u32x2*)((base) + ((db) >> 1) * 4096 + 16 * (((4 * (db) + (g)) & 7) ^ (l32e & 7))) = (u32x2){lo, hi_}
    if constexpr (!TB) {
        if (nu > 0) {
            float il = __builtin_amdgcn_rcpf(l);
            asm volatile("s_nop 1" : "+v"(il));
            const lds_ptr wp = lw + reg + l32e * 128 + 8 * hie;
            const lds_ptr rp = lw + reg + r8e * 128 + 16 * (c8e ^ r8e);
            const unsigned ob = (unsigned)(q0 + 32 * sub) * QROW + hsel * 384;
            u32x4 w[4];
            auto scale = [&](int db) {
                float y[16];
#pragma unroll
                for (int r = 0; r < 16; ++r) y[r] = vmul(o[db][r], il);
                unsigned c[8];
#pragma unroll
                for (int r = 0; r < 8; ++r) c[r] = vcvt(y[2 * r], y[2 * r + 1]);
#pragma unroll
                for (int g = 0; g < 4; ++g) STG(wp, db, g, c[2 * g], c[2 * g + 1]);
                SBAR();
            };
            auto rd = [&](int s) {
#pragma unroll
                for (int g = 0; g < 4; ++g) w[g] = *(const __attribute__((address_space(3))) u32x4*)(rp + s * 4096 + g * 1024);
                SBAR();
            };
            auto st_ = [&](int s) {
#pragma unroll
                for (int g = 0; g < 4; ++g) __builtin_amdgcn_raw_buffer_store_b128(w[g], srdO, vO + s * 128, ob + g * 8 * QROW, 0);
                SBAR();
            };
            scale(0); scale(1); rd(0); scale(2); st_(0); scale(3); rd(1); scale(4); st_(1); scale(5); rd(2); st_(2);
        }
    } else {
        const lds_ptr xp = lw + reg + lane2 * 16;
        const lds_ptr xq = lw + (sub ? r1 : r2) + hsel * BLK + lane2 * 16;
        __attribute__((address_space(3))) f32x2* ml = (__attribute__((address_space(3))) f32x2*)(lw + LDS_MISC);
        ml[wid * 64 + lane2] = (f32x2){m, l};
#pragma unroll
        for (int i = 0; i < 3; ++i)
#pragma unroll
            for (int g = 0; g < 4; ++g) *(__attribute__((address_space(3))) f32x4*)(xp + (4 * i + g) * 1024) = (f32x4){o[2 * i + 1][4 * g], o[2 * i + 1][4 * g + 1], o[2 * i + 1][4 * g + 2], o[2 * i + 1][4 * g + 3]};
        BARRIER();
        const f32x2 pm = ml[(wid ^ 2) * 64 + lane2];
        f32x4 x[12];
#pragma unroll
        for (int i = 0; i < 12; ++i) x[i] = *(const __attribute__((address_space(3))) f32x4*)(xq + i * 1024);
        SBAR();
        const float mm = max2f(m, pm[0]);
        const float fa = __builtin_amdgcn_exp2f(m - mm), fb = __builtin_amdgcn_exp2f(pm[0] - mm);
        const float il = __builtin_amdgcn_rcpf(l * fa + pm[1] * fb);
        float wa = fa * il, wb2 = fb * il;
        asm volatile("s_nop 1" : "+v"(wa), "+v"(wb2));
        const lds_ptr wp = lw + r0 + hsel * BLK + l32e * 128 + 8 * hie + 16 * ((4 * sub) ^ (l32e & 4));
#pragma unroll
        for (int i = 0; i < 3; ++i) {
            float y[16];
#pragma unroll
            for (int r = 0; r < 16; ++r) y[r] = vmul(x[4 * i + (r >> 2)][r & 3], wb2);
#pragma unroll
            for (int r = 0; r < 16; ++r) y[r] = vfma_v(o[2 * i][r], wa, y[r]);
            unsigned c[8];
#pragma unroll
            for (int r = 0; r < 8; ++r) c[r] = vcvt(y[2 * r], y[2 * r + 1]);
#pragma unroll
            for (int g = 0; g < 4; ++g) *(__attribute__((address_space(3))) u32x2*)(wp + i * 4096 + 16 * (g ^ (l32e & 3))) = (u32x2){c[2 * g], c[2 * g + 1]};
            SBAR();
        }
        BARRIER();
        const lds_ptr rp = lw + r0 + hsel * BLK + sub * 2048 + r8e * 128 + 16 * (c8e ^ r8e);
        const unsigned ob = (unsigned)(q0 + 16 * sub) * QROW + hsel * 384;
        u32x4 w[6];
#pragma unroll
        for (int i = 0; i < 6; ++i) w[i] = *(const __attribute__((address_space(3))) u32x4*)(rp + (i % 3) * 4096 + (i / 3) * 1024);
#pragma unroll
        for (int i = 0; i < 6; ++i) __builtin_amdgcn_raw_buffer_store_b128(w[i], srdO, vO + (i % 3) * 128, ob + (i / 3) * 8 * QROW, 0);
    }
#undef STG
    STAMP(6);
    asm volatile("s_waitcnt vmcnt(0)" ::: "memory");
    STAMP(7);
    st[10] = nu; st[11] = NU; st[12] = TB;
#undef EXP
#undef FM
#undef KRD
#undef VRD
#undef VFR
#undef QK
#undef PV
#undef ADD0
#undef ADD1
#undef ROTATE
#undef BK_WAIT
#undef CV
#undef KADDR
}

DI int wave_max63(int x) {
    asm volatile("s_nop 1\n\tv_max_i32_dpp %0, %0, %0 row_shr:1 row_mask:0xf bank_mask:0xf\n\ts_nop 1\n\tv_max_i32_dpp %0, %0, %0 row_shr:2 row_mask:0xf bank_mask:0xf\n\t"
                 "s_nop 1\n\tv_max_i32_dpp %0, %0, %0 row_shr:4 row_mask:0xf bank_mask:0xf\n\ts_nop 1\n\tv_max_i32_dpp %0, %0, %0 row_shr:8 row_mask:0xf bank_mask:0xf\n\t"
                 "s_nop 1\n\tv_max_i32_dpp %0, %0, %0 row_bcast:15 row_mask:0xa bank_mask:0xf\n\ts_nop 1\n\tv_max_i32_dpp %0, %0, %0 row_bcast:31 row_mask:0xc bank_mask:0xf\n\ts_nop 0" : "+v"(x));
    return x;
}
constexpr int first_pure_b() { int i = TH; while (WB * ((i + 2) >> 1) < WA * TH) ++i; return i; }
constexpr int I0 = first_pure_b();
constexpr int NMIX = (I0 - TH) + TH / 2;
static_assert(NMIX <= 12 && I0 < 60, "level table (16 levels; thresholds are inline constants)");
constexpr int level(int k) {
    if (k < 4) return (I0 + 3 - k) | 0x10000;
    k -= 4;
    if (k >= NMIX) return (int)0xffff0040;
    int i = I0 - 1, jj = TH / 2 - 1, out = 0;
    for (int lev = 0; lev <= k; ++lev) {
        const bool useB = i >= TH && (jj < 0 || WB * ((i + 2) >> 1) >= WA * (2 * jj + 2));
        if (useB) { out = i | 0x10000; --i; } else { out = 2 * jj; --jj; }
    }
    return out;
}

__global__ __launch_bounds__(256, 2) void attn_v11(const unsigned short* __restrict__ Q, const unsigned short* __restrict__ K,
                                                  const unsigned short* __restrict__ V, const int* __restrict__ cu,
                                                  unsigned short* __restrict__ O, int nseq, int T, unsigned long long* dbg) {
    extern __shared__ __attribute__((aligned(16))) char lds[];
    unsigned long long st[16];
    STAMP(0);
    const int lane = threadIdx.x & 63;
    const int kvh = blockIdx.x & 7, j = blockIdx.x >> 3;
    const int wv = rfl(threadIdx.x >> 6);
    unsigned vp, vq;
    LaneAddr la;
    {
        const unsigned li = (j * 4 + wv) * 32 + (lane & 31), tk = li / 3u;
        vp = (tk * KROW + (li - 3u * tk) * 128u) | ((PFB && lane < 32 && j < 64) ? 0u : OOB);
        const unsigned lq = (j * 4 + wv) * 64 + lane, tq = lq / 6u;
        vq = (tq * QROW + (lq - 6u * tq) * 128u) | ((PFQ && j < 64) ? 0u : OOB);
        const int dr = (lane >> 2) & 7, dh = lane >> 5, dc = lane & 3, dg = (lane >> 4) & 1;
        la.vK = (8 * wv + dr) * KROW + 64u * dh + 16u * (dc ^ ((2 * wv + dg) & 3));
        la.vV = (8 * wv + dr) * KROW + 64u * dh + 16u * dc;
        la.vQ0 = dr * QROW + 64u * dh + 16u * (dc ^ dg);
        la.vQ1 = dr * QROW + 64u * dh + 16u * (dc ^ (2 + dg));
        asm volatile("" : "+v"(vp), "+v"(vq), "+v"(la.vK), "+v"(la.vV), "+v"(la.vQ0), "+v"(la.vQ1));
    }
    asm volatile("" :: "s"(Q), "s"(K), "s"(V), "s"(O), "s"(cu), "s"(nseq), "s"(T));
    int r = j < 32 ? j : (j < 64 ? 95 - j : j);
    int a, b;
    if (nseq <= 15) {
        const unsigned pm = (unsigned)(uintptr_t)lds + LDS_MISC + wv * 256;
        const __amdgpu_buffer_rsrc_t srdKa = __builtin_amdgcn_make_buffer_rsrc((void*)((const char*)K + kvh * 384), (short)0, (int)((unsigned)T * KROW - kvh * 384), 0x00020000);
        const __amdgpu_buffer_rsrc_t srdVa = __builtin_amdgcn_make_buffer_rsrc((void*)((const char*)V + kvh * 384), (short)0, (int)((unsigned)T * KROW - kvh * 384), 0x00020000);
        const __amdgpu_buffer_rsrc_t srdQa = __builtin_amdgcn_make_buffer_rsrc((void*)((const char*)Q + kvh * 768), (short)0, (int)((unsigned)T * QROW - kvh * 768), 0x00020000);
        i32x16 c;
        if (PFQ) asm volatile("s_load_dwordx16 %0, %1, 0x0\n\ts_mov_b32 m0, %2\n\ts_nop 0\n\tbuffer_load_dword %6, %7, 0 offen lds\n\tbuffer_load_dword %3, %4, 0 offen lds\n\tbuffer_load_dword %3, %5, 0 offen lds\n\ts_waitcnt lgkmcnt(0)"
                     : "=&s"(c) : "s"(cu), "s"(pm), "v"(vp), "s"(srdKa), "s"(srdVa), "v"(vq), "s"(srdQa) : "memory");
        else asm volatile("s_load_dwordx16 %0, %1, 0x0\n\ts_mov_b32 m0, %2\n\ts_nop 0\n\tbuffer_load_dword %3, %4, 0 offen lds\n\tbuffer_load_dword %3, %5, 0 offen lds\n\ts_waitcnt lgkmcnt(0)"
                     : "=&s"(c) : "s"(cu), "s"(pm), "v"(vp), "s"(srdKa), "s"(srdVa) : "memory");
        asm volatile("v_mov_b32 %0, %1\n\tv_writelane_b32 %0, %2, 1\n\tv_writelane_b32 %0, %3, 2\n\tv_writelane_b32 %0, %4, 3\n\tv_writelane_b32 %0, %5, 4\n\tv_writelane_b32 %0, %6, 5\n\t"
                     "v_writelane_b32 %0, %7, 6\n\tv_writelane_b32 %0, %8, 7\n\tv_writelane_b32 %0, %9, 8\n\tv_writelane_b32 %0, %10, 9\n\tv_writelane_b32 %0, %11, 10\n\t"
                     "v_writelane_b32 %0, %12, 11\n\tv_writelane_b32 %0, %13, 12\n\tv_writelane_b32 %0, %14, 13\n\tv_writelane_b32 %0, %15, 14\n\tv_writelane_b32 %0, %16, 15\n\ts_nop 1"
                     : "=&v"(a) : "s"(c[0]), "s"(c[1]), "s"(c[2]), "s"(c[3]), "s"(c[4]), "s"(c[5]), "s"(c[6]), "s"(c[7]), "s"(c[8]), "s"(c[9]), "s"(c[10]), "s"(c[11]), "s"(c[12]), "s"(c[13]), "s"(c[14]), "s"(c[15]));
        b = __builtin_amdgcn_update_dpp(a, a, 0x101, 0xf, 0xf, false);
    } else {
        u32x2 ab;
        asm volatile("global_load_dwordx2 %0, %1, %2\n\ts_waitcnt vmcnt(0)" : "=&v"(ab) : "v"(min(lane, nseq - 1) * 4), "s"(cu) : "memory");
        a = ab[0]; b = ab[1];
    }
    const int n = lane < nseq ? (b - a + 31) >> 5 : 0;
    int code = -1;
    unsigned long long msk = 0;
    if (__builtin_expect(__ballot(n > I0 + 4) != 0, 0)) {
        const int nmax = __builtin_amdgcn_readlane(wave_max63(n), 63);
        for (int i = nmax - 1; i >= I0 + 4; --i) {
            msk = __ballot(n > i);
            const int c = __popcll(msk);
            if (r < c) { code = i | 0x10000; goto found; }
            r -= c;
        }
    }
    {
        int cnt;
        asm volatile("v_cmp_lt_i32_e64 %0, (%c5&65535), %4\n\ts_bcnt1_i32_b64 %2, %0\n\ts_mov_b32 %3, %c5\n\ts_cmp_lt_u32 %1, %2\n\ts_cbranch_scc1 1f\n\ts_sub_u32 %1, %1, %2\n\t"
                     "v_cmp_lt_i32_e64 %0, (%c6&65535), %4\n\ts_bcnt1_i32_b64 %2, %0\n\ts_mov_b32 %3, %c6\n\ts_cmp_lt_u32 %1, %2\n\ts_cbranch_scc1 1f\n\ts_sub_u32 %1, %1, %2\n\t"
                     "v_cmp_lt_i32_e64 %0, (%c7&65535), %4\n\ts_bcnt1_i32_b64 %2, %0\n\ts_mov_b32 %3, %c7\n\ts_cmp_lt_u32 %1, %2\n\ts_cbranch_scc1 1f\n\ts_sub_u32 %1, %1, %2\n\t"
                     "v_cmp_lt_i32_e64 %0, (%c8&65535), %4\n\ts_bcnt1_i32_b64 %2, %0\n\ts_mov_b32 %3, %c8\n\ts_cmp_lt_u32 %1, %2\n\ts_cbranch_scc1 1f\n\ts_sub_u32 %1, %1, %2\n\t"
                     "v_cmp_lt_i32_e64 %0, (%c9&65535), %4\n\ts_bcnt1_i32_b64 %2, %0\n\ts_mov_b32 %3, %c9\n\ts_cmp_lt_u32 %1, %2\n\ts_cbranch_scc1 1f\n\ts_sub_u32 %1, %1, %2\n\t"
                     "v_cmp_lt_i32_e64 %0, (%c10&65535), %4\n\ts_bcnt1_i32_b64 %2, %0\n\ts_mov_b32 %3, %c10\n\ts_cmp_lt_u32 %1, %2\n\ts_cbranch_scc1 1f\n\ts_sub_u32 %1, %1, %2\n\t"
                     "v_cmp_lt_i32_e64 %0, (%c11&65535), %4\n\ts_bcnt1_i32_b64 %2, %0\n\ts_mov_b32 %3, %c11\n\ts_cmp_lt_u32 %1, %2\n\ts_cbranch_scc1 1f\n\ts_sub_u32 %1, %1, %2\n\t"
                     "v_cmp_lt_i32_e64 %0, (%c12&65535), %4\n\ts_bcnt1_i32_b64 %2, %0\n\ts_mov_b32 %3, %c12\n\ts_cmp_lt_u32 %1, %2\n\ts_cbranch_scc1 1f\n\ts_sub_u32 %1, %1, %2\n\t"
                     "v_cmp_lt_i32_e64 %0, (%c13&65535), %4\n\ts_bcnt1_i32_b64 %2, %0\n\ts_mov_b32 %3, %c13\n\ts_cmp_lt_u32 %1, %2\n\ts_cbranch_scc1 1f\n\ts_sub_u32 %1, %1, %2\n\t"
                     "v_cmp_lt_i32_e64 %0, (%c14&65535), %4\n\ts_bcnt1_i32_b64 %2, %0\n\ts_mov_b32 %3, %c14\n\ts_cmp_lt_u32 %1, %2\n\ts_cbranch_scc1 1f\n\ts_sub_u32 %1, %1, %2\n\t"
                     "v_cmp_lt_i32_e64 %0, (%c15&65535), %4\n\ts_bcnt1_i32_b64 %2, %0\n\ts_mov_b32 %3, %c15\n\ts_cmp_lt_u32 %1, %2\n\ts_cbranch_scc1 1f\n\ts_sub_u32 %1, %1, %2\n\t"
                     "v_cmp_lt_i32_e64 %0, (%c16&65535), %4\n\ts_bcnt1_i32_b64 %2, %0\n\ts_mov_b32 %3, %c16\n\ts_cmp_lt_u32 %1, %2\n\ts_cbranch_scc1 1f\n\ts_sub_u32 %1, %1, %2\n\t"
                     "v_cmp_lt_i32_e64 %0, (%c17&65535), %4\n\ts_bcnt1_i32_b64 %2, %0\n\ts_mov_b32 %3, %c17\n\ts_cmp_lt_u32 %1, %2\n\ts_cbranch_scc1 1f\n\ts_sub_u32 %1, %1, %2\n\t"
                     "v_cmp_lt_i32_e64 %0, (%c18&65535), %4\n\ts_bcnt1_i32_b64 %2, %0\n\ts_mov_b32 %3, %c18\n\ts_cmp_lt_u32 %1, %2\n\ts_cbranch_scc1 1f\n\ts_sub_u32 %1, %1, %2\n\t"
                     "v_cmp_lt_i32_e64 %0, (%c19&65535), %4\n\ts_bcnt1_i32_b64 %2, %0\n\ts_mov_b32 %3, %c19\n\ts_cmp_lt_u32 %1, %2\n\ts_cbranch_scc1 1f\n\ts_sub_u32 %1, %1, %2\n\t"
                     "v_cmp_lt_i32_e64 %0, (%c20&65535), %4\n\ts_bcnt1_i32_b64 %2, %0\n\ts_mov_b32 %3, %c20\n\ts_cmp_lt_u32 %1, %2\n\ts_cbranch_scc1 1f\n\ts_sub_u32 %1, %1, %2\n\t"
                     "s_mov_b32 %3, -1\n1:"
                     : "=&s"(msk), "+s"(r), "=&s"(cnt), "=&s"(code) : "v"(n), "i"(level(0)), "i"(level(1)), "i"(level(2)), "i"(level(3)), "i"(level(4)), "i"(level(5)), "i"(level(6)), "i"(level(7)), "i"(level(8)), "i"(level(9)), "i"(level(10)), "i"(level(11)), "i"(level(12)), "i"(level(13)), "i"(level(14)), "i"(level(15)) : "scc");
    }
found:
    if (__builtin_expect(code < 0, 0)) { asm volatile("s_waitcnt vmcnt(0)" ::: "memory"); return; }
    const int tb = code >> 16, idx = (code & 0xffff) >> (1 - tb);
    const unsigned below = __builtin_amdgcn_mbcnt_hi((unsigned)(msk >> 32), __builtin_amdgcn_mbcnt_lo((unsigned)msk, 0u));
    const unsigned long long hit = __ballot(below == (unsigned)r) & msk;
    const int sl = __builtin_ctzll(hit);
    const int s0 = min(max(__builtin_amdgcn_readlane(a, sl), 0), T), L = min(__builtin_amdgcn_readlane(b, sl), T) - s0;
    if (__builtin_expect(L <= 0, 0)) { asm volatile("s_waitcnt vmcnt(0)" ::: "memory"); return; }
    if (DELAY2 > 0 && j >= 32) __builtin_amdgcn_s_sleep(DELAY2);
    STAMP(1);
    if (tb) attn_item<true>(st, Q, K, V, O, kvh, s0, L, idx, lds, wv, la);
    else attn_item<false>(st, Q, K, V, O, kvh, s0, L, idx, lds, wv, la);
    if (j >= JLO && j < JHI && (threadIdx.x >> 6) == 3) {
        unsigned long long d = AMPL == 1 ? st[7] - st[0] : AMPL == 2 ? st[4] - st[0] : AMPL == 3 ? st[5] - st[4] : AMPL == 4 ? st[7] - st[5] : 0;
        const unsigned long long until = __builtin_amdgcn_s_memrealtime() + 10 * d;
        while (__builtin_amdgcn_s_memrealtime() < until) __builtin_amdgcn_s_sleep(2);
    }
}

extern "C" void kernel_launch(void* const* d_in, const int* in_sizes, int n_in, void* d_out, int out_size, void* d_ws, size_t ws_size, hipStream_t stream) {
    const int T = in_sizes[0] / (HQ * D), nseq = in_sizes[3] - 1;
    int slots = (T + 31) / 32 + nseq;
    if (slots < 64) slots = 64;
    (void)hipFuncSetAttribute((const void*)attn_v11, hipFuncAttributeMaxDynamicSharedMemorySize, LDS_BYTES);
    attn_v11<<<slots * 8, 256, LDS_BYTES, stream>>>((const unsigned short*)d_in[0], (const unsigned short*)d_in[1], (const unsigned short*)d_in[2],
                                                 (const int*)d_in[3], (unsigned short*)d_out, nseq, T, (unsigned long long*)d_ws);
}
```

```cpp
#include <hip/hip_runtime.h>
#include <hip/hip_bf16.h>
#include <stdint.h>

typedef short bf16x8 __attribute__((ext_vector_type(8)));
typedef short s16x4 __attribute__((ext_vector_type(4)));
typedef float f32x16 __attribute__((ext_vector_type(16)));
typedef float f32x4 __attribute__((ext_vector_type(4)));
typedef float f32x2 __attribute__((ext_vector_type(2)));
typedef unsigned u32x4 __attribute__((ext_vector_type(4)));
typedef unsigned u32x2 __attribute__((ext_vector_type(2)));
typedef int i32x16 __attribute__((ext_vector_type(16)));
typedef __attribute__((address_space(3))) const char* lds_cptr;
typedef __attribute__((address_space(3))) char* lds_ptr;
typedef short v4i16_t __attribute__((ext_vector_type(4)));

constexpr int HQ = 16, HKV = 8, D = 192;
constexpr unsigned QROW = HQ * D * 2, KROW = HKV * D * 2;
constexpr unsigned KBLK = 32 * KROW;
constexpr int BLK = 12288;
constexpr int LDS_MISC = 6 * BLK;
constexpr int LDS_BYTES = LDS_MISC + 2048;
constexpr float C2 = 0.07216878364870322f * 1.4426950408889634f;
constexpr float LBIG = 1073741824.f;
#define TH 8
#define PFKV 0
#define PFFIRST 0
#define PFSKIP 0
#define STAUX 16
#define PFB 1
#define DELAY2 0
#define PFQ 0
#define WA 10
#define WB 11
constexpr unsigned LDS_NOWHERE = 0x30000u;
constexpr unsigned OOB = 0x80000000u;

#define DI __device__ __forceinline__
#define MFMA(a, b, c) __builtin_amdgcn_mfma_f32_32x32x16_bf16(a, b, c, 0, 0, 0)
#define SBAR() __builtin_amdgcn_sched_barrier(0)
#define PIN(x) asm volatile("" : "+v"(x))
typedef __bf16 bf16x2_t __attribute__((ext_vector_type(2)));
DI unsigned cvtpk(float lo, float hi) { const f32x2 v = {lo, hi}; return __builtin_bit_cast(unsigned, __builtin_convertvector(v, bf16x2_t)); }
DI float max3f(float a, float b, float c) { float r; asm("v_max3_f32 %0, %1, %2, %3" : "=v"(r) : "v"(a), "v"(b), "v"(c)); return r; }
DI float max2f(float a, float b) { float r; asm("v_max_f32_e32 %0, %1, %2" : "=v"(r) : "v"(a), "v"(b)); return r; }
DI float fmul_s(float a, float b) { float r; asm("v_mul_f32_e32 %0, %1, %2" : "=v"(r) : "v"(a), "v"(b)); return r; }
DI float fma_v(float a, float b, float c) { float r; asm("v_fma_f32 %0, %1, %2, %3" : "=v"(r) : "v"(a), "v"(b), "v"(c)); return r; }
DI void vexp(float& x) { asm volatile("v_exp_f32_e32 %0, %0" : "+v"(x)); }
DI float vadd(float a, float b) { float r; asm volatile("v_add_f32_e32 %0, %1, %2" : "=v"(r) : "v"(a), "v"(b)); return r; }
DI float vfma(float a, float b, float c) { float r; asm volatile("v_fma_f32 %0, %1, %2, %3" : "=v"(r) : "v"(a), "s"(b), "v"(c)); return r; }
DI float vmul(float a, float b) { float r; asm volatile("v_mul_f32_e32 %0, %1, %2" : "=v"(r) : "v"(a), "v"(b)); return r; }
DI float vfma_v(float a, float b, float c) { float r; asm volatile("v_fma_f32 %0, %1, %2, %3" : "=v"(r) : "v"(a), "v"(b), "v"(c)); return r; }
DI unsigned vcvt(float lo, float hi) { unsigned r; asm volatile("v_cvt_pk_bf16_f32 %0, %1, %2" : "=v"(r) : "v"(lo), "v"(hi)); return r; }
DI s16x4 vtr(lds_cptr p) { return __builtin_bit_cast(s16x4, __builtin_amdgcn_ds_read_tr16_b64_v4i16((__attribute__((address_space(3))) v4i16_t*)p)); }
DI bf16x8 ldk(lds_cptr p) { return *(const __attribute__((address_space(3))) bf16x8*)p; }
DI int rfl(int x) { return __builtin_amdgcn_readfirstlane(x); }
DI float swapmax(float x) { auto r = __builtin_amdgcn_permlane32_swap(__float_as_uint(x), __float_as_uint(x), false, false); return max2f(__uint_as_float(r[0]), __uint_as_float(r[1])); }
DI float swapsum(float x) { auto r = __builtin_amdgcn_permlane32_swap(__float_as_uint(x), __float_as_uint(x), false, false); return __uint_as_float(r[0]) + __uint_as_float(r[1]); }

template <int S, int OFF> DI void dma(unsigned mb, unsigned voff, __amdgpu_buffer_rsrc_t srd, unsigned soff) {
    asm volatile("s_add_u32 m0, %0, %5\n\ts_nop 0\n\tbuffer_load_dwordx4 %1, %2, %3 offen offset:%4 lds"
                 :: "s"(mb), "v"(voff), "s"(srd), "s"(soff), "i"(128 * S), "i"(OFF + S * (4096 - 128)) : "memory", "scc");
}
template <int S, int OFF> DI void setm0(unsigned mb) { asm volatile("s_add_u32 m0, %0, %1" :: "s"(mb), "i"(OFF + S * (4096 - 128)) : "scc"); __builtin_amdgcn_sched_barrier(0); }
template <int S> DI void ldm0(unsigned voff, __amdgpu_buffer_rsrc_t srd, unsigned soff) {
    asm volatile("buffer_load_dwordx4 %0, %1, %2 offen offset:%3 lds" :: "v"(voff), "s"(srd), "s"(soff), "i"(128 * S) : "memory");
}
DI void dma3(unsigned mb, unsigned voff, __amdgpu_buffer_rsrc_t srd, unsigned soff) { dma<0, 0>(mb, voff, srd, soff); dma<1, 0>(mb, voff, srd, soff); dma<2, 0>(mb, voff, srd, soff); }
#define DMAL dma
#define LOOPBAR() BARRIER()
#define LOOPWAIT() WAIT_VM(6)
#define LOOPSYNC() asm volatile("s_waitcnt vmcnt(6) lgkmcnt(0)\n\ts_barrier" ::: "memory")
#define M0 setm0
#define LDL ldm0
#define WAIT_VM(n) asm volatile("s_waitcnt vmcnt(" #n ")" ::: "memory")
#define BARRIER() asm volatile("s_waitcnt lgkmcnt(0)\n\ts_barrier" ::: "memory")
#define OPIN(nop) asm volatile(nop : "+v"(o[0]), "+v"(o[1]), "+v"(o[2]), "+v"(o[3]), "+v"(o[4]), "+v"(o[5]))

#define STAMP(i)
#define STAMPC(i)

struct LaneAddr { unsigned vK, vV, vQ0, vQ1; };

template <bool TB>
DI void attn_item(unsigned long long* st, const unsigned short* __restrict__ Q, const unsigned short* __restrict__ K, const unsigned short* __restrict__ V,
                  unsigned short* __restrict__ O, int kvh, int s0, int L, int idx, char* lds, int wid, const LaneAddr& la) {
    const int tid = threadIdx.x, lane = tid & 63, l32 = lane & 31, hi = lane >> 5;
    const int hsel = wid & 1, sub = wid >> 1;
    const int n = (L + 31) >> 5;
    const int q0 = TB ? 32 * idx : 64 * idx;
    const int NB = TB ? idx + 1 : min(2 * idx + 2, n);
    const int NU = TB ? (NB + 1) >> 1 : NB;
    const int nu = TB ? (NB + 1 - sub) >> 1 : (2 * idx + sub < n ? 2 * idx + sub + 1 : 0);
    const int diagu = TB ? (((idx & 1) == sub) ? nu - 1 : -1) : nu - 1;

    const unsigned krec = (unsigned)min(L, 32 * NB) * KROW - kvh * 384;
    const __amdgpu_buffer_rsrc_t srdK = __builtin_amdgcn_make_buffer_rsrc((void*)((const char*)K + (size_t)s0 * KROW + kvh * 384), (short)0, (int)krec, 0x00020000);
    const __amdgpu_buffer_rsrc_t srdQ = __builtin_amdgcn_make_buffer_rsrc((void*)((const char*)Q + (size_t)s0 * QROW + kvh * 768), (short)0, (int)((unsigned)L * QROW - kvh * 768), 0x00020000);
    const unsigned ldsb = (unsigned)(uintptr_t)lds;
    const unsigned wb = ldsb + wid * 1024;
    const unsigned vK = la.vK, vV = la.vV;

    dma3(wb, vK, srdK, 0);
    if (TB) dma3(wb + BLK, vK, srdK, KBLK);
    {
        const unsigned qreg = TB ? 4 * BLK + hsel * BLK : (wid == 3 ? 2 * BLK : (3 + wid) * BLK);
        const unsigned qs = (unsigned)(q0 + (TB ? 16 * sub : 32 * sub)) * QROW + hsel * 384;
        const unsigned m = ldsb + qreg + (TB ? 2048 * sub : 0);
#pragma unroll
        for (int g = 0; g < (TB ? 2 : 4); ++g) dma3(m + g * 1024, (g & 1) ? la.vQ1 : la.vQ0, srdQ, qs + g * 8 * QROW);
    }
    SBAR();
    const __amdgpu_buffer_rsrc_t srdV = __builtin_amdgcn_make_buffer_rsrc((void*)((const char*)V + (size_t)s0 * KROW + kvh * 384), (short)0, (int)krec, 0x00020000);
    if (PFKV) {
        const int per = TB ? 8 : 16, tk = (lane * 43) >> 7;
        const unsigned vp = tk < per ? (unsigned)(q0 + wid * per + tk) * KROW + (lane - 3 * tk) * 128 : OOB;
        const unsigned pm = ldsb + LDS_MISC + wid * 256;
        asm volatile("s_mov_b32 m0, %0\n\ts_nop 0\n\tbuffer_load_dword %1, %2, 0 offen lds" :: "s"(pm), "v"(vp), "s"(srdK) : "memory");
        asm volatile("s_mov_b32 m0, %0\n\ts_nop 0\n\tbuffer_load_dword %1, %2, 0 offen lds" :: "s"(pm), "v"(vp), "s"(srdV) : "memory");
    }
    const int qrel = q0 + (TB ? 0 : 32 * sub) + l32;
    const lds_cptr lp = (lds_cptr)lds;
    lds_cptr kb[2], vb, vbo;
#pragma unroll
    for (int i = 0; i < 2; ++i) kb[i] = lp + (l32 >> 3) * 1024 + (l32 & 7) * 64 + 16 * ((2 * i + hi) ^ ((l32 >> 2) & 3));
    vb = lp + (4 * hi + ((lane & 15) >> 2)) * 64 + 32 * ((lane >> 4) & 1) + 8 * (lane & 3);
#define KADDR(ks) (kb[(ks) & 1] + ((ks) >> 2) * 4096 + (((ks) >> 1) & 1) * 512)
    f32x16 o[6];
#pragma unroll
    for (int i = 0; i < 6; ++i) o[i] = f32x16{};
    OPIN("");

    STAMP(2);
    bf16x8 qf[12];
    if (PFKV) { WAIT_VM(2); } else { WAIT_VM(0); }
    if (TB) BARRIER();
    {
        const int qo = TB ? 4 * BLK + hsel * BLK : (wid == 3 ? 2 * BLK : (3 + wid) * BLK);
#pragma unroll
        for (int ks = 0; ks < 12; ++ks) {
            qf[ks] = ldk(KADDR(ks) + qo);
            if (TB) {
                if (ks == 1) dma<0, 0>(wb + 2 * BLK, vK, srdK, 2 * KBLK); if (ks == 3) dma<1, 0>(wb + 2 * BLK, vK, srdK, 2 * KBLK); if (ks == 5) dma<2, 0>(wb + 2 * BLK, vK, srdK, 2 * KBLK);
                if (ks == 7) dma<0, 0>(wb + 3 * BLK, vK, srdK, 3 * KBLK); if (ks == 9) dma<1, 0>(wb + 3 * BLK, vK, srdK, 3 * KBLK); if (ks == 11) dma<2, 0>(wb + 3 * BLK, vK, srdK, 3 * KBLK);
            } else {
                if (ks == 3) dma<0, 0>(wb + BLK, vK, srdK, KBLK); if (ks == 7) dma<1, 0>(wb + BLK, vK, srdK, KBLK); if (ks == 11) dma<2, 0>(wb + BLK, vK, srdK, KBLK);
            }
        }
    }
    BARRIER();
    STAMP(3);
    kb[0] += TB ? sub * BLK : 0; kb[1] += TB ? sub * BLK : 0;
    vb += TB ? sub * BLK : 3 * BLK;
    vbo = vb + (TB ? 512 - sub * 512 : 512);
    vb += TB ? sub * 512 : 0;

    f32x16 S, X;
    float m = 0.f, l = 0.f;
    auto mask = [&](int kblock) {
        const int d = qrel - kblock * 32 - 4 * hi;
        const float ninf = -__builtin_inff();
        unsigned long long k[16];
#define MK(r) asm volatile("v_cmp_le_i32_e64 %0, %2, %1" : "=s"(k[r]) : "v"(d), "i"(((r) & 3) + 8 * ((r) >> 2)))
#define MS(r) do { float t_ = S[r]; asm volatile("v_cndmask_b32_e64 %0, %1, %0, %2" : "+v"(t_) : "v"(ninf), "s"(k[r])); S[r] = t_; } while (0)
        MK(0); MK(1); MK(2); MK(3); MK(4); MK(5); MK(6); MK(7); MK(8); MK(9); MK(10); MK(11); MK(12); MK(13); MK(14); MK(15);
        MS(0); MS(1); MS(2); MS(3); MS(4); MS(5); MS(6); MS(7); MS(8); MS(9); MS(10); MS(11); MS(12); MS(13); MS(14); MS(15);
#undef MK
#undef MS
    };
    auto rowmax = [&]() -> float {
        const float a = max3f(S[0], S[1], S[2]), b = max3f(S[3], S[4], S[5]), c = max3f(S[6], S[7], S[8]), d = max3f(S[9], S[10], S[11]), e = max3f(S[12], S[13], S[14]);
        return swapmax(max2f(max3f(a, b, S[15]), max3f(c, d, e)));
    };
    const unsigned vK1 = vK + KBLK, vV1 = vV + KBLK;
    u32x4 p0, p1;
    bf16x8 kf[6];
    s16x4 vlo[6], vhi[6];
#define EXP(e) do { float t_ = X[e]; vexp(t_); X[e] = t_; } while (0)
#define FM(e) X[e] = vfma(S[e], C2, nm)
#define KRD(k) do { kf[(k) % 6] = ldk(KADDR(k)); SBAR(); } while (0)
#define VRD(k) do { const lds_cptr vp_ = ((k) & 1 ? vbo : vb) + (((k) % 6) >> 1) * 4096 + ((k) / 6) * 2048; vlo[(k) % 6] = vtr(vp_); vhi[(k) % 6] = vtr(vp_ + 1024); SBAR(); } while (0)
#define VFR(k) ((bf16x8){vlo[(k) % 6][0], vlo[(k) % 6][1], vlo[(k) % 6][2], vlo[(k) % 6][3], vhi[(k) % 6][0], vhi[(k) % 6][1], vhi[(k) % 6][2], vhi[(k) % 6][3]})
#define QK(k) do { S = MFMA(kf[(k) % 6], qf[k], (k) == 0 ? f32x16{} : S); PIN(S); SBAR(); } while (0)
#define PV(k) do { o[(k) % 6] = MFMA(VFR(k), __builtin_bit_cast(bf16x8, (k) < 6 ? p0 : p1), o[(k) % 6]); PIN(o[(k) % 6]); SBAR(); } while (0)
#define ADD0(e) ps0 = vadd(ps0, X[e])
#define ADD1(e) ps1 = vadd(ps1, X[e])
#define CV(P, i, e) P[i] = vcvt(X[e], X[(e) + 1])

    int r0 = 0, r1 = TB ? 2 * BLK : BLK, r2 = TB ? 4 * BLK : 2 * BLK;
    const unsigned mV1 = (TB || 1 < NB) ? wb + (TB ? 5 : 4) * BLK : LDS_NOWHERE, mK2 = 2 < NB ? wb + 2 * BLK : LDS_NOWHERE;
    if (nu > 0) {
        KRD(1); KRD(0); KRD(3); KRD(2); KRD(5); KRD(4);
        if (TB) {
            QK(0); dma<0, 0>(wb + 4 * BLK, vV, srdV, 0); SBAR();
            QK(1); KRD(7); SBAR();
            QK(2); KRD(6); dma<1, 0>(wb + 4 * BLK, vV, srdV, 0); SBAR();
            QK(3); KRD(9); SBAR();
            QK(4); KRD(8); dma<2, 0>(wb + 4 * BLK, vV, srdV, 0); SBAR();
            QK(5); KRD(11); SBAR();
            QK(6); KRD(10); dma<0, 0>(mV1, vV, srdV, KBLK); SBAR();
            QK(7);
            QK(8); dma<1, 0>(mV1, vV, srdV, KBLK); SBAR();
            QK(9);
            QK(10); dma<2, 0>(mV1, vV, srdV, KBLK); SBAR();
            QK(11);
        } else {
            QK(0); dma<0, 0>(wb + 3 * BLK, vV, srdV, 0); SBAR();
            QK(1); KRD(7); dma<1, 0>(wb + 3 * BLK, vV, srdV, 0); SBAR();
            QK(2); KRD(6); dma<2, 0>(wb + 3 * BLK, vV, srdV, 0); SBAR();
            QK(3); KRD(9); SBAR();
            QK(4); KRD(8); dma<0, 0>(mV1, vV, srdV, KBLK); SBAR();
            QK(5); KRD(11); dma<1, 0>(mV1, vV, srdV, KBLK); SBAR();
            QK(6); KRD(10); dma<2, 0>(mV1, vV, srdV, KBLK); SBAR();
            QK(7);
            QK(8); dma<0, 0>(mK2, vK, srdK, 2 * KBLK); SBAR();
            QK(9); dma<1, 0>(mK2, vK, srdK, 2 * KBLK); SBAR();
            QK(10); dma<2, 0>(mK2, vK, srdK, 2 * KBLK); SBAR();
            QK(11);
        }
    } else {
        dma3(wb + 3 * BLK, vV, srdV, 0); dma3(mV1, vV, srdV, KBLK); dma3(mK2, vK, srdK, 2 * KBLK);
    }
    WAIT_VM(6); BARRIER();
    kb[0] += r1; kb[1] += r1; if (TB) { vb += r2; vbo += r2; }
    if (nu > 1) { KRD(1); KRD(0); KRD(3); KRD(2); KRD(5); KRD(4); }
    if (TB) { dma<0, 0>(wb + r0, vK, srdK, 4 * KBLK); dma<1, 0>(wb + r0, vK, srdK, 4 * KBLK); }
    if (nu > 0) {
        asm volatile("s_nop 11" : "+v"(S));
        if (diagu == 0) mask(TB ? sub : 0);
        m = rowmax() * C2;
        const float nm = -m;
        FM(0); FM(1); FM(2); FM(3); FM(4); FM(5); FM(6); FM(7); FM(8); FM(9); FM(10); FM(11); FM(12); FM(13); FM(14); FM(15);
        EXP(0); EXP(1); EXP(2); EXP(3);
    } else X = f32x16{};
    const float nm = -m;

#define ROTATE() do { const int t_ = TB ? r2 : r0; if (TB) { r2 = r1; r1 = r0; r0 = t_; } else { r0 = r1; r1 = r2; r2 = t_; } so += (TB ? 2 : 1) * KBLK; ++u; } while (0)
#define BK_WAIT() LOOPWAIT()
    STAMP(4); STAMPC(8);
    unsigned barw = 0, barn = 0; (void)barw; (void)barn;
    int u = 0;
    unsigned so = 0;
    while (u + 1 < nu) {
        float ps0, ps1;
        const unsigned soK = so + (TB ? 4 : 3) * KBLK, soV = so + 2 * KBLK;
        const unsigned mK = (TB || u + 3 < NU) ? wb + r0 : LDS_NOWHERE, mV = TB ? wb + r1 : (u + 2 < NU ? wb + 3 * BLK + r2 : LDS_NOWHERE);
        const int dk = TB ? r0 - r1 : r2 - r1, dv = TB ? r1 - r2 : r1 - r0;
        if (TB) {
            QK(0);  EXP(4); EXP(5); ps0 = vadd(X[0], X[1]); SBAR();
            M0<2, 0>(mK); QK(1); LDL<2>(vK, srdK, soK); KRD(7); EXP(6); SBAR();
            QK(2);  KRD(6); EXP(7); EXP(8); ps1 = vadd(X[2], X[3]); SBAR();
            M0<0, BLK>(mK); QK(3); LDL<0>(vK1, srdK, soK); KRD(9); EXP(9); SBAR();
            QK(4);  KRD(8); EXP(10); CV(p0, 0, 0); ADD0(4); ADD1(5); SBAR();
            M0<1, BLK>(mK); QK(5); LDL<1>(vK1, srdK, soK); KRD(11); EXP(11); SBAR();
            QK(6);  KRD(10); EXP(12); CV(p0, 1, 2); CV(p0, 2, 4); SBAR();
            M0<2, BLK>(mK); QK(7); LDL<2>(vK1, srdK, soK); EXP(13); ADD0(6); SBAR();
            QK(8);
            LOOPSYNC();
            VRD(1); VRD(0); VRD(3); VRD(2);
            M0<0, 0>(mV); QK(9); LDL<0>(vV, srdV, soV); VRD(5); EXP(14); SBAR();
            QK(10); VRD(4); EXP(15); CV(p0, 3, 6); ADD1(7); ADD0(8); SBAR();
            M0<1, 0>(mV); QK(11); LDL<1>(vV, srdV, soV); ADD1(9); CV(p1, 0, 8); ADD0(10); SBAR();
            PV(0);  ADD1(11); CV(p1, 1, 10); ADD0(12); ADD1(13); CV(p1, 2, 12); SBAR();
            M0<2, 0>(mV); PV(1); LDL<2>(vV, srdV, soV); VRD(7); ADD0(14); SBAR();
            if (__builtin_expect(u + 1 == diagu, 0)) { asm volatile("s_nop 7" : "+v"(S)); mask(2 * u + 2 + sub); }
            PV(2);  VRD(6); ADD1(15); CV(p1, 3, 14); FM(0); FM(1); SBAR();
            M0<0, BLK>(mV); PV(3); LDL<0>(vV1, srdV, soV); VRD(9); FM(2); SBAR();
            PV(4);  VRD(8); FM(3); FM(4); FM(5); ps0 = vadd(ps0, ps1); SBAR();
            M0<1, BLK>(mV); PV(5); LDL<1>(vV1, srdV, soV); VRD(11); FM(6); SBAR();
            PV(6);  VRD(10); FM(7); FM(8); FM(9); l = vadd(l, ps0); SBAR();
            M0<2, BLK>(mV); PV(7); LDL<2>(vV1, srdV, soV); FM(10); FM(11); kb[0] += dk; kb[1] += dk; PIN(kb[0]); PIN(kb[1]); SBAR();
            PV(8);
        } else {
            QK(0);  EXP(4); EXP(5); ps0 = vadd(X[0], X[1]); SBAR();
            QK(1);  KRD(7); EXP(6); EXP(7); ps1 = vadd(X[2], X[3]); SBAR();
            QK(2);  KRD(6); EXP(8); CV(p0, 0, 0); ADD0(4); ADD1(5); SBAR();
            M0<0, 0>(mK); QK(3); LDL<0>(vK, srdK, soK); KRD(9); EXP(9); SBAR();
            QK(4);  KRD(8); EXP(10); CV(p0, 1, 2); CV(p0, 2, 4); SBAR();
            M0<1, 0>(mK); QK(5); LDL<1>(vK, srdK, soK); KRD(11); EXP(11); SBAR();
            QK(6);  KRD(10); EXP(12); ADD0(6); CV(p0, 3, 6); ADD1(7); SBAR();
            M0<2, 0>(mK); QK(7); LDL<2>(vK, srdK, soK); VRD(1); EXP(13); SBAR();
            QK(8);  VRD(0); EXP(14); ADD0(8); ADD1(9); SBAR();
            QK(9);  VRD(3); EXP(15); CV(p1, 0, 8); SBAR();
            QK(10); VRD(2); ADD0(10); ADD1(11); CV(p1, 1, 10); SBAR();
            QK(11); VRD(5); ADD0(12); ADD1(13); CV(p1, 2, 12); SBAR();
            M0<0, 0>(mV); PV(0); LDL<0>(vV, srdV, soV); VRD(4); ADD0(14); SBAR();
            PV(1);  VRD(7); ADD1(15); CV(p1, 3, 14); SBAR();
            if (__builtin_expect(u + 1 == diagu, 0)) { asm volatile("s_nop 7" : "+v"(S)); mask(u + 1); }
            M0<1, 0>(mV); PV(2); LDL<1>(vV, srdV, soV); VRD(6); FM(0); SBAR();
            PV(3);  VRD(9); FM(1); FM(2); FM(3); ps0 = vadd(ps0, ps1); SBAR();
            M0<2, 0>(mV); PV(4); LDL<2>(vV, srdV, soV); VRD(8); FM(4); SBAR();
            PV(5);  VRD(11); FM(5); FM(6); FM(7); l = vadd(l, ps0); SBAR();
            PV(6);  VRD(10); FM(8); FM(9); FM(10); SBAR();
            PV(7);  FM(11); FM(12); FM(13); kb[0] += dk; kb[1] += dk; PIN(kb[0]); PIN(kb[1]); SBAR();
            PV(8);
        }
        LOOPSYNC();
        KRD(1); KRD(0); KRD(3); KRD(2);
        if (TB) {
            M0<0, 0>(wb + r2); PV(9); LDL<0>(vK, srdK, soK + 2 * KBLK); KRD(5); FM(12); FM(13); SBAR();
            PV(10); KRD(4); FM(14); FM(15); EXP(0); vb += dv; vbo += dv; PIN(vb); PIN(vbo); SBAR();
            M0<1, 0>(wb + r2); PV(11); LDL<1>(vK, srdK, soK + 2 * KBLK); EXP(1); EXP(2); EXP(3); SBAR();
        } else {
            PV(9);  KRD(5); FM(14); FM(15); EXP(0); SBAR();
            PV(10); KRD(4); EXP(1); EXP(2); vb += dv; vbo += dv; PIN(vb); PIN(vbo); SBAR();
            PV(11); EXP(3); SBAR();
        }
        ROTATE();
    }
    STAMPC(9);
#define TAIL_HEAD() \
        const bool last = u == NU - 1; \
        if (!last) { \
            if (TB) { dma<2, 0>(wb + r0, vK, srdK, so + 4 * KBLK); dma3(wb + r0 + BLK, vK1, srdK, so + 4 * KBLK); } \
            else { dma3(u + 3 < NU ? wb + r0 : LDS_NOWHERE, vK, srdK, so + 3 * KBLK); dma3(u + 2 < NU ? wb + 3 * BLK + r2 : LDS_NOWHERE, vV, srdV, so + 2 * KBLK); } \
        }
#define TAIL_MID() \
        if (TB) { if (last) { WAIT_VM(0); } else { WAIT_VM(6); } BARRIER(); }
#define TAIL_END() \
        if (!last) { BK_WAIT(); BARRIER(); if (TB) { dma<0, 0>(wb + r2, vK, srdK, so + 6 * KBLK); dma<1, 0>(wb + r2, vK, srdK, so + 6 * KBLK); } }
    if (u < nu) {
        TAIL_HEAD()
#pragma unroll
        for (int e = 4; e < 8; ++e) EXP(e);
        float ps0 = vadd(X[0], X[1]), ps1 = vadd(X[2], X[3]);
        CV(p0, 0, 0); CV(p0, 1, 2); EXP(8); EXP(9); CV(p0, 2, 4); CV(p0, 3, 6);
        TAIL_MID()
        VRD(1); VRD(0); VRD(3); VRD(2); VRD(5); VRD(4);
        EXP(10); EXP(11); EXP(12); EXP(13); SBAR();
        const unsigned mVt = last ? LDS_NOWHERE : wb + r1;
        PV(0);  if (TB) dma<0, 0>(mVt, vV, srdV, so + 2 * KBLK); EXP(14); EXP(15); SBAR();
        PV(1);  VRD(7); ADD0(4); ADD1(5); SBAR();
        PV(2);  VRD(6); if (TB) dma<1, 0>(mVt, vV, srdV, so + 2 * KBLK); CV(p1, 0, 8); CV(p1, 1, 10); SBAR();
        PV(3);  VRD(9); CV(p1, 2, 12); CV(p1, 3, 14); SBAR();
        PV(4);  VRD(8); if (TB) dma<2, 0>(mVt, vV, srdV, so + 2 * KBLK); ADD0(6); ADD1(7); SBAR();
        PV(5);  VRD(11); ADD0(8); ADD1(9); ADD0(10); SBAR();
        PV(6);  VRD(10); if (TB) dma<0, BLK>(mVt, vV1, srdV, so + 2 * KBLK); ADD1(11); ADD0(12); SBAR();
        PV(7);  if (TB) dma<1, BLK>(mVt, vV1, srdV, so + 2 * KBLK); ADD1(13); SBAR();
        PV(8);  if (TB) dma<2, BLK>(mVt, vV1, srdV, so + 2 * KBLK); ADD0(14); ADD1(15); SBAR();
        PV(9); PV(10); PV(11);
        l += ps0 + ps1;
        TAIL_END()
        ROTATE();
    }
    while (u < NU) {
        TAIL_HEAD()
        TAIL_MID()
        if (TB && !last) { dma3(wb + r1, vV, srdV, so + 2 * KBLK); dma3(wb + r1 + BLK, vV1, srdV, so + 2 * KBLK); }
        TAIL_END()
        ROTATE();
    }
#undef TAIL_HEAD
#undef TAIL_MID
#undef TAIL_END
    OPIN("s_nop 15");
    int lane2;
    asm volatile("v_mbcnt_lo_u32_b32 %0, -1, 0\n\tv_mbcnt_hi_u32_b32 %0, -1, %0" : "=v"(lane2));

    const int l32e = lane2 & 31, hie = lane2 >> 5, r8e = lane2 >> 3, c8e = lane2 & 7, r16e = lane2 >> 4;
    STAMP(5);
    const int reg = TB ? (sub ? r2 : r1) + hsel * BLK : (wid < 3 ? wid * BLK : 3 * BLK + r0);
    l = swapsum(l);
    if (__builtin_expect(__any(!(l < LBIG)), 0)) {
        const unsigned mb = ldsb + reg;
        const int dr2 = (lane2 >> 2) & 7, dh2 = lane2 >> 5, dc2 = lane2 & 3, dg2 = (lane2 >> 4) & 1;
        const unsigned vK0 = dr2 * KROW + 64u * dh2 + 16u * (dc2 ^ dg2), vK1_ = dr2 * KROW + 64u * dh2 + 16u * (dc2 ^ (2 + dg2)), vV0 = dr2 * KROW + 64u * dh2 + 16u * dc2;
        kb[0] = lp + reg + (l32e >> 3) * 1024 + (l32e & 7) * 64 + 16 * (hie ^ ((l32e >> 2) & 3));
        kb[1] = lp + reg + (l32e >> 3) * 1024 + (l32e & 7) * 64 + 16 * ((2 + hie) ^ ((l32e >> 2) & 3));
        vb = lp + reg + (4 * hie + ((lane2 & 15) >> 2)) * 64 + 32 * ((lane2 >> 4) & 1) + 8 * (lane2 & 3);
        vbo = vb + (TB ? 512 - sub * 512 : 512); vb += TB ? sub * 512 : 0;
        bf16x8 q2[12];
        {
            const unsigned vQ0 = dr2 * QROW + 64u * dh2 + 16u * (dc2 ^ dg2), vQ1 = dr2 * QROW + 64u * dh2 + 16u * (dc2 ^ (2 + dg2));
            const unsigned qs = (unsigned)(q0 + (TB ? 0 : 32 * sub)) * QROW + hsel * 384;
#pragma unroll
            for (int g = 0; g < 4; ++g) dma3(mb + g * 1024, (g & 1) ? vQ1 : vQ0, srdQ, qs + g * 8 * QROW);
            WAIT_VM(0);
#pragma unroll
            for (int ks = 0; ks < 12; ++ks) q2[ks] = ldk(KADDR(ks));
        }
        auto scores = [&](int t) {
            const int b = TB ? 2 * t + sub : t;
            asm volatile("s_waitcnt lgkmcnt(0)" ::: "memory");
#pragma unroll
            for (int g = 0; g < 4; ++g) dma3(mb + g * 1024, (g & 1) ? vK1_ : vK0, srdK, b * KBLK + g * 8 * KROW);
            WAIT_VM(0);
            S = MFMA(ldk(KADDR(0)), q2[0], f32x16{});
#pragma unroll
            for (int ks = 1; ks < 12; ++ks) S = MFMA(ldk(KADDR(ks)), q2[ks], S);
            if (t == diagu) {
                const int k0 = b * 32 + 4 * hie - (q0 + (TB ? 0 : 32 * sub) + l32e);
#pragma unroll
                for (int r = 0; r < 16; ++r) if (k0 + (r & 3) + 8 * (r >> 2) > 0) S[r] = -__builtin_inff();
            }
        };
        float mx = -__builtin_inff();
#pragma unroll 1
        for (int t = 0; t < nu; ++t) { scores(t); asm volatile("s_nop 15" : "+v"(S)); mx = max2f(mx, rowmax()); }
        m = mx * C2;
#pragma unroll
        for (int i = 0; i < 6; ++i) o[i] = f32x16{};
        l = 0.f;
#pragma unroll 1
        for (int t = 0; t < nu; ++t) {
            scores(t);
            const int b = TB ? 2 * t + sub : t;
            asm volatile("s_waitcnt lgkmcnt(0)" ::: "memory");
#pragma unroll
            for (int g = 0; g < 4; ++g) dma3(mb + g * 1024, vV0, srdV, b * KBLK + g * 8 * KROW);
            float ps = 0.f;
#pragma unroll
            for (int e = 0; e < 16; ++e) { X[e] = __builtin_amdgcn_exp2f(__builtin_fmaf(S[e], C2, -m)); ps += X[e]; }
            l += ps;
            p0 = (u32x4){cvtpk(X[0], X[1]), cvtpk(X[2], X[3]), cvtpk(X[4], X[5]), cvtpk(X[6], X[7])};
            p1 = (u32x4){cvtpk(X[8], X[9]), cvtpk(X[10], X[11]), cvtpk(X[12], X[13]), cvtpk(X[14], X[15])};
            WAIT_VM(0);
            #pragma unroll
            for (int k = 0; k < 12; ++k) { VRD(k); o[k % 6] = MFMA(VFR(k), __builtin_bit_cast(bf16x8, k < 6 ? p0 : p1), o[k % 6]); }
        }
        l = swapsum(l);
        asm volatile("s_waitcnt lgkmcnt(0)" ::: "memory");
    }

    const __amdgpu_buffer_rsrc_t srdO = __builtin_amdgcn_make_buffer_rsrc((void*)((char*)O + (size_t)s0 * QROW + kvh * 768), (short)0, (int)((unsigned)L * QROW - kvh * 768), 0x00020000);
    const lds_ptr lw = (lds_ptr)lds;
    const unsigned vO = r8e * QROW + c8e * 16;
#define STG(base, db, g, lo, hi_) *(__attribute__((address_space(3))) u32x2*)((base) + ((db) >> 1) * 4096 + 16 * (((4 * (db) + (g)) & 7) ^ (l32e & 7))) = (u32x2){lo, hi_}
    if constexpr (!TB) {
        if (nu > 0) {
            float il = __builtin_amdgcn_rcpf(l);
            asm volatile("s_nop 1" : "+v"(il));
            const lds_ptr wp = lw + reg + l32e * 128 + 8 * hie;
            const lds_ptr rp = lw + reg + r8e * 128 + 16 * (c8e ^ r8e);
            const unsigned ob = (unsigned)(q0 + 32 * sub) * QROW + hsel * 384;
            u32x4 w[4];
            auto scale = [&](int db) {
                float y[16];
#pragma unroll
                for (int r = 0; r < 16; ++r) y[r] = vmul(o[db][r], il);
                unsigned c[8];
#pragma unroll
                for (int r = 0; r < 8; ++r) c[r] = vcvt(y[2 * r], y[2 * r + 1]);
#pragma unroll
                for (int g = 0; g < 4; ++g) STG(wp, db, g, c[2 * g], c[2 * g + 1]);
                SBAR();
            };
            auto rd = [&](int s) {
#pragma unroll
                for (int g = 0; g < 4; ++g) w[g] = *(const __attribute__((address_space(3))) u32x4*)(rp + s * 4096 + g * 1024);
                SBAR();
            };
            auto st_ = [&](int s) {
#pragma unroll
                for (int g = 0; g < 4; ++g) __builtin_amdgcn_raw_buffer_store_b128(w[g], srdO, vO + s * 128, ob + g * 8 * QROW, STAUX);
                SBAR();
            };
            scale(0); scale(1); rd(0); scale(2); st_(0); scale(3); rd(1); scale(4); st_(1); scale(5); rd(2); st_(2);
        }
    } else {
        const lds_ptr xp = lw + reg + lane2 * 16;
        const lds_ptr xq = lw + (sub ? r1 : r2) + hsel * BLK + lane2 * 16;
        __attribute__((address_space(3))) f32x2* ml = (__attribute__((address_space(3))) f32x2*)(lw + LDS_MISC);
        ml[wid * 64 + lane2] = (f32x2){m, l};
#pragma unroll
        for (int i = 0; i < 3; ++i)
#pragma unroll
            for (int g = 0; g < 4; ++g) *(__attribute__((address_space(3))) f32x4*)(xp + (4 * i + g) * 1024) = (f32x4){o[2 * i + 1][4 * g], o[2 * i + 1][4 * g + 1], o[2 * i + 1][4 * g + 2], o[2 * i + 1][4 * g + 3]};
        BARRIER();
        const f32x2 pm = ml[(wid ^ 2) * 64 + lane2];
        f32x4 x[12];
#pragma unroll
        for (int i = 0; i < 12; ++i) x[i] = *(const __attribute__((address_space(3))) f32x4*)(xq + i * 1024);
        SBAR();
        const float mm = max2f(m, pm[0]);
        const float fa = __builtin_amdgcn_exp2f(m - mm), fb = __builtin_amdgcn_exp2f(pm[0] - mm);
        const float il = __builtin_amdgcn_rcpf(l * fa + pm[1] * fb);
        float wa = fa * il, wb2 = fb * il;
        asm volatile("s_nop 1" : "+v"(wa), "+v"(wb2));
        const lds_ptr wp = lw + r0 + hsel * BLK + l32e * 128 + 8 * hie + 16 * ((4 * sub) ^ (l32e & 4));
#pragma unroll
        for (int i = 0; i < 3; ++i) {
            float y[16];
#pragma unroll
            for (int r = 0; r < 16; ++r) y[r] = vmul(x[4 * i + (r >> 2)][r & 3], wb2);
#pragma unroll
            for (int r = 0; r < 16; ++r) y[r] = vfma_v(o[2 * i][r], wa, y[r]);
            unsigned c[8];
#pragma unroll
            for (int r = 0; r < 8; ++r) c[r] = vcvt(y[2 * r], y[2 * r + 1]);
#pragma unroll
            for (int g = 0; g < 4; ++g) *(__attribute__((address_space(3))) u32x2*)(wp + i * 4096 + 16 * (g ^ (l32e & 3))) = (u32x2){c[2 * g], c[2 * g + 1]};
            SBAR();
        }
        BARRIER();
        const lds_ptr rp = lw + r0 + hsel * BLK + sub * 2048 + r8e * 128 + 16 * (c8e ^ r8e);
        const unsigned ob = (unsigned)(q0 + 16 * sub) * QROW + hsel * 384;
        u32x4 w[6];
#pragma unroll
        for (int i = 0; i < 6; ++i) w[i] = *(const __attribute__((address_space(3))) u32x4*)(rp + (i % 3) * 4096 + (i / 3) * 1024);
#pragma unroll
        for (int i = 0; i < 6; ++i) __builtin_amdgcn_raw_buffer_store_b128(w[i], srdO, vO + (i % 3) * 128, ob + (i / 3) * 8 * QROW, STAUX);
    }
#undef STG
    STAMP(6);
#undef EXP
#undef FM
#undef KRD
#undef VRD
#undef VFR
#undef QK
#undef PV
#undef ADD0
#undef ADD1
#undef ROTATE
#undef BK_WAIT
#undef CV
#undef KADDR
}

DI int wave_max63(int x) {
    asm volatile("s_nop 1\n\tv_max_i32_dpp %0, %0, %0 row_shr:1 row_mask:0xf bank_mask:0xf\n\ts_nop 1\n\tv_max_i32_dpp %0, %0, %0 row_shr:2 row_mask:0xf bank_mask:0xf\n\t"
                 "s_nop 1\n\tv_max_i32_dpp %0, %0, %0 row_shr:4 row_mask:0xf bank_mask:0xf\n\ts_nop 1\n\tv_max_i32_dpp %0, %0, %0 row_shr:8 row_mask:0xf bank_mask:0xf\n\t"
                 "s_nop 1\n\tv_max_i32_dpp %0, %0, %0 row_bcast:15 row_mask:0xa bank_mask:0xf\n\ts_nop 1\n\tv_max_i32_dpp %0, %0, %0 row_bcast:31 row_mask:0xc bank_mask:0xf\n\ts_nop 0" : "+v"(x));
    return x;
}
constexpr int first_pure_b() { int i = TH; while (WB * ((i + 2) >> 1) < WA * TH) ++i; return i; }
constexpr int I0 = first_pure_b();
constexpr int NMIX = (I0 - TH) + TH / 2;
static_assert(NMIX <= 12 && I0 < 60, "level table (16 levels; thresholds are inline constants)");
constexpr int level(int k) {
    if (k < 4) return (I0 + 3 - k) | 0x10000;
    k -= 4;
    if (k >= NMIX) return (int)0xffff0040;
    int i = I0 - 1, jj = TH / 2 - 1, out = 0;
    for (int lev = 0; lev <= k; ++lev) {
        const bool useB = i >= TH && (jj < 0 || WB * ((i + 2) >> 1) >= WA * (2 * jj + 2));
        if (useB) { out = i | 0x10000; --i; } else { out = 2 * jj; --jj; }
    }
    return out;
}

__global__ __launch_bounds__(256, 2) void attn_v12(const unsigned short* __restrict__ Q, const unsigned short* __restrict__ K,
                                                  const unsigned short* __restrict__ V, const int* __restrict__ cu,
                                                  unsigned short* __restrict__ O, int nseq, int T, unsigned long long* dbg) {
    extern __shared__ __attribute__((aligned(16))) char lds[];
    unsigned long long st[16];
    STAMP(0);
    const int lane = threadIdx.x & 63;
    const int kvh = blockIdx.x & 7, j = blockIdx.x >> 3;
    const int wv = rfl(threadIdx.x >> 6);
    unsigned vp, vq;
    LaneAddr la;
    {
        const int jj = j & 31, pj = (j >> 5) * (32 - PFSKIP) + jj - PFSKIP;
        constexpr int LPW = (8192 + 4 * (64 - 2 * PFSKIP) - 1) / (4 * (64 - 2 * PFSKIP));
        const unsigned li = (pj * 4 + wv) * LPW + lane, tk = li / 3u;
        vp = (tk * KROW + (li - 3u * tk) * 128u) | ((PFB && lane < LPW && j < 64 && jj >= PFSKIP) ? 0u : OOB);
        const unsigned lq = (j * 4 + wv) * 64 + lane, tq = lq / 6u;
        vq = (tq * QROW + (lq - 6u * tq) * 128u) | ((PFQ && j < 64) ? 0u : OOB);
        const int dr = (lane >> 2) & 7, dh = lane >> 5, dc = lane & 3, dg = (lane >> 4) & 1;
        la.vK = (8 * wv + dr) * KROW + 64u * dh + 16u * (dc ^ ((2 * wv + dg) & 3));
        la.vV = (8 * wv + dr) * KROW + 64u * dh + 16u * dc;
        la.vQ0 = dr * QROW + 64u * dh + 16u * (dc ^ dg);
        la.vQ1 = dr * QROW + 64u * dh + 16u * (dc ^ (2 + dg));
        asm volatile("" : "+v"(vp), "+v"(vq), "+v"(la.vK), "+v"(la.vV), "+v"(la.vQ0), "+v"(la.vQ1));
    }
    asm volatile("" :: "s"(Q), "s"(K), "s"(V), "s"(O), "s"(cu), "s"(nseq), "s"(T));
    int r = j < 32 ? j : (j < 64 ? 95 - j : j);
    int a, b;
    if (nseq <= 15) {
        const unsigned pm = (unsigned)(uintptr_t)lds + LDS_MISC + wv * 256;
        const __amdgpu_buffer_rsrc_t srdKa = __builtin_amdgcn_make_buffer_rsrc((void*)((const char*)K + kvh * 384), (short)0, (int)((unsigned)T * KROW - kvh * 384), 0x00020000);
        const __amdgpu_buffer_rsrc_t srdVa = __builtin_amdgcn_make_buffer_rsrc((void*)((const char*)V + kvh * 384), (short)0, (int)((unsigned)T * KROW - kvh * 384), 0x00020000);
        const __amdgpu_buffer_rsrc_t srdQa = __builtin_amdgcn_make_buffer_rsrc((void*)((const char*)Q + kvh * 768), (short)0, (int)((unsigned)T * QROW - kvh * 768), 0x00020000);
        i32x16 c;
        if (PFQ) asm volatile("s_load_dwordx16 %0, %1, 0x0\n\ts_mov_b32 m0, %2\n\ts_nop 0\n\tbuffer_load_dword %6, %7, 0 offen lds\n\tbuffer_load_dword %3, %4, 0 offen lds\n\tbuffer_load_dword %3, %5, 0 offen lds\n\ts_waitcnt lgkmcnt(0)"
                     : "=&s"(c) : "s"(cu), "s"(pm), "v"(vp), "s"(srdKa), "s"(srdVa), "v"(vq), "s"(srdQa) : "memory");
        else asm volatile("s_load_dwordx16 %0, %1, 0x0\n\ts_mov_b32 m0, %2\n\ts_nop 0\n\tbuffer_load_dword %3, %4, 0 offen lds\n\tbuffer_load_dword %3, %5, 0 offen lds\n\ts_waitcnt lgkmcnt(0)"
                     : "=&s"(c) : "s"(cu), "s"(pm), "v"(vp), "s"(srdKa), "s"(srdVa) : "memory");
        asm volatile("v_mov_b32 %0, %1\n\tv_writelane_b32 %0, %2, 1\n\tv_writelane_b32 %0, %3, 2\n\tv_writelane_b32 %0, %4, 3\n\tv_writelane_b32 %0, %5, 4\n\tv_writelane_b32 %0, %6, 5\n\t"
                     "v_writelane_b32 %0, %7, 6\n\tv_writelane_b32 %0, %8, 7\n\tv_writelane_b32 %0, %9, 8\n\tv_writelane_b32 %0, %10, 9\n\tv_writelane_b32 %0, %11, 10\n\t"
                     "v_writelane_b32 %0, %12, 11\n\tv_writelane_b32 %0, %13, 12\n\tv_writelane_b32 %0, %14, 13\n\tv_writelane_b32 %0, %15, 14\n\tv_writelane_b32 %0, %16, 15\n\ts_nop 1"
                     : "=&v"(a) : "s"(c[0]), "s"(c[1]), "s"(c[2]), "s"(c[3]), "s"(c[4]), "s"(c[5]), "s"(c[6]), "s"(c[7]), "s"(c[8]), "s"(c[9]), "s"(c[10]), "s"(c[11]), "s"(c[12]), "s"(c[13]), "s"(c[14]), "s"(c[15]));
        b = __builtin_amdgcn_update_dpp(a, a, 0x101, 0xf, 0xf, false);
    } else {
        u32x2 ab;
        asm volatile("global_load_dwordx2 %0, %1, %2\n\ts_waitcnt vmcnt(0)" : "=&v"(ab) : "v"(min(lane, nseq - 1) * 4), "s"(cu) : "memory");
        a = ab[0]; b = ab[1];
    }
    const int n = lane < nseq ? (b - a + 31) >> 5 : 0;
    int code = -1;
    unsigned long long msk = 0;
    if (__builtin_expect(__ballot(n > I0 + 4) != 0, 0)) {
        const int nmax = __builtin_amdgcn_readlane(wave_max63(n), 63);
        for (int i = nmax - 1; i >= I0 + 4; --i) {
            msk = __ballot(n > i);
            const int c = __popcll(msk);
            if (r < c) { code = i | 0x10000; goto found; }
            r -= c;
        }
    }
    {
        int cnt;
        asm volatile("v_cmp_lt_i32_e64 %0, (%c5&65535), %4\n\ts_bcnt1_i32_b64 %2, %0\n\ts_mov_b32 %3, %c5\n\ts_cmp_lt_u32 %1, %2\n\ts_cbranch_scc1 1f\n\ts_sub_u32 %1, %1, %2\n\t"
                     "v_cmp_lt_i32_e64 %0, (%c6&65535), %4\n\ts_bcnt1_i32_b64 %2, %0\n\ts_mov_b32 %3, %c6\n\ts_cmp_lt_u32 %1, %2\n\ts_cbranch_scc1 1f\n\ts_sub_u32 %1, %1, %2\n\t"
                     "v_cmp_lt_i32_e64 %0, (%c7&65535), %4\n\ts_bcnt1_i32_b64 %2, %0\n\ts_mov_b32 %3, %c7\n\ts_cmp_lt_u32 %1, %2\n\ts_cbranch_scc1 1f\n\ts_sub_u32 %1, %1, %2\n\t"
                     "v_cmp_lt_i32_e64 %0, (%c8&65535), %4\n\ts_bcnt1_i32_b64 %2, %0\n\ts_mov_b32 %3, %c8\n\ts_cmp_lt_u32 %1, %2\n\ts_cbranch_scc1 1f\n\ts_sub_u32 %1, %1, %2\n\t"
                     "v_cmp_lt_i32_e64 %0, (%c9&65535), %4\n\ts_bcnt1_i32_b64 %2, %0\n\ts_mov_b32 %3, %c9\n\ts_cmp_lt_u32 %1, %2\n\ts_cbranch_scc1 1f\n\ts_sub_u32 %1, %1, %2\n\t"
                     "v_cmp_lt_i32_e64 %0, (%c10&65535), %4\n\ts_bcnt1_i32_b64 %2, %0\n\ts_mov_b32 %3, %c10\n\ts_cmp_lt_u32 %1, %2\n\ts_cbranch_scc1 1f\n\ts_sub_u32 %1, %1, %2\n\t"
                     "v_cmp_lt_i32_e64 %0, (%c11&65535), %4\n\ts_bcnt1_i32_b64 %2, %0\n\ts_mov_b32 %3, %c11\n\ts_cmp_lt_u32 %1, %2\n\ts_cbranch_scc1 1f\n\ts_sub_u32 %1, %1, %2\n\t"
                     "v_cmp_lt_i32_e64 %0, (%c12&65535), %4\n\ts_bcnt1_i32_b64 %2, %0\n\ts_mov_b32 %3, %c12\n\ts_cmp_lt_u32 %1, %2\n\ts_cbranch_scc1 1f\n\ts_sub_u32 %1, %1, %2\n\t"
                     "v_cmp_lt_i32_e64 %0, (%c13&65535), %4\n\ts_bcnt1_i32_b64 %2, %0\n\ts_mov_b32 %3, %c13\n\ts_cmp_lt_u32 %1, %2\n\ts_cbranch_scc1 1f\n\ts_sub_u32 %1, %1, %2\n\t"
                     "v_cmp_lt_i32_e64 %0, (%c14&65535), %4\n\ts_bcnt1_i32_b64 %2, %0\n\ts_mov_b32 %3, %c14\n\ts_cmp_lt_u32 %1, %2\n\ts_cbranch_scc1 1f\n\ts_sub_u32 %1, %1, %2\n\t"
                     "v_cmp_lt_i32_e64 %0, (%c15&65535), %4\n\ts_bcnt1_i32_b64 %2, %0\n\ts_mov_b32 %3, %c15\n\ts_cmp_lt_u32 %1, %2\n\ts_cbranch_scc1 1f\n\ts_sub_u32 %1, %1, %2\n\t"
                     "v_cmp_lt_i32_e64 %0, (%c16&65535), %4\n\ts_bcnt1_i32_b64 %2, %0\n\ts_mov_b32 %3, %c16\n\ts_cmp_lt_u32 %1, %2\n\ts_cbranch_scc1 1f\n\ts_sub_u32 %1, %1, %2\n\t"
                     "v_cmp_lt_i32_e64 %0, (%c17&65535), %4\n\ts_bcnt1_i32_b64 %2, %0\n\ts_mov_b32 %3, %c17\n\ts_cmp_lt_u32 %1, %2\n\ts_cbranch_scc1 1f\n\ts_sub_u32 %1, %1, %2\n\t"
                     "v_cmp_lt_i32_e64 %0, (%c18&65535), %4\n\ts_bcnt1_i32_b64 %2, %0\n\ts_mov_b32 %3, %c18\n\ts_cmp_lt_u32 %1, %2\n\ts_cbranch_scc1 1f\n\ts_sub_u32 %1, %1, %2\n\t"
                     "v_cmp_lt_i32_e64 %0, (%c19&65535), %4\n\ts_bcnt1_i32_b64 %2, %0\n\ts_mov_b32 %3, %c19\n\ts_cmp_lt_u32 %1, %2\n\ts_cbranch_scc1 1f\n\ts_sub_u32 %1, %1, %2\n\t"
                     "v_cmp_lt_i32_e64 %0, (%c20&65535), %4\n\ts_bcnt1_i32_b64 %2, %0\n\ts_mov_b32 %3, %c20\n\ts_cmp_lt_u32 %1, %2\n\ts_cbranch_scc1 1f\n\ts_sub_u32 %1, %1, %2\n\t"
                     "s_mov_b32 %3, -1\n1:"
                     : "=&s"(msk), "+s"(r), "=&s"(cnt), "=&s"(code) : "v"(n), "i"(level(0)), "i"(level(1)), "i"(level(2)), "i"(level(3)), "i"(level(4)), "i"(level(5)), "i"(level(6)), "i"(level(7)), "i"(level(8)), "i"(level(9)), "i"(level(10)), "i"(level(11)), "i"(level(12)), "i"(level(13)), "i"(level(14)), "i"(level(15)) : "scc");
    }
found:
    if (__builtin_expect(code < 0, 0)) { asm volatile("s_waitcnt vmcnt(0)" ::: "memory"); return; }
    const int tb = code >> 16, idx = (code & 0xffff) >> (1 - tb);
    const unsigned below = __builtin_amdgcn_mbcnt_hi((unsigned)(msk >> 32), __builtin_amdgcn_mbcnt_lo((unsigned)msk, 0u));
    const unsigned long long hit = __ballot(below == (unsigned)r) & msk;
    const int sl = __builtin_ctzll(hit);
    const int s0 = min(max(__builtin_amdgcn_readlane(a, sl), 0), T), L = min(__builtin_amdgcn_readlane(b, sl), T) - s0;
    if (__builtin_expect(L <= 0, 0)) { asm volatile("s_waitcnt vmcnt(0)" ::: "memory"); return; }
    if (DELAY2 > 0 && j >= 32) __builtin_amdgcn_s_sleep(DELAY2);
    STAMP(1);
    if (tb) attn_item<true>(st, Q, K, V, O, kvh, s0, L, idx, lds, wv, la);
    else attn_item<false>(st, Q, K, V, O, kvh, s0, L, idx, lds, wv, la);
}

extern "C" void kernel_launch(void* const* d_in, const int* in_sizes, int n_in, void* d_out, int out_size, void* d_ws, size_t ws_size, hipStream_t stream) {
    const int T = in_sizes[0] / (HQ * D), nseq = in_sizes[3] - 1;
    int slots = (T + 31) / 32 + nseq;
    if (slots < 64) slots = 64;
    (void)hipFuncSetAttribute((const void*)attn_v12, hipFuncAttributeMaxDynamicSharedMemorySize, LDS_BYTES);
    attn_v12<<<slots * 8, 256, LDS_BYTES, stream>>>((const unsigned short*)d_in[0], (const unsigned short*)d_in[1], (const unsigned short*)d_in[2],
                                                 (const int*)d_in[3], (unsigned short*)d_out, nseq, T, (unsigned long long*)d_ws);
}
```

```cpp
#include <hip/hip_runtime.h>
#include <hip/hip_bf16.h>
#include <stdint.h>

typedef short bf16x8 __attribute__((ext_vector_type(8)));
typedef short s16x4 __attribute__((ext_vector_type(4)));
typedef float f32x16 __attribute__((ext_vector_type(16)));
typedef float f32x4 __attribute__((ext_vector_type(4)));
typedef float f32x2 __attribute__((ext_vector_type(2)));
typedef unsigned u32x4 __attribute__((ext_vector_type(4)));
typedef unsigned u32x2 __attribute__((ext_vector_type(2)));
typedef int i32x16 __attribute__((ext_vector_type(16)));
typedef __attribute__((address_space(3))) const char* lds_cptr;
typedef __attribute__((address_space(3))) char* lds_ptr;
typedef short v4i16_t __attribute__((ext_vector_type(4)));

constexpr int HQ = 16, HKV = 8, D = 192;
constexpr unsigned QROW = HQ * D * 2, KROW = HKV * D * 2;
constexpr unsigned KBLK = 32 * KROW;
constexpr int BLK = 12288;
constexpr int LDS_MISC = 6 * BLK;
constexpr int LDS_BYTES = LDS_MISC + 2048;
constexpr float C2 = 0.07216878364870322f * 1.4426950408889634f;
constexpr float LBIG = 1073741824.f;
#define TH 8
#define PFKV 0
#define PFFIRST 0
#define PFSKIP 0
#define QNT 2
#define STAUX 17
#define PFB 1
#define DELAY2 0
#define PFQ 0
#define WA 10
#define WB 11
constexpr unsigned LDS_NOWHERE = 0x30000u;
constexpr unsigned OOB = 0x80000000u;

#define DI __device__ __forceinline__
#define MFMA(a, b, c) __builtin_amdgcn_mfma_f32_32x32x16_bf16(a, b, c, 0, 0, 0)
#define SBAR() __builtin_amdgcn_sched_barrier(0)
#define PIN(x) asm volatile("" : "+v"(x))
typedef __bf16 bf16x2_t __attribute__((ext_vector_type(2)));
DI unsigned cvtpk(float lo, float hi) { const f32x2 v = {lo, hi}; return __builtin_bit_cast(unsigned, __builtin_convertvector(v, bf16x2_t)); }
DI float max3f(float a, float b, float c) { float r; asm("v_max3_f32 %0, %1, %2, %3" : "=v"(r) : "v"(a), "v"(b), "v"(c)); return r; }
DI float max2f(float a, float b) { float r; asm("v_max_f32_e32 %0, %1, %2" : "=v"(r) : "v"(a), "v"(b)); return r; }
DI float fmul_s(float a, float b) { float r; asm("v_mul_f32_e32 %0, %1, %2" : "=v"(r) : "v"(a), "v"(b)); return r; }
DI float fma_v(float a, float b, float c) { float r; asm("v_fma_f32 %0, %1, %2, %3" : "=v"(r) : "v"(a), "v"(b), "v"(c)); return r; }
DI void vexp(float& x) { asm volatile("v_exp_f32_e32 %0, %0" : "+v"(x)); }
DI float vadd(float a, float b) { float r; asm volatile("v_add_f32_e32 %0, %1, %2" : "=v"(r) : "v"(a), "v"(b)); return r; }
DI float vfma(float a, float b, float c) { float r; asm volatile("v_fma_f32 %0, %1, %2, %3" : "=v"(r) : "v"(a), "s"(b), "v"(c)); return r; }
DI float vmul(float a, float b) { float r; asm volatile("v_mul_f32_e32 %0, %1, %2" : "=v"(r) : "v"(a), "v"(b)); return r; }
DI float vfma_v(float a, float b, float c) { float r; asm volatile("v_fma_f32 %0, %1, %2, %3" : "=v"(r) : "v"(a), "v"(b), "v"(c)); return r; }
DI unsigned vcvt(float lo, float hi) { unsigned r; asm volatile("v_cvt_pk_bf16_f32 %0, %1, %2" : "=v"(r) : "v"(lo), "v"(hi)); return r; }
DI s16x4 vtr(lds_cptr p) { return __builtin_bit_cast(s16x4, __builtin_amdgcn_ds_read_tr16_b64_v4i16((__attribute__((address_space(3))) v4i16_t*)p)); }
DI bf16x8 ldk(lds_cptr p) { return *(const __attribute__((address_space(3))) bf16x8*)p; }
DI int rfl(int x) { return __builtin_amdgcn_readfirstlane(x); }
DI float swapmax(float x) { auto r = __builtin_amdgcn_permlane32_swap(__float_as_uint(x), __float_as_uint(x), false, false); return max2f(__uint_as_float(r[0]), __uint_as_float(r[1])); }
DI float swapsum(float x) { auto r = __builtin_amdgcn_permlane32_swap(__float_as_uint(x), __float_as_uint(x), false, false); return __uint_as_float(r[0]) + __uint_as_float(r[1]); }

template <int S, int OFF> DI void dma(unsigned mb, unsigned voff, __amdgpu_buffer_rsrc_t srd, unsigned soff) {
    asm volatile("s_add_u32 m0, %0, %5\n\ts_nop 0\n\tbuffer_load_dwordx4 %1, %2, %3 offen offset:%4 lds"
                 :: "s"(mb), "v"(voff), "s"(srd), "s"(soff), "i"(128 * S), "i"(OFF + S * (4096 - 128)) : "memory", "scc");
}
template <int S, int OFF> DI void setm0(unsigned mb) { asm volatile("s_add_u32 m0, %0, %1" :: "s"(mb), "i"(OFF + S * (4096 - 128)) : "scc"); __builtin_amdgcn_sched_barrier(0); }
template <int S> DI void ldm0(unsigned voff, __amdgpu_buffer_rsrc_t srd, unsigned soff) {
    asm volatile("buffer_load_dwordx4 %0, %1, %2 offen offset:%3 lds" :: "v"(voff), "s"(srd), "s"(soff), "i"(128 * S) : "memory");
}
template <int S> DI void dmaq(unsigned mb, unsigned voff, __amdgpu_buffer_rsrc_t srd, unsigned soff) {
    if (QNT == 1) asm volatile("s_add_u32 m0, %0, %5\n\ts_nop 0\n\tbuffer_load_dwordx4 %1, %2, %3 offen offset:%4 nt lds" :: "s"(mb), "v"(voff), "s"(srd), "s"(soff), "i"(128 * S), "i"(S * (4096 - 128)) : "memory", "scc");
    else if (QNT == 2) asm volatile("s_add_u32 m0, %0, %5\n\ts_nop 0\n\tbuffer_load_dwordx4 %1, %2, %3 offen offset:%4 sc1 lds" :: "s"(mb), "v"(voff), "s"(srd), "s"(soff), "i"(128 * S), "i"(S * (4096 - 128)) : "memory", "scc");
    else asm volatile("s_add_u32 m0, %0, %5\n\ts_nop 0\n\tbuffer_load_dwordx4 %1, %2, %3 offen offset:%4 lds" :: "s"(mb), "v"(voff), "s"(srd), "s"(soff), "i"(128 * S), "i"(S * (4096 - 128)) : "memory", "scc");
}
DI void dma3(unsigned mb, unsigned voff, __amdgpu_buffer_rsrc_t srd, unsigned soff) { dma<0, 0>(mb, voff, srd, soff); dma<1, 0>(mb, voff, srd, soff); dma<2, 0>(mb, voff, srd, soff); }
#define DMAL dma
#define LOOPBAR() BARRIER()
#define LOOPWAIT() WAIT_VM(6)
#define LOOPSYNC() asm volatile("s_waitcnt vmcnt(6) lgkmcnt(0)\n\ts_barrier" ::: "memory")
#define M0 setm0
#define LDL ldm0
#define WAIT_VM(n) asm volatile("s_waitcnt vmcnt(" #n ")" ::: "memory")
#define BARRIER() asm volatile("s_waitcnt lgkmcnt(0)\n\ts_barrier" ::: "memory")
#define OPIN(nop) asm volatile(nop : "+v"(o[0]), "+v"(o[1]), "+v"(o[2]), "+v"(o[3]), "+v"(o[4]), "+v"(o[5]))

#define STAMP(i)
#define STAMPC(i)

struct LaneAddr { unsigned vK, vV, vQ0, vQ1; };

template <bool TB>
DI void attn_item(unsigned long long* st, const unsigned short* __restrict__ Q, const unsigned short* __restrict__ K, const unsigned short* __restrict__ V,
                  unsigned short* __restrict__ O, int kvh, int s0, int L, int idx, char* lds, int wid, const LaneAddr& la) {
    const int tid = threadIdx.x, lane = tid & 63, l32 = lane & 31, hi = lane >> 5;
    const int hsel = wid & 1, sub = wid >> 1;
    const int n = (L + 31) >> 5;
    const int q0 = TB ? 32 * idx : 64 * idx;
    const int NB = TB ? idx + 1 : min(2 * idx + 2, n);
    const int NU = TB ? (NB + 1) >> 1 : NB;
    const int nu = TB ? (NB + 1 - sub) >> 1 : (2 * idx + sub < n ? 2 * idx + sub + 1 : 0);
    const int diagu = TB ? (((idx & 1) == sub) ? nu - 1 : -1) : nu - 1;

    const unsigned krec = (unsigned)min(L, 32 * NB) * KROW - kvh * 384;
    const __amdgpu_buffer_rsrc_t srdK = __builtin_amdgcn_make_buffer_rsrc((void*)((const char*)K + (size_t)s0 * KROW + kvh * 384), (short)0, (int)krec, 0x00020000);
    const __amdgpu_buffer_rsrc_t srdQ = __builtin_amdgcn_make_buffer_rsrc((void*)((const char*)Q + (size_t)s0 * QROW + kvh * 768), (short)0, (int)((unsigned)L * QROW - kvh * 768), 0x00020000);
    const unsigned ldsb = (unsigned)(uintptr_t)lds;
    const unsigned wb = ldsb + wid * 1024;
    const unsigned vK = la.vK, vV = la.vV;

    dma3(wb, vK, srdK, 0);
    if (TB) dma3(wb + BLK, vK, srdK, KBLK);
    {
        const unsigned qreg = TB ? 4 * BLK + hsel * BLK : (wid == 3 ? 2 * BLK : (3 + wid) * BLK);
        const unsigned qs = (unsigned)(q0 + (TB ? 16 * sub : 32 * sub)) * QROW + hsel * 384;
        const unsigned m = ldsb + qreg + (TB ? 2048 * sub : 0);
#pragma unroll
        for (int g = 0; g < (TB ? 2 : 4); ++g) { const unsigned vq_ = (g & 1) ? la.vQ1 : la.vQ0; dmaq<0>(m + g * 1024, vq_, srdQ, qs + g * 8 * QROW); dmaq<1>(m + g * 1024, vq_, srdQ, qs + g * 8 * QROW); dmaq<2>(m + g * 1024, vq_, srdQ, qs + g * 8 * QROW); }
    }
    SBAR();
    const __amdgpu_buffer_rsrc_t srdV = __builtin_amdgcn_make_buffer_rsrc((void*)((const char*)V + (size_t)s0 * KROW + kvh * 384), (short)0, (int)krec, 0x00020000);
    if (PFKV) {
        const int per = TB ? 8 : 16, tk = (lane * 43) >> 7;
        const unsigned vp = tk < per ? (unsigned)(q0 + wid * per + tk) * KROW + (lane - 3 * tk) * 128 : OOB;
        const unsigned pm = ldsb + LDS_MISC + wid * 256;
        asm volatile("s_mov_b32 m0, %0\n\ts_nop 0\n\tbuffer_load_dword %1, %2, 0 offen lds" :: "s"(pm), "v"(vp), "s"(srdK) : "memory");
        asm volatile("s_mov_b32 m0, %0\n\ts_nop 0\n\tbuffer_load_dword %1, %2, 0 offen lds" :: "s"(pm), "v"(vp), "s"(srdV) : "memory");
    }
    const int qrel = q0 + (TB ? 0 : 32 * sub) + l32;
    const lds_cptr lp = (lds_cptr)lds;
    lds_cptr kb[2], vb, vbo;
#pragma unroll
    for (int i = 0; i < 2; ++i) kb[i] = lp + (l32 >> 3) * 1024 + (l32 & 7) * 64 + 16 * ((2 * i + hi) ^ ((l32 >> 2) & 3));
    vb = lp + (4 * hi + ((lane & 15) >> 2)) * 64 + 32 * ((lane >> 4) & 1) + 8 * (lane & 3);
#define KADDR(ks) (kb[(ks) & 1] + ((ks) >> 2) * 4096 + (((ks) >> 1) & 1) * 512)
    f32x16 o[6];
#pragma unroll
    for (int i = 0; i < 6; ++i) o[i] = f32x16{};
    OPIN("");

    STAMP(2);
    bf16x8 qf[12];
    if (PFKV) { WAIT_VM(2); } else { WAIT_VM(0); }
    if (TB) BARRIER();
    {
        const int qo = TB ? 4 * BLK + hsel * BLK : (wid == 3 ? 2 * BLK : (3 + wid) * BLK);
#pragma unroll
        for (int ks = 0; ks < 12; ++ks) {
            qf[ks] = ldk(KADDR(ks) + qo);
            if (TB) {
                if (ks == 1) dma<0, 0>(wb + 2 * BLK, vK, srdK, 2 * KBLK); if (ks == 3) dma<1, 0>(wb + 2 * BLK, vK, srdK, 2 * KBLK); if (ks == 5) dma<2, 0>(wb + 2 * BLK, vK, srdK, 2 * KBLK);
                if (ks == 7) dma<0, 0>(wb + 3 * BLK, vK, srdK, 3 * KBLK); if (ks == 9) dma<1, 0>(wb + 3 * BLK, vK, srdK, 3 * KBLK); if (ks == 11) dma<2, 0>(wb + 3 * BLK, vK, srdK, 3 * KBLK);
            } else {
                if (ks == 3) dma<0, 0>(wb + BLK, vK, srdK, KBLK); if (ks == 7) dma<1, 0>(wb + BLK, vK, srdK, KBLK); if (ks == 11) dma<2, 0>(wb + BLK, vK, srdK, KBLK);
            }
        }
    }
    BARRIER();
    STAMP(3);
    kb[0] += TB ? sub * BLK : 0; kb[1] += TB ? sub * BLK : 0;
    vb += TB ? sub * BLK : 3 * BLK;
    vbo = vb + (TB ? 512 - sub * 512 : 512);
    vb += TB ? sub * 512 : 0;

    f32x16 S, X;
    float m = 0.f, l = 0.f;
    auto mask = [&](int kblock) {
        const int d = qrel - kblock * 32 - 4 * hi;
        const float ninf = -__builtin_inff();
        unsigned long long k[16];
#define MK(r) asm volatile("v_cmp_le_i32_e64 %0, %2, %1" : "=s"(k[r]) : "v"(d), "i"(((r) & 3) + 8 * ((r) >> 2)))
#define MS(r) do { float t_ = S[r]; asm volatile("v_cndmask_b32_e64 %0, %1, %0, %2" : "+v"(t_) : "v"(ninf), "s"(k[r])); S[r] = t_; } while (0)
        MK(0); MK(1); MK(2); MK(3); MK(4); MK(5); MK(6); MK(7); MK(8); MK(9); MK(10); MK(11); MK(12); MK(13); MK(14); MK(15);
        MS(0); MS(1); MS(2); MS(3); MS(4); MS(5); MS(6); MS(7); MS(8); MS(9); MS(10); MS(11); MS(12); MS(13); MS(14); MS(15);
#undef MK
#undef MS
    };
    auto rowmax = [&]() -> float {
        const float a = max3f(S[0], S[1], S[2]), b = max3f(S[3], S[4], S[5]), c = max3f(S[6], S[7], S[8]), d = max3f(S[9], S[10], S[11]), e = max3f(S[12], S[13], S[14]);
        return swapmax(max2f(max3f(a, b, S[15]), max3f(c, d, e)));
    };
    const unsigned vK1 = vK + KBLK, vV1 = vV + KBLK;
    u32x4 p0, p1;
    bf16x8 kf[6];
    s16x4 vlo[6], vhi[6];
#define EXP(e) do { float t_ = X[e]; vexp(t_); X[e] = t_; } while (0)
#define FM(e) X[e] = vfma(S[e], C2, nm)
#define KRD(k) do { kf[(k) % 6] = ldk(KADDR(k)); SBAR(); } while (0)
#define VRD(k) do { const lds_cptr vp_ = ((k) & 1 ? vbo : vb) + (((k) % 6) >> 1) * 4096 + ((k) / 6) * 2048; vlo[(k) % 6] = vtr(vp_); vhi[(k) % 6] = vtr(vp_ + 1024); SBAR(); } while (0)
#define VFR(k) ((bf16x8){vlo[(k) % 6][0], vlo[(k) % 6][1], vlo[(k) % 6][2], vlo[(k) % 6][3], vhi[(k) % 6][0], vhi[(k) % 6][1], vhi[(k) % 6][2], vhi[(k) % 6][3]})
#define QK(k) do { S = MFMA(kf[(k) % 6], qf[k], (k) == 0 ? f32x16{} : S); PIN(S); SBAR(); } while (0)
#define PV(k) do { o[(k) % 6] = MFMA(VFR(k), __builtin_bit_cast(bf16x8, (k) < 6 ? p0 : p1), o[(k) % 6]); PIN(o[(k) % 6]); SBAR(); } while (0)
#define ADD0(e) ps0 = vadd(ps0, X[e])
#define ADD1(e) ps1 = vadd(ps1, X[e])
#define CV(P, i, e) P[i] = vcvt(X[e], X[(e) + 1])

    int r0 = 0, r1 = TB ? 2 * BLK : BLK, r2 = TB ? 4 * BLK : 2 * BLK;
    const unsigned mV1 = (TB || 1 < NB) ? wb + (TB ? 5 : 4) * BLK : LDS_NOWHERE, mK2 = 2 < NB ? wb + 2 * BLK : LDS_NOWHERE;
    if (nu > 0) {
        KRD(1); KRD(0); KRD(3); KRD(2); KRD(5); KRD(4);
        if (TB) {
            QK(0); dma<0, 0>(wb + 4 * BLK, vV, srdV, 0); SBAR();
            QK(1); KRD(7); SBAR();
            QK(2); KRD(6); dma<1, 0>(wb + 4 * BLK, vV, srdV, 0); SBAR();
            QK(3); KRD(9); SBAR();
            QK(4); KRD(8); dma<2, 0>(wb + 4 * BLK, vV, srdV, 0); SBAR();
            QK(5); KRD(11); SBAR();
            QK(6); KRD(10); dma<0, 0>(mV1, vV, srdV, KBLK); SBAR();
            QK(7);
            QK(8); dma<1, 0>(mV1, vV, srdV, KBLK); SBAR();
            QK(9);
            QK(10); dma<2, 0>(mV1, vV, srdV, KBLK); SBAR();
            QK(11);
        } else {
            QK(0); dma<0, 0>(wb + 3 * BLK, vV, srdV, 0); SBAR();
            QK(1); KRD(7); dma<1, 0>(wb + 3 * BLK, vV, srdV, 0); SBAR();
            QK(2); KRD(6); dma<2, 0>(wb + 3 * BLK, vV, srdV, 0); SBAR();
            QK(3); KRD(9); SBAR();
            QK(4); KRD(8); dma<0, 0>(mV1, vV, srdV, KBLK); SBAR();
            QK(5); KRD(11); dma<1, 0>(mV1, vV, srdV, KBLK); SBAR();
            QK(6); KRD(10); dma<2, 0>(mV1, vV, srdV, KBLK); SBAR();
            QK(7);
            QK(8); dma<0, 0>(mK2, vK, srdK, 2 * KBLK); SBAR();
            QK(9); dma<1, 0>(mK2, vK, srdK, 2 * KBLK); SBAR();
            QK(10); dma<2, 0>(mK2, vK, srdK, 2 * KBLK); SBAR();
            QK(11);
        }
    } else {
        dma3(wb + 3 * BLK, vV, srdV, 0); dma3(mV1, vV, srdV, KBLK); dma3(mK2, vK, srdK, 2 * KBLK);
    }
    WAIT_VM(6); BARRIER();
    kb[0] += r1; kb[1] += r1; if (TB) { vb += r2; vbo += r2; }
    if (nu > 1) { KRD(1); KRD(0); KRD(3); KRD(2); KRD(5); KRD(4); }
    if (TB) { dma<0, 0>(wb + r0, vK, srdK, 4 * KBLK); dma<1, 0>(wb + r0, vK, srdK, 4 * KBLK); }
    if (nu > 0) {
        asm volatile("s_nop 11" : "+v"(S));
        if (diagu == 0) mask(TB ? sub : 0);
        m = rowmax() * C2;
        const float nm = -m;
        FM(0); FM(1); FM(2); FM(3); FM(4); FM(5); FM(6); FM(7); FM(8); FM(9); FM(10); FM(11); FM(12); FM(13); FM(14); FM(15);
        EXP(0); EXP(1); EXP(2); EXP(3);
    } else X = f32x16{};
    const float nm = -m;

#define ROTATE() do { const int t_ = TB ? r2 : r0; if (TB) { r2 = r1; r1 = r0; r0 = t_; } else { r0 = r1; r1 = r2; r2 = t_; } so += (TB ? 2 : 1) * KBLK; ++u; } while (0)
#define BK_WAIT() LOOPWAIT()
    STAMP(4); STAMPC(8);
    unsigned barw = 0, barn = 0; (void)barw; (void)barn;
    int u = 0;
    unsigned so = 0;
    while (u + 1 < nu) {
        float ps0, ps1;
        const unsigned soK = so + (TB ? 4 : 3) * KBLK, soV = so + 2 * KBLK;
        const unsigned mK = (TB || u + 3 < NU) ? wb + r0 : LDS_NOWHERE, mV = TB ? wb + r1 : (u + 2 < NU ? wb + 3 * BLK + r2 : LDS_NOWHERE);
        const int dk = TB ? r0 - r1 : r2 - r1, dv = TB ? r1 - r2 : r1 - r0;
        if (TB) {
            QK(0);  EXP(4); EXP(5); ps0 = vadd(X[0], X[1]); SBAR();
            M0<2, 0>(mK); QK(1); LDL<2>(vK, srdK, soK); KRD(7); EXP(6); SBAR();
            QK(2);  KRD(6); EXP(7); EXP(8); ps1 = vadd(X[2], X[3]); SBAR();
            M0<0, BLK>(mK); QK(3); LDL<0>(vK1, srdK, soK); KRD(9); EXP(9); SBAR();
            QK(4);  KRD(8); EXP(10); CV(p0, 0, 0); ADD0(4); ADD1(5); SBAR();
            M0<1, BLK>(mK); QK(5); LDL<1>(vK1, srdK, soK); KRD(11); EXP(11); SBAR();
            QK(6);  KRD(10); EXP(12); CV(p0, 1, 2); CV(p0, 2, 4); SBAR();
            M0<2, BLK>(mK); QK(7); LDL<2>(vK1, srdK, soK); EXP(13); ADD0(6); SBAR();
            QK(8);
            LOOPSYNC();
            VRD(1); VRD(0); VRD(3); VRD(2);
            M0<0, 0>(mV); QK(9); LDL<0>(vV, srdV, soV); VRD(5); EXP(14); SBAR();
            QK(10); VRD(4); EXP(15); CV(p0, 3, 6); ADD1(7); ADD0(8); SBAR();
            M0<1, 0>(mV); QK(11); LDL<1>(vV, srdV, soV); ADD1(9); CV(p1, 0, 8); ADD0(10); SBAR();
            PV(0);  ADD1(11); CV(p1, 1, 10); ADD0(12); ADD1(13); CV(p1, 2, 12); SBAR();
            M0<2, 0>(mV); PV(1); LDL<2>(vV, srdV, soV); VRD(7); ADD0(14); SBAR();
            if (__builtin_expect(u + 1 == diagu, 0)) { asm volatile("s_nop 7" : "+v"(S)); mask(2 * u + 2 + sub); }
            PV(2);  VRD(6); ADD1(15); CV(p1, 3, 14); FM(0); FM(1); SBAR();
            M0<0, BLK>(mV); PV(3); LDL<0>(vV1, srdV, soV); VRD(9); FM(2); SBAR();
            PV(4);  VRD(8); FM(3); FM(4); FM(5); ps0 = vadd(ps0, ps1); SBAR();
            M0<1, BLK>(mV); PV(5); LDL<1>(vV1, srdV, soV); VRD(11); FM(6); SBAR();
            PV(6);  VRD(10); FM(7); FM(8); FM(9); l = vadd(l, ps0); SBAR();
            M0<2, BLK>(mV); PV(7); LDL<2>(vV1, srdV, soV); FM(10); FM(11); kb[0] += dk; kb[1] += dk; PIN(kb[0]); PIN(kb[1]); SBAR();
            PV(8);
        } else {
            QK(0);  EXP(4); EXP(5); ps0 = vadd(X[0], X[1]); SBAR();
            QK(1);  KRD(7); EXP(6); EXP(7); ps1 = vadd(X[2], X[3]); SBAR();
            QK(2);  KRD(6); EXP(8); CV(p0, 0, 0); ADD0(4); ADD1(5); SBAR();
            M0<0, 0>(mK); QK(3); LDL<0>(vK, srdK, soK); KRD(9); EXP(9); SBAR();
            QK(4);  KRD(8); EXP(10); CV(p0, 1, 2); CV(p0, 2, 4); SBAR();
            M0<1, 0>(mK); QK(5); LDL<1>(vK, srdK, soK); KRD(11); EXP(11); SBAR();
            QK(6);  KRD(10); EXP(12); ADD0(6); CV(p0, 3, 6); ADD1(7); SBAR();
            M0<2, 0>(mK); QK(7); LDL<2>(vK, srdK, soK); VRD(1); EXP(13); SBAR();
            QK(8);  VRD(0); EXP(14); ADD0(8); ADD1(9); SBAR();
            QK(9);  VRD(3); EXP(15); CV(p1, 0, 8); SBAR();
            QK(10); VRD(2); ADD0(10); ADD1(11); CV(p1, 1, 10); SBAR();
            QK(11); VRD(5); ADD0(12); ADD1(13); CV(p1, 2, 12); SBAR();
            M0<0, 0>(mV); PV(0); LDL<0>(vV, srdV, soV); VRD(4); ADD0(14); SBAR();
            PV(1);  VRD(7); ADD1(15); CV(p1, 3, 14); SBAR();
            if (__builtin_expect(u + 1 == diagu, 0)) { asm volatile("s_nop 7" : "+v"(S)); mask(u + 1); }
            M0<1, 0>(mV); PV(2); LDL<1>(vV, srdV, soV); VRD(6); FM(0); SBAR();
            PV(3);  VRD(9); FM(1); FM(2); FM(3); ps0 = vadd(ps0, ps1); SBAR();
            M0<2, 0>(mV); PV(4); LDL<2>(vV, srdV, soV); VRD(8); FM(4); SBAR();
            PV(5);  VRD(11); FM(5); FM(6); FM(7); l = vadd(l, ps0); SBAR();
            PV(6);  VRD(10); FM(8); FM(9); FM(10); SBAR();
            PV(7);  FM(11); FM(12); FM(13); kb[0] += dk; kb[1] += dk; PIN(kb[0]); PIN(kb[1]); SBAR();
            PV(8);
        }
        LOOPSYNC();
        KRD(1); KRD(0); KRD(3); KRD(2);
        if (TB) {
            M0<0, 0>(wb + r2); PV(9); LDL<0>(vK, srdK, soK + 2 * KBLK); KRD(5); FM(12); FM(13); SBAR();
            PV(10); KRD(4); FM(14); FM(15); EXP(0); vb += dv; vbo += dv; PIN(vb); PIN(vbo); SBAR();
            M0<1, 0>(wb + r2); PV(11); LDL<1>(vK, srdK, soK + 2 * KBLK); EXP(1); EXP(2); EXP(3); SBAR();
        } else {
            PV(9);  KRD(5); FM(14); FM(15); EXP(0); SBAR();
            PV(10); KRD(4); EXP(1); EXP(2); vb += dv; vbo += dv; PIN(vb); PIN(vbo); SBAR();
            PV(11); EXP(3); SBAR();
        }
        ROTATE();
    }
    STAMPC(9);
#define TAIL_HEAD() \
        const bool last = u == NU - 1; \
        if (!last) { \
            if (TB) { dma<2, 0>(wb + r0, vK, srdK, so + 4 * KBLK); dma3(wb + r0 + BLK, vK1, srdK, so + 4 * KBLK); } \
            else { dma3(u + 3 < NU ? wb + r0 : LDS_NOWHERE, vK, srdK, so + 3 * KBLK); dma3(u + 2 < NU ? wb + 3 * BLK + r2 : LDS_NOWHERE, vV, srdV, so + 2 * KBLK); } \
        }
#define TAIL_MID() \
        if (TB) { if (last) { WAIT_VM(0); } else { WAIT_VM(6); } BARRIER(); }
#define TAIL_END() \
        if (!last) { BK_WAIT(); BARRIER(); if (TB) { dma<0, 0>(wb + r2, vK, srdK, so + 6 * KBLK); dma<1, 0>(wb + r2, vK, srdK, so + 6 * KBLK); } }
    if (u < nu) {
        TAIL_HEAD()
#pragma unroll
        for (int e = 4; e < 8; ++e) EXP(e);
        float ps0 = vadd(X[0], X[1]), ps1 = vadd(X[2], X[3]);
        CV(p0, 0, 0); CV(p0, 1, 2); EXP(8); EXP(9); CV(p0, 2, 4); CV(p0, 3, 6);
        TAIL_MID()
        VRD(1); VRD(0); VRD(3); VRD(2); VRD(5); VRD(4);
        EXP(10); EXP(11); EXP(12); EXP(13); SBAR();
        const unsigned mVt = last ? LDS_NOWHERE : wb + r1;
        PV(0);  if (TB) dma<0, 0>(mVt, vV, srdV, so + 2 * KBLK); EXP(14); EXP(15); SBAR();
        PV(1);  VRD(7); ADD0(4); ADD1(5); SBAR();
        PV(2);  VRD(6); if (TB) dma<1, 0>(mVt, vV, srdV, so + 2 * KBLK); CV(p1, 0, 8); CV(p1, 1, 10); SBAR();
        PV(3);  VRD(9); CV(p1, 2, 12); CV(p1, 3, 14); SBAR();
        PV(4);  VRD(8); if (TB) dma<2, 0>(mVt, vV, srdV, so + 2 * KBLK); ADD0(6); ADD1(7); SBAR();
        PV(5);  VRD(11); ADD0(8); ADD1(9); ADD0(10); SBAR();
        PV(6);  VRD(10); if (TB) dma<0, BLK>(mVt, vV1, srdV, so + 2 * KBLK); ADD1(11); ADD0(12); SBAR();
        PV(7);  if (TB) dma<1, BLK>(mVt, vV1, srdV, so + 2 * KBLK); ADD1(13); SBAR();
        PV(8);  if (TB) dma<2, BLK>(mVt, vV1, srdV, so + 2 * KBLK); ADD0(14); ADD1(15); SBAR();
        PV(9); PV(10); PV(11);
        l += ps0 + ps1;
        TAIL_END()
        ROTATE();
    }
    while (u < NU) {
        TAIL_HEAD()
        TAIL_MID()
        if (TB && !last) { dma3(wb + r1, vV, srdV, so + 2 * KBLK); dma3(wb + r1 + BLK, vV1, srdV, so + 2 * KBLK); }
        TAIL_END()
        ROTATE();
    }
#undef TAIL_HEAD
#undef TAIL_MID
#undef TAIL_END
    OPIN("s_nop 15");
    int lane2;
    asm volatile("v_mbcnt_lo_u32_b32 %0, -1, 0\n\tv_mbcnt_hi_u32_b32 %0, -1, %0" : "=v"(lane2));

    const int l32e = lane2 & 31, hie = lane2 >> 5, r8e = lane2 >> 3, c8e = lane2 & 7, r16e = lane2 >> 4;
    STAMP(5);
    const int reg = TB ? (sub ? r2 : r1) + hsel * BLK : (wid < 3 ? wid * BLK : 3 * BLK + r0);
    l = swapsum(l);
    if (__builtin_expect(__any(!(l < LBIG)), 0)) {
        const unsigned mb = ldsb + reg;
        const int dr2 = (lane2 >> 2) & 7, dh2 = lane2 >> 5, dc2 = lane2 & 3, dg2 = (lane2 >> 4) & 1;
        const unsigned vK0 = dr2 * KROW + 64u * dh2 + 16u * (dc2 ^ dg2), vK1_ = dr2 * KROW + 64u * dh2 + 16u * (dc2 ^ (2 + dg2)), vV0 = dr2 * KROW + 64u * dh2 + 16u * dc2;
        kb[0] = lp + reg + (l32e >> 3) * 1024 + (l32e & 7) * 64 + 16 * (hie ^ ((l32e >> 2) & 3));
        kb[1] = lp + reg + (l32e >> 3) * 1024 + (l32e & 7) * 64 + 16 * ((2 + hie) ^ ((l32e >> 2) & 3));
        vb = lp + reg + (4 * hie + ((lane2 & 15) >> 2)) * 64 + 32 * ((lane2 >> 4) & 1) + 8 * (lane2 & 3);
        vbo = vb + (TB ? 512 - sub * 512 : 512); vb += TB ? sub * 512 : 0;
        bf16x8 q2[12];
        {
            const unsigned vQ0 = dr2 * QROW + 64u * dh2 + 16u * (dc2 ^ dg2), vQ1 = dr2 * QROW + 64u * dh2 + 16u * (dc2 ^ (2 + dg2));
            const unsigned qs = (unsigned)(q0 + (TB ? 0 : 32 * sub)) * QROW + hsel * 384;
#pragma unroll
            for (int g = 0; g < 4; ++g) dma3(mb + g * 1024, (g & 1) ? vQ1 : vQ0, srdQ, qs + g * 8 * QROW);
            WAIT_VM(0);
#pragma unroll
            for (int ks = 0; ks < 12; ++ks) q2[ks] = ldk(KADDR(ks));
        }
        auto scores = [&](int t) {
            const int b = TB ? 2 * t + sub : t;
            asm volatile("s_waitcnt lgkmcnt(0)" ::: "memory");
#pragma unroll
            for (int g = 0; g < 4; ++g) dma3(mb + g * 1024, (g & 1) ? vK1_ : vK0, srdK, b * KBLK + g * 8 * KROW);
            WAIT_VM(0);
            S = MFMA(ldk(KADDR(0)), q2[0], f32x16{});
#pragma unroll
            for (int ks = 1; ks < 12; ++ks) S = MFMA(ldk(KADDR(ks)), q2[ks], S);
            if (t == diagu) {
                const int k0 = b * 32 + 4 * hie - (q0 + (TB ? 0 : 32 * sub) + l32e);
#pragma unroll
                for (int r = 0; r < 16; ++r) if (k0 + (r & 3) + 8 * (r >> 2) > 0) S[r] = -__builtin_inff();
            }
        };
        float mx = -__builtin_inff();
#pragma unroll 1
        for (int t = 0; t < nu; ++t) { scores(t); asm volatile("s_nop 15" : "+v"(S)); mx = max2f(mx, rowmax()); }
        m = mx * C2;
#pragma unroll
        for (int i = 0; i < 6; ++i) o[i] = f32x16{};
        l = 0.f;
#pragma unroll 1
        for (int t = 0; t < nu; ++t) {
            scores(t);
            const int b = TB ? 2 * t + sub : t;
            asm volatile("s_waitcnt lgkmcnt(0)" ::: "memory");
#pragma unroll
            for (int g = 0; g < 4; ++g) dma3(mb + g * 1024, vV0, srdV, b * KBLK + g * 8 * KROW);
            float ps = 0.f;
#pragma unroll
            for (int e = 0; e < 16; ++e) { X[e] = __builtin_amdgcn_exp2f(__builtin_fmaf(S[e], C2, -m)); ps += X[e]; }
            l += ps;
            p0 = (u32x4){cvtpk(X[0], X[1]), cvtpk(X[2], X[3]), cvtpk(X[4], X[5]), cvtpk(X[6], X[7])};
            p1 = (u32x4){cvtpk(X[8], X[9]), cvtpk(X[10], X[11]), cvtpk(X[12], X[13]), cvtpk(X[14], X[15])};
            WAIT_VM(0);
            #pragma unroll
            for (int k = 0; k < 12; ++k) { VRD(k); o[k % 6] = MFMA(VFR(k), __builtin_bit_cast(bf16x8, k < 6 ? p0 : p1), o[k % 6]); }
        }
        l = swapsum(l);
        asm volatile("s_waitcnt lgkmcnt(0)" ::: "memory");
    }

    const __amdgpu_buffer_rsrc_t srdO = __builtin_amdgcn_make_buffer_rsrc((void*)((char*)O + (size_t)s0 * QROW + kvh * 768), (short)0, (int)((unsigned)L * QROW - kvh * 768), 0x00020000);
    const lds_ptr lw = (lds_ptr)lds;
    const unsigned vO = r8e * QROW + c8e * 16;
#define STG(base, db, g, lo, hi_) *(__attribute__((address_space(3))) u32x2*)((base) + ((db) >> 1) * 4096 + 16 * (((4 * (db) + (g)) & 7) ^ (l32e & 7))) = (u32x2){lo, hi_}
    if constexpr (!TB) {
        if (nu > 0) {
            float il = __builtin_amdgcn_rcpf(l);
            asm volatile("s_nop 1" : "+v"(il));
            const lds_ptr wp = lw + reg + l32e * 128 + 8 * hie;
            const lds_ptr rp = lw + reg + r8e * 128 + 16 * (c8e ^ r8e);
            const unsigned ob = (unsigned)(q0 + 32 * sub) * QROW + hsel * 384;
            u32x4 w[4];
            auto scale = [&](int db) {
                float y[16];
#pragma unroll
                for (int r = 0; r < 16; ++r) y[r] = vmul(o[db][r], il);
                unsigned c[8];
#pragma unroll
                for (int r = 0; r < 8; ++r) c[r] = vcvt(y[2 * r], y[2 * r + 1]);
#pragma unroll
                for (int g = 0; g < 4; ++g) STG(wp, db, g, c[2 * g], c[2 * g + 1]);
                SBAR();
            };
            auto rd = [&](int s) {
#pragma unroll
                for (int g = 0; g < 4; ++g) w[g] = *(const __attribute__((address_space(3))) u32x4*)(rp + s * 4096 + g * 1024);
                SBAR();
            };
            auto st_ = [&](int s) {
#pragma unroll
                for (int g = 0; g < 4; ++g) __builtin_amdgcn_raw_buffer_store_b128(w[g], srdO, vO + s * 128, ob + g * 8 * QROW, STAUX);
                SBAR();
            };
            scale(0); scale(1); rd(0); scale(2); st_(0); scale(3); rd(1); scale(4); st_(1); scale(5); rd(2); st_(2);
        }
    } else {
        const lds_ptr xp = lw + reg + lane2 * 16;
        const lds_ptr xq = lw + (sub ? r1 : r2) + hsel * BLK + lane2 * 16;
        __attribute__((address_space(3))) f32x2* ml = (__attribute__((address_space(3))) f32x2*)(lw + LDS_MISC);
        ml[wid * 64 + lane2] = (f32x2){m, l};
#pragma unroll
        for (int i = 0; i < 3; ++i)
#pragma unroll
            for (int g = 0; g < 4; ++g) *(__attribute__((address_space(3))) f32x4*)(xp + (4 * i + g) * 1024) = (f32x4){o[2 * i + 1][4 * g], o[2 * i + 1][4 * g + 1], o[2 * i + 1][4 * g + 2], o[2 * i + 1][4 * g + 3]};
        BARRIER();
        const f32x2 pm = ml[(wid ^ 2) * 64 + lane2];
        f32x4 x[12];
#pragma unroll
        for (int i = 0; i < 12; ++i) x[i] = *(const __attribute__((address_space(3))) f32x4*)(xq + i * 1024);
        SBAR();
        const float mm = max2f(m, pm[0]);
        const float fa = __builtin_amdgcn_exp2f(m - mm), fb = __builtin_amdgcn_exp2f(pm[0] - mm);
        const float il = __builtin_amdgcn_rcpf(l * fa + pm[1] * fb);
        float wa = fa * il, wb2 = fb * il;
        asm volatile("s_nop 1" : "+v"(wa), "+v"(wb2));
        const lds_ptr wp = lw + r0 + hsel * BLK + l32e * 128 + 8 * hie + 16 * ((4 * sub) ^ (l32e & 4));
#pragma unroll
        for (int i = 0; i < 3; ++i) {
            float y[16];
#pragma unroll
            for (int r = 0; r < 16; ++r) y[r] = vmul(x[4 * i + (r >> 2)][r & 3], wb2);
#pragma unroll
            for (int r = 0; r < 16; ++r) y[r] = vfma_v(o[2 * i][r], wa, y[r]);
            unsigned c[8];
#pragma unroll
            for (int r = 0; r < 8; ++r) c[r] = vcvt(y[2 * r], y[2 * r + 1]);
#pragma unroll
            for (int g = 0; g < 4; ++g) *(__attribute__((address_space(3))) u32x2*)(wp + i * 4096 + 16 * (g ^ (l32e & 3))) = (u32x2){c[2 * g], c[2 * g + 1]};
            SBAR();
        }
        BARRIER();
        const lds_ptr rp = lw + r0 + hsel * BLK + sub * 2048 + r8e * 128 + 16 * (c8e ^ r8e);
        const unsigned ob = (unsigned)(q0 + 16 * sub) * QROW + hsel * 384;
        u32x4 w[6];
#pragma unroll
        for (int i = 0; i < 6; ++i) w[i] = *(const __attribute__((address_space(3))) u32x4*)(rp + (i % 3) * 4096 + (i / 3) * 1024);
#pragma unroll
        for (int i = 0; i < 6; ++i) __builtin_amdgcn_raw_buffer_store_b128(w[i], srdO, vO + (i % 3) * 128, ob + (i / 3) * 8 * QROW, STAUX);
    }
#undef STG
    STAMP(6);
#undef EXP
#undef FM
#undef KRD
#undef VRD
#undef VFR
#undef QK
#undef PV
#undef ADD0
#undef ADD1
#undef ROTATE
#undef BK_WAIT
#undef CV
#undef KADDR
}

DI int wave_max63(int x) {
    asm volatile("s_nop 1\n\tv_max_i32_dpp %0, %0, %0 row_shr:1 row_mask:0xf bank_mask:0xf\n\ts_nop 1\n\tv_max_i32_dpp %0, %0, %0 row_shr:2 row_mask:0xf bank_mask:0xf\n\t"
                 "s_nop 1\n\tv_max_i32_dpp %0, %0, %0 row_shr:4 row_mask:0xf bank_mask:0xf\n\ts_nop 1\n\tv_max_i32_dpp %0, %0, %0 row_shr:8 row_mask:0xf bank_mask:0xf\n\t"
                 "s_nop 1\n\tv_max_i32_dpp %0, %0, %0 row_bcast:15 row_mask:0xa bank_mask:0xf\n\ts_nop 1\n\tv_max_i32_dpp %0, %0, %0 row_bcast:31 row_mask:0xc bank_mask:0xf\n\ts_nop 0" : "+v"(x));
    return x;
}
constexpr int first_pure_b() { int i = TH; while (WB * ((i + 2) >> 1) < WA * TH) ++i; return i; }
constexpr int I0 = first_pure_b();
constexpr int NMIX = (I0 - TH) + TH / 2;
static_assert(NMIX <= 12 && I0 < 60, "level table (16 levels; thresholds are inline constants)");
constexpr int level(int k) {
    if (k < 4) return (I0 + 3 - k) | 0x10000;
    k -= 4;
    if (k >= NMIX) return (int)0xffff0040;
    int i = I0 - 1, jj = TH / 2 - 1, out = 0;
    for (int lev = 0; lev <= k; ++lev) {
        const bool useB = i >= TH && (jj < 0 || WB * ((i + 2) >> 1) >= WA * (2 * jj + 2));
        if (useB) { out = i | 0x10000; --i; } else { out = 2 * jj; --jj; }
    }
    return out;
}

__global__ __launch_bounds__(256, 2) void attn_v12(const unsigned short* __restrict__ Q, const unsigned short* __restrict__ K,
                                                  const unsigned short* __restrict__ V, const int* __restrict__ cu,
                                                  unsigned short* __restrict__ O, int nseq, int T, unsigned long long* dbg) {
    extern __shared__ __attribute__((aligned(16))) char lds[];
    unsigned long long st[16];
    STAMP(0);
    const int lane = threadIdx.x & 63;
    const int kvh = blockIdx.x & 7, j = blockIdx.x >> 3;
    const int wv = rfl(threadIdx.x >> 6);
    unsigned vp, vq;
    LaneAddr la;
    {
        const int jj = j & 31, pj = (j >> 5) * (32 - PFSKIP) + jj - PFSKIP;
        constexpr int LPW = (8192 + 4 * (64 - 2 * PFSKIP) - 1) / (4 * (64 - 2 * PFSKIP));
        const unsigned li = (pj * 4 + wv) * LPW + lane, tk = li / 3u;
        vp = (tk * KROW + (li - 3u * tk) * 128u) | ((PFB && lane < LPW && j < 64 && jj >= PFSKIP) ? 0u : OOB);
        const unsigned lq = (j * 4 + wv) * 64 + lane, tq = lq / 6u;
        vq = (tq * QROW + (lq - 6u * tq) * 128u) | ((PFQ && j < 64) ? 0u : OOB);
        const int dr = (lane >> 2) & 7, dh = lane >> 5, dc = lane & 3, dg = (lane >> 4) & 1;
        la.vK = (8 * wv + dr) * KROW + 64u * dh + 16u * (dc ^ ((2 * wv + dg) & 3));
        la.vV = (8 * wv + dr) * KROW + 64u * dh + 16u * dc;
        la.vQ0 = dr * QROW + 64u * dh + 16u * (dc ^ dg);
        la.vQ1 = dr * QROW + 64u * dh + 16u * (dc ^ (2 + dg));
        asm volatile("" : "+v"(vp), "+v"(vq), "+v"(la.vK), "+v"(la.vV), "+v"(la.vQ0), "+v"(la.vQ1));
    }
    asm volatile("" :: "s"(Q), "s"(K), "s"(V), "s"(O), "s"(cu), "s"(nseq), "s"(T));
    int r = j < 32 ? j : (j < 64 ? 95 - j : j);
    int a, b;
    if (nseq <= 15) {
        const unsigned pm = (unsigned)(uintptr_t)lds + LDS_MISC + wv * 256;
        const __amdgpu_buffer_rsrc_t srdKa = __builtin_amdgcn_make_buffer_rsrc((void*)((const char*)K + kvh * 384), (short)0, (int)((unsigned)T * KROW - kvh * 384), 0x00020000);
        const __amdgpu_buffer_rsrc_t srdVa = __builtin_amdgcn_make_buffer_rsrc((void*)((const char*)V + kvh * 384), (short)0, (int)((unsigned)T * KROW - kvh * 384), 0x00020000);
        const __amdgpu_buffer_rsrc_t srdQa = __builtin_amdgcn_make_buffer_rsrc((void*)((const char*)Q + kvh * 768), (short)0, (int)((unsigned)T * QROW - kvh * 768), 0x00020000);
        i32x16 c;
        if (PFQ) asm volatile("s_load_dwordx16 %0, %1, 0x0\n\ts_mov_b32 m0, %2\n\ts_nop 0\n\tbuffer_load_dword %6, %7, 0 offen lds\n\tbuffer_load_dword %3, %4, 0 offen lds\n\tbuffer_load_dword %3, %5, 0 offen lds\n\ts_waitcnt lgkmcnt(0)"
                     : "=&s"(c) : "s"(cu), "s"(pm), "v"(vp), "s"(srdKa), "s"(srdVa), "v"(vq), "s"(srdQa) : "memory");
        else asm volatile("s_load_dwordx16 %0, %1, 0x0\n\ts_mov_b32 m0, %2\n\ts_nop 0\n\tbuffer_load_dword %3, %4, 0 offen lds\n\tbuffer_load_dword %3, %5, 0 offen lds\n\ts_waitcnt lgkmcnt(0)"
                     : "=&s"(c) : "s"(cu), "s"(pm), "v"(vp), "s"(srdKa), "s"(srdVa) : "memory");
        asm volatile("v_mov_b32 %0, %1\n\tv_writelane_b32 %0, %2, 1\n\tv_writelane_b32 %0, %3, 2\n\tv_writelane_b32 %0, %4, 3\n\tv_writelane_b32 %0, %5, 4\n\tv_writelane_b32 %0, %6, 5\n\t"
                     "v_writelane_b32 %0, %7, 6\n\tv_writelane_b32 %0, %8, 7\n\tv_writelane_b32 %0, %9, 8\n\tv_writelane_b32 %0, %10, 9\n\tv_writelane_b32 %0, %11, 10\n\t"
                     "v_writelane_b32 %0, %12, 11\n\tv_writelane_b32 %0, %13, 12\n\tv_writelane_b32 %0, %14, 13\n\tv_writelane_b32 %0, %15, 14\n\tv_writelane_b32 %0, %16, 15\n\ts_nop 1"
                     : "=&v"(a) : "s"(c[0]), "s"(c[1]), "s"(c[2]), "s"(c[3]), "s"(c[4]), "s"(c[5]), "s"(c[6]), "s"(c[7]), "s"(c[8]), "s"(c[9]), "s"(c[10]), "s"(c[11]), "s"(c[12]), "s"(c[13]), "s"(c[14]), "s"(c[15]));
        b = __builtin_amdgcn_update_dpp(a, a, 0x101, 0xf, 0xf, false);
    } else {
        u32x2 ab;
        asm volatile("global_load_dwordx2 %0, %1, %2\n\ts_waitcnt vmcnt(0)" : "=&v"(ab) : "v"(min(lane, nseq - 1) * 4), "s"(cu) : "memory");
        a = ab[0]; b = ab[1];
    }
    const int n = lane < nseq ? (b - a + 31) >> 5 : 0;
    int code = -1;
    unsigned long long msk = 0;
    if (__builtin_expect(__ballot(n > I0 + 4) != 0, 0)) {
        const int nmax = __builtin_amdgcn_readlane(wave_max63(n), 63);
        for (int i = nmax - 1; i >= I0 + 4; --i) {
            msk = __ballot(n > i);
            const int c = __popcll(msk);
            if (r < c) { code = i | 0x10000; goto found; }
            r -= c;
        }
    }
    {
        int cnt;
        asm volatile("v_cmp_lt_i32_e64 %0, (%c5&65535), %4\n\ts_bcnt1_i32_b64 %2, %0\n\ts_mov_b32 %3, %c5\n\ts_cmp_lt_u32 %1, %2\n\ts_cbranch_scc1 1f\n\ts_sub_u32 %1, %1, %2\n\t"
                     "v_cmp_lt_i32_e64 %0, (%c6&65535), %4\n\ts_bcnt1_i32_b64 %2, %0\n\ts_mov_b32 %3, %c6\n\ts_cmp_lt_u32 %1, %2\n\ts_cbranch_scc1 1f\n\ts_sub_u32 %1, %1, %2\n\t"
                     "v_cmp_lt_i32_e64 %0, (%c7&65535), %4\n\ts_bcnt1_i32_b64 %2, %0\n\ts_mov_b32 %3, %c7\n\ts_cmp_lt_u32 %1, %2\n\ts_cbranch_scc1 1f\n\ts_sub_u32 %1, %1, %2\n\t"
                     "v_cmp_lt_i32_e64 %0, (%c8&65535), %4\n\ts_bcnt1_i32_b64 %2, %0\n\ts_mov_b32 %3, %c8\n\ts_cmp_lt_u32 %1, %2\n\ts_cbranch_scc1 1f\n\ts_sub_u32 %1, %1, %2\n\t"
                     "v_cmp_lt_i32_e64 %0, (%c9&65535), %4\n\ts_bcnt1_i32_b64 %2, %0\n\ts_mov_b32 %3, %c9\n\ts_cmp_lt_u32 %1, %2\n\ts_cbranch_scc1 1f\n\ts_sub_u32 %1, %1, %2\n\t"
                     "v_cmp_lt_i32_e64 %0, (%c10&65535), %4\n\ts_bcnt1_i32_b64 %2, %0\n\ts_mov_b32 %3, %c10\n\ts_cmp_lt_u32 %1, %2\n\ts_cbranch_scc1 1f\n\ts_sub_u32 %1, %1, %2\n\t"
                     "v_cmp_lt_i32_e64 %0, (%c11&65535), %4\n\ts_bcnt1_i32_b64 %2, %0\n\ts_mov_b32 %3, %c11\n\ts_cmp_lt_u32 %1, %2\n\ts_cbranch_scc1 1f\n\ts_sub_u32 %1, %1, %2\n\t"
                     "v_cmp_lt_i32_e64 %0, (%c12&65535), %4\n\ts_bcnt1_i32_b64 %2, %0\n\ts_mov_b32 %3, %c12\n\ts_cmp_lt_u32 %1, %2\n\ts_cbranch_scc1 1f\n\ts_sub_u32 %1, %1, %2\n\t"
                     "v_cmp_lt_i32_e64 %0, (%c13&65535), %4\n\ts_bcnt1_i32_b64 %2, %0\n\ts_mov_b32 %3, %c13\n\ts_cmp_lt_u32 %1, %2\n\ts_cbranch_scc1 1f\n\ts_sub_u32 %1, %1, %2\n\t"
                     "v_cmp_lt_i32_e64 %0, (%c14&65535), %4\n\ts_bcnt1_i32_b64 %2, %0\n\ts_mov_b32 %3, %c14\n\ts_cmp_lt_u32 %1, %2\n\ts_cbranch_scc1 1f\n\ts_sub_u32 %1, %1, %2\n\t"
                     "v_cmp_lt_i32_e64 %0, (%c15&65535), %4\n\ts_bcnt1_i32_b64 %2, %0\n\ts_mov_b32 %3, %c15\n\ts_cmp_lt_u32 %1, %2\n\ts_cbranch_scc1 1f\n\ts_sub_u32 %1, %1, %2\n\t"
                     "v_cmp_lt_i32_e64 %0, (%c16&65535), %4\n\ts_bcnt1_i32_b64 %2, %0\n\ts_mov_b32 %3, %c16\n\ts_cmp_lt_u32 %1, %2\n\ts_cbranch_scc1 1f\n\ts_sub_u32 %1, %1, %2\n\t"
                     "v_cmp_lt_i32_e64 %0, (%c17&65535), %4\n\ts_bcnt1_i32_b64 %2, %0\n\ts_mov_b32 %3, %c17\n\ts_cmp_lt_u32 %1, %2\n\ts_cbranch_scc1 1f\n\ts_sub_u32 %1, %1, %2\n\t"
                     "v_cmp_lt_i32_e64 %0, (%c18&65535), %4\n\ts_bcnt1_i32_b64 %2, %0\n\ts_mov_b32 %3, %c18\n\ts_cmp_lt_u32 %1, %2\n\ts_cbranch_scc1 1f\n\ts_sub_u32 %1, %1, %2\n\t"
                     "v_cmp_lt_i32_e64 %0, (%c19&65535), %4\n\ts_bcnt1_i32_b64 %2, %0\n\ts_mov_b32 %3, %c19\n\ts_cmp_lt_u32 %1, %2\n\ts_cbranch_scc1 1f\n\ts_sub_u32 %1, %1, %2\n\t"
                     "v_cmp_lt_i32_e64 %0, (%c20&65535), %4\n\ts_bcnt1_i32_b64 %2, %0\n\ts_mov_b32 %3, %c20\n\ts_cmp_lt_u32 %1, %2\n\ts_cbranch_scc1 1f\n\ts_sub_u32 %1, %1, %2\n\t"
                     "s_mov_b32 %3, -1\n1:"
                     : "=&s"(msk), "+s"(r), "=&s"(cnt), "=&s"(code) : "v"(n), "i"(level(0)), "i"(level(1)), "i"(level(2)), "i"(level(3)), "i"(level(4)), "i"(level(5)), "i"(level(6)), "i"(level(7)), "i"(level(8)), "i"(level(9)), "i"(level(10)), "i"(level(11)), "i"(level(12)), "i"(level(13)), "i"(level(14)), "i"(level(15)) : "scc");
    }
found:
    if (__builtin_expect(code < 0, 0)) { asm volatile("s_waitcnt vmcnt(0)" ::: "memory"); return; }
    const int tb = code >> 16, idx = (code & 0xffff) >> (1 - tb);
    const unsigned below = __builtin_amdgcn_mbcnt_hi((unsigned)(msk >> 32), __builtin_amdgcn_mbcnt_lo((unsigned)msk, 0u));
    const unsigned long long hit = __ballot(below == (unsigned)r) & msk;
    const int sl = __builtin_ctzll(hit);
    const int s0 = min(max(__builtin_amdgcn_readlane(a, sl), 0), T), L = min(__builtin_amdgcn_readlane(b, sl), T) - s0;
    if (__builtin_expect(L <= 0, 0)) { asm volatile("s_waitcnt vmcnt(0)" ::: "memory"); return; }
    if (DELAY2 > 0 && j >= 32) __builtin_amdgcn_s_sleep(DELAY2);
    STAMP(1);
    if (tb) attn_item<true>(st, Q, K, V, O, kvh, s0, L, idx, lds, wv, la);
    else attn_item<false>(st, Q, K, V, O, kvh, s0, L, idx, lds, wv, la);
}

extern "C" void kernel_launch(void* const* d_in, const int* in_sizes, int n_in, void* d_out, int out_size, void* d_ws, size_t ws_size, hipStream_t stream) {
    const int T = in_sizes[0] / (HQ * D), nseq = in_sizes[3] - 1;
    int slots = (T + 31) / 32 + nseq;
    if (slots < 64) slots = 64;
    (void)hipFuncSetAttribute((const void*)attn_v12, hipFuncAttributeMaxDynamicSharedMemorySize, LDS_BYTES);
    attn_v12<<<slots * 8, 256, LDS_BYTES, stream>>>((const unsigned short*)d_in[0], (const unsigned short*)d_in[1], (const unsigned short*)d_in[2],
                                                 (const int*)d_in[3], (unsigned short*)d_out, nseq, T, (unsigned long long*)d_ws);
}
```

```cpp
#include <hip/hip_runtime.h>
#include <hip/hip_bf16.h>
#include <stdint.h>

typedef short bf16x8 __attribute__((ext_vector_type(8)));
typedef short s16x4 __attribute__((ext_vector_type(4)));
typedef float f32x16 __attribute__((ext_vector_type(16)));
typedef float f32x4 __attribute__((ext_vector_type(4)));
typedef float f32x2 __attribute__((ext_vector_type(2)));
typedef unsigned u32x4 __attribute__((ext_vector_type(4)));
typedef unsigned u32x2 __attribute__((ext_vector_type(2)));
typedef int i32x16 __attribute__((ext_vector_type(16)));
typedef __attribute__((address_space(3))) const char* lds_cptr;
typedef __attribute__((address_space(3))) char* lds_ptr;
typedef short v4i16_t __attribute__((ext_vector_type(4)));

constexpr int HQ = 16, HKV = 8, D = 192;
constexpr unsigned QROW = HQ * D * 2, KROW = HKV * D * 2;
constexpr unsigned KBLK = 32 * KROW;
constexpr int BLK = 12288;
constexpr int LDS_MISC = 6 * BLK;
constexpr int LDS_BYTES = LDS_MISC + 2048;
constexpr float C2 = 0.07216878364870322f * 1.4426950408889634f;
constexpr float LBIG = 1073741824.f;
#define TH 8
#define PFKV 0
#define PFFIRST 0
#define PFSKIP 0
#define QNT 0
#define PFCODE 0
#define STAUX 17
#define PFB 1
#define DELAY2 0
#define PFQ 0
#define WA 10
#define WB 11
constexpr unsigned LDS_NOWHERE = 0x30000u;
constexpr unsigned OOB = 0x80000000u;

#define DI __device__ __forceinline__
#define MFMA(a, b, c) __builtin_amdgcn_mfma_f32_32x32x16_bf16(a, b, c, 0, 0, 0)
#define SBAR() __builtin_amdgcn_sched_barrier(0)
#define PIN(x) asm volatile("" : "+v"(x))
typedef __bf16 bf16x2_t __attribute__((ext_vector_type(2)));
DI unsigned cvtpk(float lo, float hi) { const f32x2 v = {lo, hi}; return __builtin_bit_cast(unsigned, __builtin_convertvector(v, bf16x2_t)); }
DI float max3f(float a, float b, float c) { float r; asm("v_max3_f32 %0, %1, %2, %3" : "=v"(r) : "v"(a), "v"(b), "v"(c)); return r; }
DI float max2f(float a, float b) { float r; asm("v_max_f32_e32 %0, %1, %2" : "=v"(r) : "v"(a), "v"(b)); return r; }
DI float fmul_s(float a, float b) { float r; asm("v_mul_f32_e32 %0, %1, %2" : "=v"(r) : "v"(a), "v"(b)); return r; }
DI float fma_v(float a, float b, float c) { float r; asm("v_fma_f32 %0, %1, %2, %3" : "=v"(r) : "v"(a), "v"(b), "v"(c)); return r; }
DI void vexp(float& x) { asm volatile("v_exp_f32_e32 %0, %0" : "+v"(x)); }
DI float vadd(float a, float b) { float r; asm volatile("v_add_f32_e32 %0, %1, %2" : "=v"(r) : "v"(a), "v"(b)); return r; }
DI float vfma(float a, float b, float c) { float r; asm volatile("v_fma_f32 %0, %1, %2, %3" : "=v"(r) : "v"(a), "s"(b), "v"(c)); return r; }
DI float vmul(float a, float b) { float r; asm volatile("v_mul_f32_e32 %0, %1, %2" : "=v"(r) : "v"(a), "v"(b)); return r; }
DI float vfma_v(float a, float b, float c) { float r; asm volatile("v_fma_f32 %0, %1, %2, %3" : "=v"(r) : "v"(a), "v"(b), "v"(c)); return r; }
DI unsigned vcvt(float lo, float hi) { unsigned r; asm volatile("v_cvt_pk_bf16_f32 %0, %1, %2" : "=v"(r) : "v"(lo), "v"(hi)); return r; }
DI s16x4 vtr(lds_cptr p) { return __builtin_bit_cast(s16x4, __builtin_amdgcn_ds_read_tr16_b64_v4i16((__attribute__((address_space(3))) v4i16_t*)p)); }
DI bf16x8 ldk(lds_cptr p) { return *(const __attribute__((address_space(3))) bf16x8*)p; }
DI int rfl(int x) { return __builtin_amdgcn_readfirstlane(x); }
DI float swapmax(float x) { auto r = __builtin_amdgcn_permlane32_swap(__float_as_uint(x), __float_as_uint(x), false, false); return max2f(__uint_as_float(r[0]), __uint_as_float(r[1])); }
DI float swapsum(float x) { auto r = __builtin_amdgcn_permlane32_swap(__float_as_uint(x), __float_as_uint(x), false, false); return __uint_as_float(r[0]) + __uint_as_float(r[1]); }

template <int S, int OFF> DI void dma(unsigned mb, unsigned voff, __amdgpu_buffer_rsrc_t srd, unsigned soff) {
    asm volatile("s_add_u32 m0, %0, %5\n\ts_nop 0\n\tbuffer_load_dwordx4 %1, %2, %3 offen offset:%4 lds"
                 :: "s"(mb), "v"(voff), "s"(srd), "s"(soff), "i"(128 * S), "i"(OFF + S * (4096 - 128)) : "memory", "scc");
}
template <int S, int OFF> DI void setm0(unsigned mb) { asm volatile("s_add_u32 m0, %0, %1" :: "s"(mb), "i"(OFF + S * (4096 - 128)) : "scc"); __builtin_amdgcn_sched_barrier(0); }
template <int S> DI void ldm0(unsigned voff, __amdgpu_buffer_rsrc_t srd, unsigned soff) {
    asm volatile("buffer_load_dwordx4 %0, %1, %2 offen offset:%3 lds" :: "v"(voff), "s"(srd), "s"(soff), "i"(128 * S) : "memory");
}
template <int S> DI void dmaq(unsigned mb, unsigned voff, __amdgpu_buffer_rsrc_t srd, unsigned soff) {
    if (QNT == 1) asm volatile("s_add_u32 m0, %0, %5\n\ts_nop 0\n\tbuffer_load_dwordx4 %1, %2, %3 offen offset:%4 nt lds" :: "s"(mb), "v"(voff), "s"(srd), "s"(soff), "i"(128 * S), "i"(S * (4096 - 128)) : "memory", "scc");
    else if (QNT == 2) asm volatile("s_add_u32 m0, %0, %5\n\ts_nop 0\n\tbuffer_load_dwordx4 %1, %2, %3 offen offset:%4 sc1 lds" :: "s"(mb), "v"(voff), "s"(srd), "s"(soff), "i"(128 * S), "i"(S * (4096 - 128)) : "memory", "scc");
    else asm volatile("s_add_u32 m0, %0, %5\n\ts_nop 0\n\tbuffer_load_dwordx4 %1, %2, %3 offen offset:%4 lds" :: "s"(mb), "v"(voff), "s"(srd), "s"(soff), "i"(128 * S), "i"(S * (4096 - 128)) : "memory", "scc");
}
DI void dma3(unsigned mb, unsigned voff, __amdgpu_buffer_rsrc_t srd, unsigned soff) { dma<0, 0>(mb, voff, srd, soff); dma<1, 0>(mb, voff, srd, soff); dma<2, 0>(mb, voff, srd, soff); }
#define DMAL dma
#define LOOPBAR() BARRIER()
#define LOOPWAIT() WAIT_VM(6)
#define LOOPSYNC() asm volatile("s_waitcnt vmcnt(6) lgkmcnt(0)\n\ts_barrier" ::: "memory")
#define M0 setm0
#define LDL ldm0
#define WAIT_VM(n) asm volatile("s_waitcnt vmcnt(" #n ")" ::: "memory")
#define BARRIER() asm volatile("s_waitcnt lgkmcnt(0)\n\ts_barrier" ::: "memory")
#define OPIN(nop) asm volatile(nop : "+v"(o[0]), "+v"(o[1]), "+v"(o[2]), "+v"(o[3]), "+v"(o[4]), "+v"(o[5]))

#define STAMP(i)
#define STAMPC(i)

struct LaneAddr { unsigned vK, vV, vQ0, vQ1; };

template <bool TB>
DI void attn_item(unsigned long long* st, const unsigned short* __restrict__ Q, const unsigned short* __restrict__ K, const unsigned short* __restrict__ V,
                  unsigned short* __restrict__ O, int kvh, int s0, int L, int idx, char* lds, int wid, const LaneAddr& la) {
    const int tid = threadIdx.x, lane = tid & 63, l32 = lane & 31, hi = lane >> 5;
    const int hsel = wid & 1, sub = wid >> 1;
    const int n = (L + 31) >> 5;
    const int q0 = TB ? 32 * idx : 64 * idx;
    const int NB = TB ? idx + 1 : min(2 * idx + 2, n);
    const int NU = TB ? (NB + 1) >> 1 : NB;
    const int nu = TB ? (NB + 1 - sub) >> 1 : (2 * idx + sub < n ? 2 * idx + sub + 1 : 0);
    const int diagu = TB ? (((idx & 1) == sub) ? nu - 1 : -1) : nu - 1;

    const unsigned krec = (unsigned)min(L, 32 * NB) * KROW - kvh * 384;
    const __amdgpu_buffer_rsrc_t srdK = __builtin_amdgcn_make_buffer_rsrc((void*)((const char*)K + (size_t)s0 * KROW + kvh * 384), (short)0, (int)krec, 0x00020000);
    const __amdgpu_buffer_rsrc_t srdQ = __builtin_amdgcn_make_buffer_rsrc((void*)((const char*)Q + (size_t)s0 * QROW + kvh * 768), (short)0, (int)((unsigned)L * QROW - kvh * 768), 0x00020000);
    const unsigned ldsb = (unsigned)(uintptr_t)lds;
    const unsigned wb = ldsb + wid * 1024;
    const unsigned vK = la.vK, vV = la.vV;

    dma3(wb, vK, srdK, 0);
    if (TB) dma3(wb + BLK, vK, srdK, KBLK);
    {
        const unsigned qreg = TB ? 4 * BLK + hsel * BLK : (wid == 3 ? 2 * BLK : (3 + wid) * BLK);
        const unsigned qs = (unsigned)(q0 + (TB ? 16 * sub : 32 * sub)) * QROW + hsel * 384;
        const unsigned m = ldsb + qreg + (TB ? 2048 * sub : 0);
#pragma unroll
        for (int g = 0; g < (TB ? 2 : 4); ++g) { const unsigned vq_ = (g & 1) ? la.vQ1 : la.vQ0; dmaq<0>(m + g * 1024, vq_, srdQ, qs + g * 8 * QROW); dmaq<1>(m + g * 1024, vq_, srdQ, qs + g * 8 * QROW); dmaq<2>(m + g * 1024, vq_, srdQ, qs + g * 8 * QROW); }
    }
    SBAR();
    const __amdgpu_buffer_rsrc_t srdV = __builtin_amdgcn_make_buffer_rsrc((void*)((const char*)V + (size_t)s0 * KROW + kvh * 384), (short)0, (int)krec, 0x00020000);
    if (PFKV) {
        const int per = TB ? 8 : 16, tk = (lane * 43) >> 7;
        const unsigned vp = tk < per ? (unsigned)(q0 + wid * per + tk) * KROW + (lane - 3 * tk) * 128 : OOB;
        const unsigned pm = ldsb + LDS_MISC + wid * 256;
        asm volatile("s_mov_b32 m0, %0\n\ts_nop 0\n\tbuffer_load_dword %1, %2, 0 offen lds" :: "s"(pm), "v"(vp), "s"(srdK) : "memory");
        asm volatile("s_mov_b32 m0, %0\n\ts_nop 0\n\tbuffer_load_dword %1, %2, 0 offen lds" :: "s"(pm), "v"(vp), "s"(srdV) : "memory");
    }
    const int qrel = q0 + (TB ? 0 : 32 * sub) + l32;
    const lds_cptr lp = (lds_cptr)lds;
    lds_cptr kb[2], vb, vbo;
#pragma unroll
    for (int i = 0; i < 2; ++i) kb[i] = lp + (l32 >> 3) * 1024 + (l32 & 7) * 64 + 16 * ((2 * i + hi) ^ ((l32 >> 2) & 3));
    vb = lp + (4 * hi + ((lane & 15) >> 2)) * 64 + 32 * ((lane >> 4) & 1) + 8 * (lane & 3);
#define KADDR(ks) (kb[(ks) & 1] + ((ks) >> 2) * 4096 + (((ks) >> 1) & 1) * 512)
    f32x16 o[6];
#pragma unroll
    for (int i = 0; i < 6; ++i) o[i] = f32x16{};
    OPIN("");

    STAMP(2);
    bf16x8 qf[12];
    if (PFKV) { WAIT_VM(2); } else { WAIT_VM(0); }
    if (TB) BARRIER();
    {
        const int qo = TB ? 4 * BLK + hsel * BLK : (wid == 3 ? 2 * BLK : (3 + wid) * BLK);
#pragma unroll
        for (int ks = 0; ks < 12; ++ks) {
            qf[ks] = ldk(KADDR(ks) + qo);
            if (TB) {
                if (ks == 1) dma<0, 0>(wb + 2 * BLK, vK, srdK, 2 * KBLK); if (ks == 3) dma<1, 0>(wb + 2 * BLK, vK, srdK, 2 * KBLK); if (ks == 5) dma<2, 0>(wb + 2 * BLK, vK, srdK, 2 * KBLK);
                if (ks == 7) dma<0, 0>(wb + 3 * BLK, vK, srdK, 3 * KBLK); if (ks == 9) dma<1, 0>(wb + 3 * BLK, vK, srdK, 3 * KBLK); if (ks == 11) dma<2, 0>(wb + 3 * BLK, vK, srdK, 3 * KBLK);
            } else {
                if (ks == 3) dma<0, 0>(wb + BLK, vK, srdK, KBLK); if (ks == 7) dma<1, 0>(wb + BLK, vK, srdK, KBLK); if (ks == 11) dma<2, 0>(wb + BLK, vK, srdK, KBLK);
            }
        }
    }
    BARRIER();
    STAMP(3);
    kb[0] += TB ? sub * BLK : 0; kb[1] += TB ? sub * BLK : 0;
    vb += TB ? sub * BLK : 3 * BLK;
    vbo = vb + (TB ? 512 - sub * 512 : 512);
    vb += TB ? sub * 512 : 0;

    f32x16 S, X;
    float m = 0.f, l = 0.f;
    auto mask = [&](int kblock) {
        const int d = qrel - kblock * 32 - 4 * hi;
        const float ninf = -__builtin_inff();
        unsigned long long k[16];
#define MK(r) asm volatile("v_cmp_le_i32_e64 %0, %2, %1" : "=s"(k[r]) : "v"(d), "i"(((r) & 3) + 8 * ((r) >> 2)))
#define MS(r) do { float t_ = S[r]; asm volatile("v_cndmask_b32_e64 %0, %1, %0, %2" : "+v"(t_) : "v"(ninf), "s"(k[r])); S[r] = t_; } while (0)
        MK(0); MK(1); MK(2); MK(3); MK(4); MK(5); MK(6); MK(7); MK(8); MK(9); MK(10); MK(11); MK(12); MK(13); MK(14); MK(15);
        MS(0); MS(1); MS(2); MS(3); MS(4); MS(5); MS(6); MS(7); MS(8); MS(9); MS(10); MS(11); MS(12); MS(13); MS(14); MS(15);
#undef MK
#undef MS
    };
    auto rowmax = [&]() -> float {
        const float a = max3f(S[0], S[1], S[2]), b = max3f(S[3], S[4], S[5]), c = max3f(S[6], S[7], S[8]), d = max3f(S[9], S[10], S[11]), e = max3f(S[12], S[13], S[14]);
        return swapmax(max2f(max3f(a, b, S[15]), max3f(c, d, e)));
    };
    const unsigned vK1 = vK + KBLK, vV1 = vV + KBLK;
    u32x4 p0, p1;
    bf16x8 kf[6];
    s16x4 vlo[6], vhi[6];
#define EXP(e) do { float t_ = X[e]; vexp(t_); X[e] = t_; } while (0)
#define FM(e) X[e] = vfma(S[e], C2, nm)
#define KRD(k) do { kf[(k) % 6] = ldk(KADDR(k)); SBAR(); } while (0)
#define VRD(k) do { const lds_cptr vp_ = ((k) & 1 ? vbo : vb) + (((k) % 6) >> 1) * 4096 + ((k) / 6) * 2048; vlo[(k) % 6] = vtr(vp_); vhi[(k) % 6] = vtr(vp_ + 1024); SBAR(); } while (0)
#define VFR(k) ((bf16x8){vlo[(k) % 6][0], vlo[(k) % 6][1], vlo[(k) % 6][2], vlo[(k) % 6][3], vhi[(k) % 6][0], vhi[(k) % 6][1], vhi[(k) % 6][2], vhi[(k) % 6][3]})
#define QK(k) do { S = MFMA(kf[(k) % 6], qf[k], (k) == 0 ? f32x16{} : S); PIN(S); SBAR(); } while (0)
#define PV(k) do { o[(k) % 6] = MFMA(VFR(k), __builtin_bit_cast(bf16x8, (k) < 6 ? p0 : p1), o[(k) % 6]); PIN(o[(k) % 6]); SBAR(); } while (0)
#define ADD0(e) ps0 = vadd(ps0, X[e])
#define ADD1(e) ps1 = vadd(ps1, X[e])
#define CV(P, i, e) P[i] = vcvt(X[e], X[(e) + 1])

    int r0 = 0, r1 = TB ? 2 * BLK : BLK, r2 = TB ? 4 * BLK : 2 * BLK;
    const unsigned mV1 = (TB || 1 < NB) ? wb + (TB ? 5 : 4) * BLK : LDS_NOWHERE, mK2 = 2 < NB ? wb + 2 * BLK : LDS_NOWHERE;
    if (nu > 0) {
        KRD(1); KRD(0); KRD(3); KRD(2); KRD(5); KRD(4);
        if (TB) {
            QK(0); dma<0, 0>(wb + 4 * BLK, vV, srdV, 0); SBAR();
            QK(1); KRD(7); SBAR();
            QK(2); KRD(6); dma<1, 0>(wb + 4 * BLK, vV, srdV, 0); SBAR();
            QK(3); KRD(9); SBAR();
            QK(4); KRD(8); dma<2, 0>(wb + 4 * BLK, vV, srdV, 0); SBAR();
            QK(5); KRD(11); SBAR();
            QK(6); KRD(10); dma<0, 0>(mV1, vV, srdV, KBLK); SBAR();
            QK(7);
            QK(8); dma<1, 0>(mV1, vV, srdV, KBLK); SBAR();
            QK(9);
            QK(10); dma<2, 0>(mV1, vV, srdV, KBLK); SBAR();
            QK(11);
        } else {
            QK(0); dma<0, 0>(wb + 3 * BLK, vV, srdV, 0); SBAR();
            QK(1); KRD(7); dma<1, 0>(wb + 3 * BLK, vV, srdV, 0); SBAR();
            QK(2); KRD(6); dma<2, 0>(wb + 3 * BLK, vV, srdV, 0); SBAR();
            QK(3); KRD(9); SBAR();
            QK(4); KRD(8); dma<0, 0>(mV1, vV, srdV, KBLK); SBAR();
            QK(5); KRD(11); dma<1, 0>(mV1, vV, srdV, KBLK); SBAR();
            QK(6); KRD(10); dma<2, 0>(mV1, vV, srdV, KBLK); SBAR();
            QK(7);
            QK(8); dma<0, 0>(mK2, vK, srdK, 2 * KBLK); SBAR();
            QK(9); dma<1, 0>(mK2, vK, srdK, 2 * KBLK); SBAR();
            QK(10); dma<2, 0>(mK2, vK, srdK, 2 * KBLK); SBAR();
            QK(11);
        }
    } else {
        dma3(wb + 3 * BLK, vV, srdV, 0); dma3(mV1, vV, srdV, KBLK); dma3(mK2, vK, srdK, 2 * KBLK);
    }
    WAIT_VM(6); BARRIER();
    kb[0] += r1; kb[1] += r1; if (TB) { vb += r2; vbo += r2; }
    if (nu > 1) { KRD(1); KRD(0); KRD(3); KRD(2); KRD(5); KRD(4); }
    if (TB) { dma<0, 0>(wb + r0, vK, srdK, 4 * KBLK); dma<1, 0>(wb + r0, vK, srdK, 4 * KBLK); }
    if (nu > 0) {
        asm volatile("s_nop 11" : "+v"(S));
        if (diagu == 0) mask(TB ? sub : 0);
        m = rowmax() * C2;
        const float nm = -m;
        FM(0); FM(1); FM(2); FM(3); FM(4); FM(5); FM(6); FM(7); FM(8); FM(9); FM(10); FM(11); FM(12); FM(13); FM(14); FM(15);
        EXP(0); EXP(1); EXP(2); EXP(3);
    } else X = f32x16{};
    const float nm = -m;

#define ROTATE() do { const int t_ = TB ? r2 : r0; if (TB) { r2 = r1; r1 = r0; r0 = t_; } else { r0 = r1; r1 = r2; r2 = t_; } so += (TB ? 2 : 1) * KBLK; ++u; } while (0)
#define BK_WAIT() LOOPWAIT()
    STAMP(4); STAMPC(8);
    unsigned barw = 0, barn = 0; (void)barw; (void)barn;
    int u = 0;
    unsigned so = 0;
    while (u + 1 < nu) {
        float ps0, ps1;
        const unsigned soK = so + (TB ? 4 : 3) * KBLK, soV = so + 2 * KBLK;
        const unsigned mK = (TB || u + 3 < NU) ? wb + r0 : LDS_NOWHERE, mV = TB ? wb + r1 : (u + 2 < NU ? wb + 3 * BLK + r2 : LDS_NOWHERE);
        const int dk = TB ? r0 - r1 : r2 - r1, dv = TB ? r1 - r2 : r1 - r0;
        if (TB) {
            QK(0);  EXP(4); EXP(5); ps0 = vadd(X[0], X[1]); SBAR();
            M0<2, 0>(mK); QK(1); LDL<2>(vK, srdK, soK); KRD(7); EXP(6); SBAR();
            QK(2);  KRD(6); EXP(7); EXP(8); ps1 = vadd(X[2], X[3]); SBAR();
            M0<0, BLK>(mK); QK(3); LDL<0>(vK1, srdK, soK); KRD(9); EXP(9); SBAR();
            QK(4);  KRD(8); EXP(10); CV(p0, 0, 0); ADD0(4); ADD1(5); SBAR();
            M0<1, BLK>(mK); QK(5); LDL<1>(vK1, srdK, soK); KRD(11); EXP(11); SBAR();
            QK(6);  KRD(10); EXP(12); CV(p0, 1, 2); CV(p0, 2, 4); SBAR();
            M0<2, BLK>(mK); QK(7); LDL<2>(vK1, srdK, soK); EXP(13); ADD0(6); SBAR();
            QK(8);
            LOOPSYNC();
            VRD(1); VRD(0); VRD(3); VRD(2);
            M0<0, 0>(mV); QK(9); LDL<0>(vV, srdV, soV); VRD(5); EXP(14); SBAR();
            QK(10); VRD(4); EXP(15); CV(p0, 3, 6); ADD1(7); ADD0(8); SBAR();
            M0<1, 0>(mV); QK(11); LDL<1>(vV, srdV, soV); ADD1(9); CV(p1, 0, 8); ADD0(10); SBAR();
            PV(0);  ADD1(11); CV(p1, 1, 10); ADD0(12); ADD1(13); CV(p1, 2, 12); SBAR();
            M0<2, 0>(mV); PV(1); LDL<2>(vV, srdV, soV); VRD(7); ADD0(14); SBAR();
            if (__builtin_expect(u + 1 == diagu, 0)) { asm volatile("s_nop 7" : "+v"(S)); mask(2 * u + 2 + sub); }
            PV(2);  VRD(6); ADD1(15); CV(p1, 3, 14); FM(0); FM(1); SBAR();
            M0<0, BLK>(mV); PV(3); LDL<0>(vV1, srdV, soV); VRD(9); FM(2); SBAR();
            PV(4);  VRD(8); FM(3); FM(4); FM(5); ps0 = vadd(ps0, ps1); SBAR();
            M0<1, BLK>(mV); PV(5); LDL<1>(vV1, srdV, soV); VRD(11); FM(6); SBAR();
            PV(6);  VRD(10); FM(7); FM(8); FM(9); l = vadd(l, ps0); SBAR();
            M0<2, BLK>(mV); PV(7); LDL<2>(vV1, srdV, soV); FM(10); FM(11); kb[0] += dk; kb[1] += dk; PIN(kb[0]); PIN(kb[1]); SBAR();
            PV(8);
        } else {
            QK(0);  EXP(4); EXP(5); ps0 = vadd(X[0], X[1]); SBAR();
            QK(1);  KRD(7); EXP(6); EXP(7); ps1 = vadd(X[2], X[3]); SBAR();
            QK(2);  KRD(6); EXP(8); CV(p0, 0, 0); ADD0(4); ADD1(5); SBAR();
            M0<0, 0>(mK); QK(3); LDL<0>(vK, srdK, soK); KRD(9); EXP(9); SBAR();
            QK(4);  KRD(8); EXP(10); CV(p0, 1, 2); CV(p0, 2, 4); SBAR();
            M0<1, 0>(mK); QK(5); LDL<1>(vK, srdK, soK); KRD(11); EXP(11); SBAR();
            QK(6);  KRD(10); EXP(12); ADD0(6); CV(p0, 3, 6); ADD1(7); SBAR();
            M0<2, 0>(mK); QK(7); LDL<2>(vK, srdK, soK); VRD(1); EXP(13); SBAR();
            QK(8);  VRD(0); EXP(14); ADD0(8); ADD1(9); SBAR();
            QK(9);  VRD(3); EXP(15); CV(p1, 0, 8); SBAR();
            QK(10); VRD(2); ADD0(10); ADD1(11); CV(p1, 1, 10); SBAR();
            QK(11); VRD(5); ADD0(12); ADD1(13); CV(p1, 2, 12); SBAR();
            M0<0, 0>(mV); PV(0); LDL<0>(vV, srdV, soV); VRD(4); ADD0(14); SBAR();
            PV(1);  VRD(7); ADD1(15); CV(p1, 3, 14); SBAR();
            if (__builtin_expect(u + 1 == diagu, 0)) { asm volatile("s_nop 7" : "+v"(S)); mask(u + 1); }
            M0<1, 0>(mV); PV(2); LDL<1>(vV, srdV, soV); VRD(6); FM(0); SBAR();
            PV(3);  VRD(9); FM(1); FM(2); FM(3); ps0 = vadd(ps0, ps1); SBAR();
            M0<2, 0>(mV); PV(4); LDL<2>(vV, srdV, soV); VRD(8); FM(4); SBAR();
            PV(5);  VRD(11); FM(5); FM(6); FM(7); l = vadd(l, ps0); SBAR();
            PV(6);  VRD(10); FM(8); FM(9); FM(10); SBAR();
            PV(7);  FM(11); FM(12); FM(13); kb[0] += dk; kb[1] += dk; PIN(kb[0]); PIN(kb[1]); SBAR();
            PV(8);
        }
        LOOPSYNC();
        KRD(1); KRD(0); KRD(3); KRD(2);
        if (TB) {
            M0<0, 0>(wb + r2); PV(9); LDL<0>(vK, srdK, soK + 2 * KBLK); KRD(5); FM(12); FM(13); SBAR();
            PV(10); KRD(4); FM(14); FM(15); EXP(0); vb += dv; vbo += dv; PIN(vb); PIN(vbo); SBAR();
            M0<1, 0>(wb + r2); PV(11); LDL<1>(vK, srdK, soK + 2 * KBLK); EXP(1); EXP(2); EXP(3); SBAR();
        } else {
            PV(9);  KRD(5); FM(14); FM(15); EXP(0); SBAR();
            PV(10); KRD(4); EXP(1); EXP(2); vb += dv; vbo += dv; PIN(vb); PIN(vbo); SBAR();
            PV(11); EXP(3); SBAR();
        }
        ROTATE();
    }
    STAMPC(9);
#define TAIL_HEAD() \
        const bool last = u == NU - 1; \
        if (!last) { \
            if (TB) { dma<2, 0>(wb + r0, vK, srdK, so + 4 * KBLK); dma3(wb + r0 + BLK, vK1, srdK, so + 4 * KBLK); } \
            else { dma3(u + 3 < NU ? wb + r0 : LDS_NOWHERE, vK, srdK, so + 3 * KBLK); dma3(u + 2 < NU ? wb + 3 * BLK + r2 : LDS_NOWHERE, vV, srdV, so + 2 * KBLK); } \
        }
#define TAIL_MID() \
        if (TB) { if (last) { WAIT_VM(0); } else { WAIT_VM(6); } BARRIER(); }
#define TAIL_END() \
        if (!last) { BK_WAIT(); BARRIER(); if (TB) { dma<0, 0>(wb + r2, vK, srdK, so + 6 * KBLK); dma<1, 0>(wb + r2, vK, srdK, so + 6 * KBLK); } }
    if (u < nu) {
        TAIL_HEAD()
#pragma unroll
        for (int e = 4; e < 8; ++e) EXP(e);
        float ps0 = vadd(X[0], X[1]), ps1 = vadd(X[2], X[3]);
        CV(p0, 0, 0); CV(p0, 1, 2); EXP(8); EXP(9); CV(p0, 2, 4); CV(p0, 3, 6);
        TAIL_MID()
        VRD(1); VRD(0); VRD(3); VRD(2); VRD(5); VRD(4);
        EXP(10); EXP(11); EXP(12); EXP(13); SBAR();
        const unsigned mVt = last ? LDS_NOWHERE : wb + r1;
        PV(0);  if (TB) dma<0, 0>(mVt, vV, srdV, so + 2 * KBLK); EXP(14); EXP(15); SBAR();
        PV(1);  VRD(7); ADD0(4); ADD1(5); SBAR();
        PV(2);  VRD(6); if (TB) dma<1, 0>(mVt, vV, srdV, so + 2 * KBLK); CV(p1, 0, 8); CV(p1, 1, 10); SBAR();
        PV(3);  VRD(9); CV(p1, 2, 12); CV(p1, 3, 14); SBAR();
        PV(4);  VRD(8); if (TB) dma<2, 0>(mVt, vV, srdV, so + 2 * KBLK); ADD0(6); ADD1(7); SBAR();
        PV(5);  VRD(11); ADD0(8); ADD1(9); ADD0(10); SBAR();
        PV(6);  VRD(10); if (TB) dma<0, BLK>(mVt, vV1, srdV, so + 2 * KBLK); ADD1(11); ADD0(12); SBAR();
        PV(7);  if (TB) dma<1, BLK>(mVt, vV1, srdV, so + 2 * KBLK); ADD1(13); SBAR();
        PV(8);  if (TB) dma<2, BLK>(mVt, vV1, srdV, so + 2 * KBLK); ADD0(14); ADD1(15); SBAR();
        PV(9); PV(10); PV(11);
        l += ps0 + ps1;
        TAIL_END()
        ROTATE();
    }
    while (u < NU) {
        TAIL_HEAD()
        TAIL_MID()
        if (TB && !last) { dma3(wb + r1, vV, srdV, so + 2 * KBLK); dma3(wb + r1 + BLK, vV1, srdV, so + 2 * KBLK); }
        TAIL_END()
        ROTATE();
    }
#undef TAIL_HEAD
#undef TAIL_MID
#undef TAIL_END
    OPIN("s_nop 15");
    int lane2;
    asm volatile("v_mbcnt_lo_u32_b32 %0, -1, 0\n\tv_mbcnt_hi_u32_b32 %0, -1, %0" : "=v"(lane2));

    const int l32e = lane2 & 31, hie = lane2 >> 5, r8e = lane2 >> 3, c8e = lane2 & 7, r16e = lane2 >> 4;
    STAMP(5);
    const int reg = TB ? (sub ? r2 : r1) + hsel * BLK : (wid < 3 ? wid * BLK : 3 * BLK + r0);
    l = swapsum(l);
    if (__builtin_expect(__any(!(l < LBIG)), 0)) {
        const unsigned mb = ldsb + reg;
        const int dr2 = (lane2 >> 2) & 7, dh2 = lane2 >> 5, dc2 = lane2 & 3, dg2 = (lane2 >> 4) & 1;
        const unsigned vK0 = dr2 * KROW + 64u * dh2 + 16u * (dc2 ^ dg2), vK1_ = dr2 * KROW + 64u * dh2 + 16u * (dc2 ^ (2 + dg2)), vV0 = dr2 * KROW + 64u * dh2 + 16u * dc2;
        kb[0] = lp + reg + (l32e >> 3) * 1024 + (l32e & 7) * 64 + 16 * (hie ^ ((l32e >> 2) & 3));
        kb[1] = lp + reg + (l32e >> 3) * 1024 + (l32e & 7) * 64 + 16 * ((2 + hie) ^ ((l32e >> 2) & 3));
        vb = lp + reg + (4 * hie + ((lane2 & 15) >> 2)) * 64 + 32 * ((lane2 >> 4) & 1) + 8 * (lane2 & 3);
        vbo = vb + (TB ? 512 - sub * 512 : 512); vb += TB ? sub * 512 : 0;
        bf16x8 q2[12];
        {
            const unsigned vQ0 = dr2 * QROW + 64u * dh2 + 16u * (dc2 ^ dg2), vQ1 = dr2 * QROW + 64u * dh2 + 16u * (dc2 ^ (2 + dg2));
            const unsigned qs = (unsigned)(q0 + (TB ? 0 : 32 * sub)) * QROW + hsel * 384;
#pragma unroll
            for (int g = 0; g < 4; ++g) dma3(mb + g * 1024, (g & 1) ? vQ1 : vQ0, srdQ, qs + g * 8 * QROW);
            WAIT_VM(0);
#pragma unroll
            for (int ks = 0; ks < 12; ++ks) q2[ks] = ldk(KADDR(ks));
        }
        auto scores = [&](int t) {
            const int b = TB ? 2 * t + sub : t;
            asm volatile("s_waitcnt lgkmcnt(0)" ::: "memory");
#pragma unroll
            for (int g = 0; g < 4; ++g) dma3(mb + g * 1024, (g & 1) ? vK1_ : vK0, srdK, b * KBLK + g * 8 * KROW);
            WAIT_VM(0);
            S = MFMA(ldk(KADDR(0)), q2[0], f32x16{});
#pragma unroll
            for (int ks = 1; ks < 12; ++ks) S = MFMA(ldk(KADDR(ks)), q2[ks], S);
            if (t == diagu) {
                const int k0 = b * 32 + 4 * hie - (q0 + (TB ? 0 : 32 * sub) + l32e);
#pragma unroll
                for (int r = 0; r < 16; ++r) if (k0 + (r & 3) + 8 * (r >> 2) > 0) S[r] = -__builtin_inff();
            }
        };
        float mx = -__builtin_inff();
#pragma unroll 1
        for (int t = 0; t < nu; ++t) { scores(t); asm volatile("s_nop 15" : "+v"(S)); mx = max2f(mx, rowmax()); }
        m = mx * C2;
#pragma unroll
        for (int i = 0; i < 6; ++i) o[i] = f32x16{};
        l = 0.f;
#pragma unroll 1
        for (int t = 0; t < nu; ++t) {
            scores(t);
            const int b = TB ? 2 * t + sub : t;
            asm volatile("s_waitcnt lgkmcnt(0)" ::: "memory");
#pragma unroll
            for (int g = 0; g < 4; ++g) dma3(mb + g * 1024, vV0, srdV, b * KBLK + g * 8 * KROW);
            float ps = 0.f;
#pragma unroll
            for (int e = 0; e < 16; ++e) { X[e] = __builtin_amdgcn_exp2f(__builtin_fmaf(S[e], C2, -m)); ps += X[e]; }
            l += ps;
            p0 = (u32x4){cvtpk(X[0], X[1]), cvtpk(X[2], X[3]), cvtpk(X[4], X[5]), cvtpk(X[6], X[7])};
            p1 = (u32x4){cvtpk(X[8], X[9]), cvtpk(X[10], X[11]), cvtpk(X[12], X[13]), cvtpk(X[14], X[15])};
            WAIT_VM(0);
            #pragma unroll
            for (int k = 0; k < 12; ++k) { VRD(k); o[k % 6] = MFMA(VFR(k), __builtin_bit_cast(bf16x8, k < 6 ? p0 : p1), o[k % 6]); }
        }
        l = swapsum(l);
        asm volatile("s_waitcnt lgkmcnt(0)" ::: "memory");
    }

    const __amdgpu_buffer_rsrc_t srdO = __builtin_amdgcn_make_buffer_rsrc((void*)((char*)O + (size_t)s0 * QROW + kvh * 768), (short)0, (int)((unsigned)L * QROW - kvh * 768), 0x00020000);
    const lds_ptr lw = (lds_ptr)lds;
    const unsigned vO = r8e * QROW + c8e * 16;
#define STG(base, db, g, lo, hi_) *(__attribute__((address_space(3))) u32x2*)((base) + ((db) >> 1) * 4096 + 16 * (((4 * (db) + (g)) & 7) ^ (l32e & 7))) = (u32x2){lo, hi_}
    if constexpr (!TB) {
        if (nu > 0) {
            float il = __builtin_amdgcn_rcpf(l);
            asm volatile("s_nop 1" : "+v"(il));
            const lds_ptr wp = lw + reg + l32e * 128 + 8 * hie;
            const lds_ptr rp = lw + reg + r8e * 128 + 16 * (c8e ^ r8e);
            const unsigned ob = (unsigned)(q0 + 32 * sub) * QROW + hsel * 384;
            u32x4 w[4];
            auto scale = [&](int db) {
                float y[16];
#pragma unroll
                for (int r = 0; r < 16; ++r) y[r] = vmul(o[db][r], il);
                unsigned c[8];
#pragma unroll
                for (int r = 0; r < 8; ++r) c[r] = vcvt(y[2 * r], y[2 * r + 1]);
#pragma unroll
                for (int g = 0; g < 4; ++g) STG(wp, db, g, c[2 * g], c[2 * g + 1]);
                SBAR();
            };
            auto rd = [&](int s) {
#pragma unroll
                for (int g = 0; g < 4; ++g) w[g] = *(const __attribute__((address_space(3))) u32x4*)(rp + s * 4096 + g * 1024);
                SBAR();
            };
            auto st_ = [&](int s) {
#pragma unroll
                for (int g = 0; g < 4; ++g) __builtin_amdgcn_raw_buffer_store_b128(w[g], srdO, vO + s * 128, ob + g * 8 * QROW, STAUX);
                SBAR();
            };
            scale(0); scale(1); rd(0); scale(2); st_(0); scale(3); rd(1); scale(4); st_(1); scale(5); rd(2); st_(2);
        }
    } else {
        const lds_ptr xp = lw + reg + lane2 * 16;
        const lds_ptr xq = lw + (sub ? r1 : r2) + hsel * BLK + lane2 * 16;
        __attribute__((address_space(3))) f32x2* ml = (__attribute__((address_space(3))) f32x2*)(lw + LDS_MISC);
        ml[wid * 64 + lane2] = (f32x2){m, l};
#pragma unroll
        for (int i = 0; i < 3; ++i)
#pragma unroll
            for (int g = 0; g < 4; ++g) *(__attribute__((address_space(3))) f32x4*)(xp + (4 * i + g) * 1024) = (f32x4){o[2 * i + 1][4 * g], o[2 * i + 1][4 * g + 1], o[2 * i + 1][4 * g + 2], o[2 * i + 1][4 * g + 3]};
        BARRIER();
        const f32x2 pm = ml[(wid ^ 2) * 64 + lane2];
        f32x4 x[12];
#pragma unroll
        for (int i = 0; i < 12; ++i) x[i] = *(const __attribute__((address_space(3))) f32x4*)(xq + i * 1024);
        SBAR();
        const float mm = max2f(m, pm[0]);
        const float fa = __builtin_amdgcn_exp2f(m - mm), fb = __builtin_amdgcn_exp2f(pm[0] - mm);
        const float il = __builtin_amdgcn_rcpf(l * fa + pm[1] * fb);
        float wa = fa * il, wb2 = fb * il;
        asm volatile("s_nop 1" : "+v"(wa), "+v"(wb2));
        const lds_ptr wp = lw + r0 + hsel * BLK + l32e * 128 + 8 * hie + 16 * ((4 * sub) ^ (l32e & 4));
#pragma unroll
        for (int i = 0; i < 3; ++i) {
            float y[16];
#pragma unroll
            for (int r = 0; r < 16; ++r) y[r] = vmul(x[4 * i + (r >> 2)][r & 3], wb2);
#pragma unroll
            for (int r = 0; r < 16; ++r) y[r] = vfma_v(o[2 * i][r], wa, y[r]);
            unsigned c[8];
#pragma unroll
            for (int r = 0; r < 8; ++r) c[r] = vcvt(y[2 * r], y[2 * r + 1]);
#pragma unroll
            for (int g = 0; g < 4; ++g) *(__attribute__((address_space(3))) u32x2*)(wp + i * 4096 + 16 * (g ^ (l32e & 3))) = (u32x2){c[2 * g], c[2 * g + 1]};
            SBAR();
        }
        BARRIER();
        const lds_ptr rp = lw + r0 + hsel * BLK + sub * 2048 + r8e * 128 + 16 * (c8e ^ r8e);
        const unsigned ob = (unsigned)(q0 + 16 * sub) * QROW + hsel * 384;
        u32x4 w[6];
#pragma unroll
        for (int i = 0; i < 6; ++i) w[i] = *(const __attribute__((address_space(3))) u32x4*)(rp + (i % 3) * 4096 + (i / 3) * 1024);
#pragma unroll
        for (int i = 0; i < 6; ++i) __builtin_amdgcn_raw_buffer_store_b128(w[i], srdO, vO + (i % 3) * 128, ob + (i / 3) * 8 * QROW, STAUX);
    }
#undef STG
    STAMP(6);
#undef EXP
#undef FM
#undef KRD
#undef VRD
#undef VFR
#undef QK
#undef PV
#undef ADD0
#undef ADD1
#undef ROTATE
#undef BK_WAIT
#undef CV
#undef KADDR
}

DI int wave_max63(int x) {
    asm volatile("s_nop 1\n\tv_max_i32_dpp %0, %0, %0 row_shr:1 row_mask:0xf bank_mask:0xf\n\ts_nop 1\n\tv_max_i32_dpp %0, %0, %0 row_shr:2 row_mask:0xf bank_mask:0xf\n\t"
                 "s_nop 1\n\tv_max_i32_dpp %0, %0, %0 row_shr:4 row_mask:0xf bank_mask:0xf\n\ts_nop 1\n\tv_max_i32_dpp %0, %0, %0 row_shr:8 row_mask:0xf bank_mask:0xf\n\t"
                 "s_nop 1\n\tv_max_i32_dpp %0, %0, %0 row_bcast:15 row_mask:0xa bank_mask:0xf\n\ts_nop 1\n\tv_max_i32_dpp %0, %0, %0 row_bcast:31 row_mask:0xc bank_mask:0xf\n\ts_nop 0" : "+v"(x));
    return x;
}
constexpr int first_pure_b() { int i = TH; while (WB * ((i + 2) >> 1) < WA * TH) ++i; return i; }
constexpr int I0 = first_pure_b();
constexpr int NMIX = (I0 - TH) + TH / 2;
static_assert(NMIX <= 12 && I0 < 60, "level table (16 levels; thresholds are inline constants)");
constexpr int level(int k) {
    if (k < 4) return (I0 + 3 - k) | 0x10000;
    k -= 4;
    if (k >= NMIX) return (int)0xffff0040;
    int i = I0 - 1, jj = TH / 2 - 1, out = 0;
    for (int lev = 0; lev <= k; ++lev) {
        const bool useB = i >= TH && (jj < 0 || WB * ((i + 2) >> 1) >= WA * (2 * jj + 2));
        if (useB) { out = i | 0x10000; --i; } else { out = 2 * jj; --jj; }
    }
    return out;
}

__global__ __launch_bounds__(256, 2) void attn_v12(const unsigned short* __restrict__ Q, const unsigned short* __restrict__ K,
                                                  const unsigned short* __restrict__ V, const int* __restrict__ cu,
                                                  unsigned short* __restrict__ O, int nseq, int T, unsigned long long* dbg) {
    extern __shared__ __attribute__((aligned(16))) char lds[];
    unsigned long long st[16];
    STAMP(0);
    const int lane = threadIdx.x & 63;
    const int kvh = blockIdx.x & 7, j = blockIdx.x >> 3;
    if (PFCODE > 0) {
        unsigned long long pc;
        asm volatile("s_getpc_b64 %0" : "=s"(pc));
        const __amdgpu_buffer_rsrc_t srdC = __builtin_amdgcn_make_buffer_rsrc((void*)(pc & ~127ull), (short)0, PFCODE, 0x00020000);
        const unsigned pm = (unsigned)(uintptr_t)lds + LDS_MISC + 1024;
        asm volatile("s_mov_b32 m0, %0\n\ts_nop 0\n\tbuffer_load_dword %1, %2, 0 offen lds\n\tbuffer_load_dword %1, %2, 0 offen offset:128 lds" :: "s"(pm), "v"(threadIdx.x * 256u), "s"(srdC) : "memory");
    }
    const int wv = rfl(threadIdx.x >> 6);
    unsigned vp, vq;
    LaneAddr la;
    {
        const int jj = j & 31, pj = (j >> 5) * (32 - PFSKIP) + jj - PFSKIP;
        constexpr int LPW = (8192 + 4 * (64 - 2 * PFSKIP) - 1) / (4 * (64 - 2 * PFSKIP));
        const unsigned li = (pj * 4 + wv) * LPW + lane, tk = li / 3u;
        vp = (tk * KROW + (li - 3u * tk) * 128u) | ((PFB && lane < LPW && j < 64 && jj >= PFSKIP) ? 0u : OOB);
        const unsigned lq = (j * 4 + wv) * 64 + lane, tq = lq / 6u;
        vq = (tq * QROW + (lq - 6u * tq) * 128u) | ((PFQ && j < 64) ? 0u : OOB);
        const int dr = (lane >> 2) & 7, dh = lane >> 5, dc = lane & 3, dg = (lane >> 4) & 1;
        la.vK = (8 * wv + dr) * KROW + 64u * dh + 16u * (dc ^ ((2 * wv + dg) & 3));
        la.vV = (8 * wv + dr) * KROW + 64u * dh + 16u * dc;
        la.vQ0 = dr * QROW + 64u * dh + 16u * (dc ^ dg);
        la.vQ1 = dr * QROW + 64u * dh + 16u * (dc ^ (2 + dg));
        asm volatile("" : "+v"(vp), "+v"(vq), "+v"(la.vK), "+v"(la.vV), "+v"(la.vQ0), "+v"(la.vQ1));
    }
    asm volatile("" :: "s"(Q), "s"(K), "s"(V), "s"(O), "s"(cu), "s"(nseq), "s"(T));
    int r = j < 32 ? j : (j < 64 ? 95 - j : j);
    int a, b;
    if (nseq <= 15) {
        const unsigned pm = (unsigned)(uintptr_t)lds + LDS_MISC + wv * 256;
        const __amdgpu_buffer_rsrc_t srdKa = __builtin_amdgcn_make_buffer_rsrc((void*)((const char*)K + kvh * 384), (short)0, (int)((unsigned)T * KROW - kvh * 384), 0x00020000);
        const __amdgpu_buffer_rsrc_t srdVa = __builtin_amdgcn_make_buffer_rsrc((void*)((const char*)V + kvh * 384), (short)0, (int)((unsigned)T * KROW - kvh * 384), 0x00020000);
        const __amdgpu_buffer_rsrc_t srdQa = __builtin_amdgcn_make_buffer_rsrc((void*)((const char*)Q + kvh * 768), (short)0, (int)((unsigned)T * QROW - kvh * 768), 0x00020000);
        i32x16 c;
        if (PFQ) asm volatile("s_load_dwordx16 %0, %1, 0x0\n\ts_mov_b32 m0, %2\n\ts_nop 0\n\tbuffer_load_dword %6, %7, 0 offen lds\n\tbuffer_load_dword %3, %4, 0 offen lds\n\tbuffer_load_dword %3, %5, 0 offen lds\n\ts_waitcnt lgkmcnt(0)"
                     : "=&s"(c) : "s"(cu), "s"(pm), "v"(vp), "s"(srdKa), "s"(srdVa), "v"(vq), "s"(srdQa) : "memory");
        else asm volatile("s_load_dwordx16 %0, %1, 0x0\n\ts_mov_b32 m0, %2\n\ts_nop 0\n\tbuffer_load_dword %3, %4, 0 offen lds\n\tbuffer_load_dword %3, %5, 0 offen lds\n\ts_waitcnt lgkmcnt(0)"
                     : "=&s"(c) : "s"(cu), "s"(pm), "v"(vp), "s"(srdKa), "s"(srdVa) : "memory");
        asm volatile("v_mov_b32 %0, %1\n\tv_writelane_b32 %0, %2, 1\n\tv_writelane_b32 %0, %3, 2\n\tv_writelane_b32 %0, %4, 3\n\tv_writelane_b32 %0, %5, 4\n\tv_writelane_b32 %0, %6, 5\n\t"
                     "v_writelane_b32 %0, %7, 6\n\tv_writelane_b32 %0, %8, 7\n\tv_writelane_b32 %0, %9, 8\n\tv_writelane_b32 %0, %10, 9\n\tv_writelane_b32 %0, %11, 10\n\t"
                     "v_writelane_b32 %0, %12, 11\n\tv_writelane_b32 %0, %13, 12\n\tv_writelane_b32 %0, %14, 13\n\tv_writelane_b32 %0, %15, 14\n\tv_writelane_b32 %0, %16, 15\n\ts_nop 1"
                     : "=&v"(a) : "s"(c[0]), "s"(c[1]), "s"(c[2]), "s"(c[3]), "s"(c[4]), "s"(c[5]), "s"(c[6]), "s"(c[7]), "s"(c[8]), "s"(c[9]), "s"(c[10]), "s"(c[11]), "s"(c[12]), "s"(c[13]), "s"(c[14]), "s"(c[15]));
        b = __builtin_amdgcn_update_dpp(a, a, 0x101, 0xf, 0xf, false);
    } else {
        u32x2 ab;
        asm volatile("global_load_dwordx2 %0, %1, %2\n\ts_waitcnt vmcnt(0)" : "=&v"(ab) : "v"(min(lane, nseq - 1) * 4), "s"(cu) : "memory");
        a = ab[0]; b = ab[1];
    }
    const int n = lane < nseq ? (b - a + 31) >> 5 : 0;
    int code = -1;
    unsigned long long msk = 0;
    if (__builtin_expect(__ballot(n > I0 + 4) != 0, 0)) {
        const int nmax = __builtin_amdgcn_readlane(wave_max63(n), 63);
        for (int i = nmax - 1; i >= I0 + 4; --i) {
            msk = __ballot(n > i);
            const int c = __popcll(msk);
            if (r < c) { code = i | 0x10000; goto found; }
            r -= c;
        }
    }
    {
        int cnt;
        asm volatile("v_cmp_lt_i32_e64 %0, (%c5&65535), %4\n\ts_bcnt1_i32_b64 %2, %0\n\ts_mov_b32 %3, %c5\n\ts_cmp_lt_u32 %1, %2\n\ts_cbranch_scc1 1f\n\ts_sub_u32 %1, %1, %2\n\t"
                     "v_cmp_lt_i32_e64 %0, (%c6&65535), %4\n\ts_bcnt1_i32_b64 %2, %0\n\ts_mov_b32 %3, %c6\n\ts_cmp_lt_u32 %1, %2\n\ts_cbranch_scc1 1f\n\ts_sub_u32 %1, %1, %2\n\t"
                     "v_cmp_lt_i32_e64 %0, (%c7&65535), %4\n\ts_bcnt1_i32_b64 %2, %0\n\ts_mov_b32 %3, %c7\n\ts_cmp_lt_u32 %1, %2\n\ts_cbranch_scc1 1f\n\ts_sub_u32 %1, %1, %2\n\t"
                     "v_cmp_lt_i32_e64 %0, (%c8&65535), %4\n\ts_bcnt1_i32_b64 %2, %0\n\ts_mov_b32 %3, %c8\n\ts_cmp_lt_u32 %1, %2\n\ts_cbranch_scc1 1f\n\ts_sub_u32 %1, %1, %2\n\t"
                     "v_cmp_lt_i32_e64 %0, (%c9&65535), %4\n\ts_bcnt1_i32_b64 %2, %0\n\ts_mov_b32 %3, %c9\n\ts_cmp_lt_u32 %1, %2\n\ts_cbranch_scc1 1f\n\ts_sub_u32 %1, %1, %2\n\t"
                     "v_cmp_lt_i32_e64 %0, (%c10&65535), %4\n\ts_bcnt1_i32_b64 %2, %0\n\ts_mov_b32 %3, %c10\n\ts_cmp_lt_u32 %1, %2\n\ts_cbranch_scc1 1f\n\ts_sub_u32 %1, %1, %2\n\t"
                     "v_cmp_lt_i32_e64 %0, (%c11&65535), %4\n\ts_bcnt1_i32_b64 %2, %0\n\ts_mov_b32 %3, %c11\n\ts_cmp_lt_u32 %1, %2\n\ts_cbranch_scc1 1f\n\ts_sub_u32 %1, %1, %2\n\t"
                     "v_cmp_lt_i32_e64 %0, (%c12&65535), %4\n\ts_bcnt1_i32_b64 %2, %0\n\ts_mov_b32 %3, %c12\n\ts_cmp_lt_u32 %1, %2\n\ts_cbranch_scc1 1f\n\ts_sub_u32 %1, %1, %2\n\t"
                     "v_cmp_lt_i32_e64 %0, (%c13&65535), %4\n\ts_bcnt1_i32_b64 %2, %0\n\ts_mov_b32 %3, %c13\n\ts_cmp_lt_u32 %1, %2\n\ts_cbranch_scc1 1f\n\ts_sub_u32 %1, %1, %2\n\t"
                     "v_cmp_lt_i32_e64 %0, (%c14&65535), %4\n\ts_bcnt1_i32_b64 %2, %0\n\ts_mov_b32 %3, %c14\n\ts_cmp_lt_u32 %1, %2\n\ts_cbranch_scc1 1f\n\ts_sub_u32 %1, %1, %2\n\t"
                     "v_cmp_lt_i32_e64 %0, (%c15&65535), %4\n\ts_bcnt1_i32_b64 %2, %0\n\ts_mov_b32 %3, %c15\n\ts_cmp_lt_u32 %1, %2\n\ts_cbranch_scc1 1f\n\ts_sub_u32 %1, %1, %2\n\t"
                     "v_cmp_lt_i32_e64 %0, (%c16&65535), %4\n\ts_bcnt1_i32_b64 %2, %0\n\ts_mov_b32 %3, %c16\n\ts_cmp_lt_u32 %1, %2\n\ts_cbranch_scc1 1f\n\ts_sub_u32 %1, %1, %2\n\t"
                     "v_cmp_lt_i32_e64 %0, (%c17&65535), %4\n\ts_bcnt1_i32_b64 %2, %0\n\ts_mov_b32 %3, %c17\n\ts_cmp_lt_u32 %1, %2\n\ts_cbranch_scc1 1f\n\ts_sub_u32 %1, %1, %2\n\t"
                     "v_cmp_lt_i32_e64 %0, (%c18&65535), %4\n\ts_bcnt1_i32_b64 %2, %0\n\ts_mov_b32 %3, %c18\n\ts_cmp_lt_u32 %1, %2\n\ts_cbranch_scc1 1f\n\ts_sub_u32 %1, %1, %2\n\t"
                     "v_cmp_lt_i32_e64 %0, (%c19&65535), %4\n\ts_bcnt1_i32_b64 %2, %0\n\ts_mov_b32 %3, %c19\n\ts_cmp_lt_u32 %1, %2\n\ts_cbranch_scc1 1f\n\ts_sub_u32 %1, %1, %2\n\t"
                     "v_cmp_lt_i32_e64 %0, (%c20&65535), %4\n\ts_bcnt1_i32_b64 %2, %0\n\ts_mov_b32 %3, %c20\n\ts_cmp_lt_u32 %1, %2\n\ts_cbranch_scc1 1f\n\ts_sub_u32 %1, %1, %2\n\t"
                     "s_mov_b32 %3, -1\n1:"
                     : "=&s"(msk), "+s"(r), "=&s"(cnt), "=&s"(code) : "v"(n), "i"(level(0)), "i"(level(1)), "i"(level(2)), "i"(level(3)), "i"(level(4)), "i"(level(5)), "i"(level(6)), "i"(level(7)), "i"(level(8)), "i"(level(9)), "i"(level(10)), "i"(level(11)), "i"(level(12)), "i"(level(13)), "i"(level(14)), "i"(level(15)) : "scc");
    }
found:
    if (__builtin_expect(code < 0, 0)) { asm volatile("s_waitcnt vmcnt(0)" ::: "memory"); return; }
    const int tb = code >> 16, idx = (code & 0xffff) >> (1 - tb);
    const unsigned below = __builtin_amdgcn_mbcnt_hi((unsigned)(msk >> 32), __builtin_amdgcn_mbcnt_lo((unsigned)msk, 0u));
    const unsigned long long hit = __ballot(below == (unsigned)r) & msk;
    const int sl = __builtin_ctzll(hit);
    const int s0 = min(max(__builtin_amdgcn_readlane(a, sl), 0), T), L = min(__builtin_amdgcn_readlane(b, sl), T) - s0;
    if (__builtin_expect(L <= 0, 0)) { asm volatile("s_waitcnt vmcnt(0)" ::: "memory"); return; }
    if (DELAY2 > 0 && j >= 32) __builtin_amdgcn_s_sleep(DELAY2);
    STAMP(1);
    if (tb) attn_item<true>(st, Q, K, V, O, kvh, s0, L, idx, lds, wv, la);
    else attn_item<false>(st, Q, K, V, O, kvh, s0, L, idx, lds, wv, la);
}

extern "C" void kernel_launch(void* const* d_in, const int* in_sizes, int n_in, void* d_out, int out_size, void* d_ws, size_t ws_size, hipStream_t stream) {
    const int T = in_sizes[0] / (HQ * D), nseq = in_sizes[3] - 1;
    int slots = (T + 31) / 32 + nseq;
    if (slots < 64) slots = 64;
    (void)hipFuncSetAttribute((const void*)attn_v12, hipFuncAttributeMaxDynamicSharedMemorySize, LDS_BYTES);
    attn_v12<<<slots * 8, 256, LDS_BYTES, stream>>>((const unsigned short*)d_in[0], (const unsigned short*)d_in[1], (const unsigned short*)d_in[2],
                                                 (const int*)d_in[3], (unsigned short*)d_out, nseq, T, (unsigned long long*)d_ws);
}
```

```cpp
#include <hip/hip_runtime.h>
#include <hip/hip_bf16.h>
#include <stdint.h>

typedef short bf16x8 __attribute__((ext_vector_type(8)));
typedef short s16x4 __attribute__((ext_vector_type(4)));
typedef float f32x16 __attribute__((ext_vector_type(16)));
typedef float f32x4 __attribute__((ext_vector_type(4)));
typedef float f32x2 __attribute__((ext_vector_type(2)));
typedef unsigned u32x4 __attribute__((ext_vector_type(4)));
typedef unsigned u32x2 __attribute__((ext_vector_type(2)));
typedef int i32x16 __attribute__((ext_vector_type(16)));
typedef __attribute__((address_space(3))) const char* lds_cptr;
typedef __attribute__((address_space(3))) char* lds_ptr;
typedef short v4i16_t __attribute__((ext_vector_type(4)));

constexpr int HQ = 16, HKV = 8, D = 192;
constexpr unsigned QROW = HQ * D * 2, KROW = HKV * D * 2;
constexpr unsigned KBLK = 32 * KROW;
constexpr int BLK = 12288;
constexpr int LDS_MISC = 6 * BLK;
constexpr int LDS_BYTES = LDS_MISC + 2048;
constexpr float C2 = 0.07216878364870322f * 1.4426950408889634f;
constexpr float LBIG = 1073741824.f;
#define TH 8
#define PFKV 0
#define PFFIRST 0
#define PFSKIP 0
#define QNT 0
#define PFCODE 0
#define PFSPLIT 0
#define STAUX 2
#define PFB 1
#define DELAY2 0
#define PFQ 0
#define WA 10
#define WB 11
constexpr unsigned LDS_NOWHERE = 0x30000u;
constexpr unsigned OOB = 0x80000000u;

#define DI __device__ __forceinline__
#define MFMA(a, b, c) __builtin_amdgcn_mfma_f32_32x32x16_bf16(a, b, c, 0, 0, 0)
#define SBAR() __builtin_amdgcn_sched_barrier(0)
#define PIN(x) asm volatile("" : "+v"(x))
typedef __bf16 bf16x2_t __attribute__((ext_vector_type(2)));
DI unsigned cvtpk(float lo, float hi) { const f32x2 v = {lo, hi}; return __builtin_bit_cast(unsigned, __builtin_convertvector(v, bf16x2_t)); }
DI float max3f(float a, float b, float c) { float r; asm("v_max3_f32 %0, %1, %2, %3" : "=v"(r) : "v"(a), "v"(b), "v"(c)); return r; }
DI float max2f(float a, float b) { float r; asm("v_max_f32_e32 %0, %1, %2" : "=v"(r) : "v"(a), "v"(b)); return r; }
DI float fmul_s(float a, float b) { float r; asm("v_mul_f32_e32 %0, %1, %2" : "=v"(r) : "v"(a), "v"(b)); return r; }
DI float fma_v(float a, float b, float c) { float r; asm("v_fma_f32 %0, %1, %2, %3" : "=v"(r) : "v"(a), "v"(b), "v"(c)); return r; }
DI void vexp(float& x) { asm volatile("v_exp_f32_e32 %0, %0" : "+v"(x)); }
DI float vadd(float a, float b) { float r; asm volatile("v_add_f32_e32 %0, %1, %2" : "=v"(r) : "v"(a), "v"(b)); return r; }
DI float vfma(float a, float b, float c) { float r; asm volatile("v_fma_f32 %0, %1, %2, %3" : "=v"(r) : "v"(a), "s"(b), "v"(c)); return r; }
DI float vmul(float a, float b) { float r; asm volatile("v_mul_f32_e32 %0, %1, %2" : "=v"(r) : "v"(a), "v"(b)); return r; }
DI float vfma_v(float a, float b, float c) { float r; asm volatile("v_fma_f32 %0, %1, %2, %3" : "=v"(r) : "v"(a), "v"(b), "v"(c)); return r; }
DI unsigned vcvt(float lo, float hi) { unsigned r; asm volatile("v_cvt_pk_bf16_f32 %0, %1, %2" : "=v"(r) : "v"(lo), "v"(hi)); return r; }
DI s16x4 vtr(lds_cptr p) { return __builtin_bit_cast(s16x4, __builtin_amdgcn_ds_read_tr16_b64_v4i16((__attribute__((address_space(3))) v4i16_t*)p)); }
DI bf16x8 ldk(lds_cptr p) { return *(const __attribute__((address_space(3))) bf16x8*)p; }
DI int rfl(int x) { return __builtin_amdgcn_readfirstlane(x); }
DI float swapmax(float x) { auto r = __builtin_amdgcn_permlane32_swap(__float_as_uint(x), __float_as_uint(x), false, false); return max2f(__uint_as_float(r[0]), __uint_as_float(r[1])); }
DI float swapsum(float x) { auto r = __builtin_amdgcn_permlane32_swap(__float_as_uint(x), __float_as_uint(x), false, false); return __uint_as_float(r[0]) + __uint_as_float(r[1]); }

template <int S, int OFF> DI void dma(unsigned mb, unsigned voff, __amdgpu_buffer_rsrc_t srd, unsigned soff) {
    asm volatile("s_add_u32 m0, %0, %5\n\ts_nop 0\n\tbuffer_load_dwordx4 %1, %2, %3 offen offset:%4 lds"
                 :: "s"(mb), "v"(voff), "s"(srd), "s"(soff), "i"(128 * S), "i"(OFF + S * (4096 - 128)) : "memory", "scc");
}
template <int S, int OFF> DI void setm0(unsigned mb) { asm volatile("s_add_u32 m0, %0, %1" :: "s"(mb), "i"(OFF + S * (4096 - 128)) : "scc"); __builtin_amdgcn_sched_barrier(0); }
template <int S> DI void ldm0(unsigned voff, __amdgpu_buffer_rsrc_t srd, unsigned soff) {
    asm volatile("buffer_load_dwordx4 %0, %1, %2 offen offset:%3 lds" :: "v"(voff), "s"(srd), "s"(soff), "i"(128 * S) : "memory");
}
template <int S> DI void dmaq(unsigned mb, unsigned voff, __amdgpu_buffer_rsrc_t srd, unsigned soff) {
    if (QNT == 1) asm volatile("s_add_u32 m0, %0, %5\n\ts_nop 0\n\tbuffer_load_dwordx4 %1, %2, %3 offen offset:%4 nt lds" :: "s"(mb), "v"(voff), "s"(srd), "s"(soff), "i"(128 * S), "i"(S * (4096 - 128)) : "memory", "scc");
    else if (QNT == 2) asm volatile("s_add_u32 m0, %0, %5\n\ts_nop 0\n\tbuffer_load_dwordx4 %1, %2, %3 offen offset:%4 sc1 lds" :: "s"(mb), "v"(voff), "s"(srd), "s"(soff), "i"(128 * S), "i"(S * (4096 - 128)) : "memory", "scc");
    else asm volatile("s_add_u32 m0, %0, %5\n\ts_nop 0\n\tbuffer_load_dwordx4 %1, %2, %3 offen offset:%4 lds" :: "s"(mb), "v"(voff), "s"(srd), "s"(soff), "i"(128 * S), "i"(S * (4096 - 128)) : "memory", "scc");
}
DI void dma3(unsigned mb, unsigned voff, __amdgpu_buffer_rsrc_t srd, unsigned soff) { dma<0, 0>(mb, voff, srd, soff); dma<1, 0>(mb, voff, srd, soff); dma<2, 0>(mb, voff, srd, soff); }
#define DMAL dma
#define LOOPBAR() BARRIER()
#define LOOPWAIT() WAIT_VM(6)
#define LOOPSYNC() asm volatile("s_waitcnt vmcnt(6) lgkmcnt(0)\n\ts_barrier" ::: "memory")
#define M0 setm0
#define LDL ldm0
#define WAIT_VM(n) asm volatile("s_waitcnt vmcnt(" #n ")" ::: "memory")
#define BARRIER() asm volatile("s_waitcnt lgkmcnt(0)\n\ts_barrier" ::: "memory")
#define OPIN(nop) asm volatile(nop : "+v"(o[0]), "+v"(o[1]), "+v"(o[2]), "+v"(o[3]), "+v"(o[4]), "+v"(o[5]))

#define STAMP(i)
#define STAMPC(i)

struct LaneAddr { unsigned vK, vV, vQ0, vQ1, vp; };
DI void prefetch_v(const unsigned short* V, int kvh, int T, unsigned vp, unsigned pm) {
    const __amdgpu_buffer_rsrc_t srd = __builtin_amdgcn_make_buffer_rsrc((void*)((const char*)V + kvh * 384), (short)0, (int)((unsigned)T * (HKV * D * 2) - kvh * 384), 0x00020000);
    asm volatile("s_mov_b32 m0, %0\n\ts_nop 0\n\tbuffer_load_dword %1, %2, 0 offen lds" :: "s"(pm), "v"(vp), "s"(srd) : "memory");
}

template <bool TB>
DI void attn_item(unsigned long long* st, const unsigned short* __restrict__ Q, const unsigned short* __restrict__ K, const unsigned short* __restrict__ V,
                  unsigned short* __restrict__ O, int kvh, int s0, int L, int idx, char* lds, int wid, const LaneAddr& la, int T) {
    const int tid = threadIdx.x, lane = tid & 63, l32 = lane & 31, hi = lane >> 5;
    const int hsel = wid & 1, sub = wid >> 1;
    const int n = (L + 31) >> 5;
    const int q0 = TB ? 32 * idx : 64 * idx;
    const int NB = TB ? idx + 1 : min(2 * idx + 2, n);
    const int NU = TB ? (NB + 1) >> 1 : NB;
    const int nu = TB ? (NB + 1 - sub) >> 1 : (2 * idx + sub < n ? 2 * idx + sub + 1 : 0);
    const int diagu = TB ? (((idx & 1) == sub) ? nu - 1 : -1) : nu - 1;

    const unsigned krec = (unsigned)min(L, 32 * NB) * KROW - kvh * 384;
    const __amdgpu_buffer_rsrc_t srdK = __builtin_amdgcn_make_buffer_rsrc((void*)((const char*)K + (size_t)s0 * KROW + kvh * 384), (short)0, (int)krec, 0x00020000);
    const __amdgpu_buffer_rsrc_t srdQ = __builtin_amdgcn_make_buffer_rsrc((void*)((const char*)Q + (size_t)s0 * QROW + kvh * 768), (short)0, (int)((unsigned)L * QROW - kvh * 768), 0x00020000);
    const unsigned ldsb = (unsigned)(uintptr_t)lds;
    const unsigned wb = ldsb + wid * 1024;
    const unsigned vK = la.vK, vV = la.vV;

    dma3(wb, vK, srdK, 0);
    if (TB) dma3(wb + BLK, vK, srdK, KBLK);
    {
        const unsigned qreg = TB ? 4 * BLK + hsel * BLK : (wid == 3 ? 2 * BLK : (3 + wid) * BLK);
        const unsigned qs = (unsigned)(q0 + (TB ? 16 * sub : 32 * sub)) * QROW + hsel * 384;
        const unsigned m = ldsb + qreg + (TB ? 2048 * sub : 0);
#pragma unroll
        for (int g = 0; g < (TB ? 2 : 4); ++g) { const unsigned vq_ = (g & 1) ? la.vQ1 : la.vQ0; dmaq<0>(m + g * 1024, vq_, srdQ, qs + g * 8 * QROW); dmaq<1>(m + g * 1024, vq_, srdQ, qs + g * 8 * QROW); dmaq<2>(m + g * 1024, vq_, srdQ, qs + g * 8 * QROW); }
    }
    if (PFSPLIT) {
        if (TB) { dma3(wb + 2 * BLK, vK, srdK, 2 * KBLK); dma3(wb + 3 * BLK, vK, srdK, 3 * KBLK); } else dma3(wb + BLK, vK, srdK, KBLK);
        prefetch_v(V, kvh, T, la.vp, ldsb + LDS_MISC + wid * 256);
    }
    SBAR();
    const __amdgpu_buffer_rsrc_t srdV = __builtin_amdgcn_make_buffer_rsrc((void*)((const char*)V + (size_t)s0 * KROW + kvh * 384), (short)0, (int)krec, 0x00020000);
    const int qrel = q0 + (TB ? 0 : 32 * sub) + l32;
    const lds_cptr lp = (lds_cptr)lds;
    lds_cptr kb[2], vb, vbo;
#pragma unroll
    for (int i = 0; i < 2; ++i) kb[i] = lp + (l32 >> 3) * 1024 + (l32 & 7) * 64 + 16 * ((2 * i + hi) ^ ((l32 >> 2) & 3));
    vb = lp + (4 * hi + ((lane & 15) >> 2)) * 64 + 32 * ((lane >> 4) & 1) + 8 * (lane & 3);
#define KADDR(ks) (kb[(ks) & 1] + ((ks) >> 2) * 4096 + (((ks) >> 1) & 1) * 512)
    f32x16 o[6];
#pragma unroll
    for (int i = 0; i < 6; ++i) o[i] = f32x16{};
    OPIN("");

    STAMP(2);
    bf16x8 qf[12];
    if (PFSPLIT) { if (TB) { WAIT_VM(7); } else { WAIT_VM(4); } } else { WAIT_VM(0); }
    BARRIER();
    const lds_cptr qb0 = kb[0] + (TB ? 4 * BLK + hsel * BLK : (wid == 3 ? 2 * BLK : (3 + wid) * BLK)), qb1 = qb0 + (kb[1] - kb[0]);
    STAMP(3);
    kb[0] += TB ? sub * BLK : 0; kb[1] += TB ? sub * BLK : 0;
    vb += TB ? sub * BLK : 3 * BLK;
    vbo = vb + (TB ? 512 - sub * 512 : 512);
    vb += TB ? sub * 512 : 0;

    f32x16 S, X;
    float m = 0.f, l = 0.f;
    auto mask = [&](int kblock) {
        const int d = qrel - kblock * 32 - 4 * hi;
        const float ninf = -__builtin_inff();
        unsigned long long k[16];
#define MK(r) asm volatile("v_cmp_le_i32_e64 %0, %2, %1" : "=s"(k[r]) : "v"(d), "i"(((r) & 3) + 8 * ((r) >> 2)))
#define MS(r) do { float t_ = S[r]; asm volatile("v_cndmask_b32_e64 %0, %1, %0, %2" : "+v"(t_) : "v"(ninf), "s"(k[r])); S[r] = t_; } while (0)
        MK(0); MK(1); MK(2); MK(3); MK(4); MK(5); MK(6); MK(7); MK(8); MK(9); MK(10); MK(11); MK(12); MK(13); MK(14); MK(15);
        MS(0); MS(1); MS(2); MS(3); MS(4); MS(5); MS(6); MS(7); MS(8); MS(9); MS(10); MS(11); MS(12); MS(13); MS(14); MS(15);
#undef MK
#undef MS
    };
    auto rowmax = [&]() -> float {
        const float a = max3f(S[0], S[1], S[2]), b = max3f(S[3], S[4], S[5]), c = max3f(S[6], S[7], S[8]), d = max3f(S[9], S[10], S[11]), e = max3f(S[12], S[13], S[14]);
        return swapmax(max2f(max3f(a, b, S[15]), max3f(c, d, e)));
    };
    const unsigned vK1 = vK + KBLK, vV1 = vV + KBLK;
    u32x4 p0, p1;
    bf16x8 kf[6];
    s16x4 vlo[6], vhi[6];
#define EXP(e) do { float t_ = X[e]; vexp(t_); X[e] = t_; } while (0)
#define FM(e) X[e] = vfma(S[e], C2, nm)
#define KRD(k) do { kf[(k) % 6] = ldk(KADDR(k)); SBAR(); } while (0)
#define VRD(k) do { const lds_cptr vp_ = ((k) & 1 ? vbo : vb) + (((k) % 6) >> 1) * 4096 + ((k) / 6) * 2048; vlo[(k) % 6] = vtr(vp_); vhi[(k) % 6] = vtr(vp_ + 1024); SBAR(); } while (0)
#define VFR(k) ((bf16x8){vlo[(k) % 6][0], vlo[(k) % 6][1], vlo[(k) % 6][2], vlo[(k) % 6][3], vhi[(k) % 6][0], vhi[(k) % 6][1], vhi[(k) % 6][2], vhi[(k) % 6][3]})
#define QK(k) do { S = MFMA(kf[(k) % 6], qf[k], (k) == 0 ? f32x16{} : S); PIN(S); SBAR(); } while (0)
#define PV(k) do { o[(k) % 6] = MFMA(VFR(k), __builtin_bit_cast(bf16x8, (k) < 6 ? p0 : p1), o[(k) % 6]); PIN(o[(k) % 6]); SBAR(); } while (0)
#define ADD0(e) ps0 = vadd(ps0, X[e])
#define ADD1(e) ps1 = vadd(ps1, X[e])
#define CV(P, i, e) P[i] = vcvt(X[e], X[(e) + 1])

    int r0 = 0, r1 = TB ? 2 * BLK : BLK, r2 = TB ? 4 * BLK : 2 * BLK;
    const unsigned mV1 = (TB || 1 < NB) ? wb + (TB ? 5 : 4) * BLK : LDS_NOWHERE, mK2 = 2 < NB ? wb + 2 * BLK : LDS_NOWHERE;
#define QRD(ks) do { qf[ks] = ldk(((ks) & 1 ? qb1 : qb0) + ((ks) >> 2) * 4096 + (((ks) >> 1) & 1) * 512); SBAR(); } while (0)
    if (nu > 0) {
        QRD(0); KRD(1); KRD(0); QRD(1); QRD(2); QRD(3);
        if (!PFSPLIT) { if (TB) dma<0, 0>(wb + 2 * BLK, vK, srdK, 2 * KBLK); else dma<0, 0>(wb + BLK, vK, srdK, KBLK); }
        KRD(3); KRD(2); QRD(4); QRD(5); QRD(6); QRD(7);
        if (!PFSPLIT) { if (TB) dma<1, 0>(wb + 2 * BLK, vK, srdK, 2 * KBLK); else dma<1, 0>(wb + BLK, vK, srdK, KBLK); }
        KRD(5); KRD(4); QRD(8); QRD(9); QRD(10); QRD(11);
        if (!PFSPLIT) { if (TB) dma<2, 0>(wb + 2 * BLK, vK, srdK, 2 * KBLK); else dma<2, 0>(wb + BLK, vK, srdK, KBLK); }
        SBAR();
        QK(0); if (TB && !PFSPLIT) dma<0, 0>(wb + 3 * BLK, vK, srdK, 3 * KBLK); SBAR();
        QK(1); KRD(7); if (TB && !PFSPLIT) dma<1, 0>(wb + 3 * BLK, vK, srdK, 3 * KBLK); SBAR();
        QK(2); KRD(6); if (TB && !PFSPLIT) dma<2, 0>(wb + 3 * BLK, vK, srdK, 3 * KBLK); SBAR();
        QK(3);
        BARRIER();
        if (TB) {
            QK(4); KRD(9); dma<0, 0>(wb + 4 * BLK, vV, srdV, 0); SBAR();
            QK(5); KRD(8); dma<1, 0>(wb + 4 * BLK, vV, srdV, 0); SBAR();
            QK(6); KRD(11); dma<2, 0>(wb + 4 * BLK, vV, srdV, 0); SBAR();
            QK(7); KRD(10); dma<0, 0>(mV1, vV, srdV, KBLK); SBAR();
            QK(8); dma<1, 0>(mV1, vV, srdV, KBLK); SBAR();
            QK(9);
            QK(10); dma<2, 0>(mV1, vV, srdV, KBLK); SBAR();
            QK(11);
        } else {
            QK(4); KRD(9); dma<0, 0>(wb + 3 * BLK, vV, srdV, 0); SBAR();
            QK(5); KRD(8); dma<1, 0>(wb + 3 * BLK, vV, srdV, 0); SBAR();
            QK(6); KRD(11); dma<2, 0>(wb + 3 * BLK, vV, srdV, 0); SBAR();
            QK(7); KRD(10); dma<0, 0>(mV1, vV, srdV, KBLK); dma<1, 0>(mV1, vV, srdV, KBLK); SBAR();
            QK(8); dma<2, 0>(mV1, vV, srdV, KBLK); SBAR();
            QK(9); dma<0, 0>(mK2, vK, srdK, 2 * KBLK); SBAR();
            QK(10); dma<1, 0>(mK2, vK, srdK, 2 * KBLK); SBAR();
            QK(11); dma<2, 0>(mK2, vK, srdK, 2 * KBLK); SBAR();
        }
    } else {
        if (!PFSPLIT) dma3(wb + BLK, vK, srdK, KBLK);
        BARRIER();
        dma3(wb + 3 * BLK, vV, srdV, 0); dma3(mV1, vV, srdV, KBLK); dma3(mK2, vK, srdK, 2 * KBLK);
    }
#undef QRD
    WAIT_VM(6); BARRIER();
    kb[0] += r1; kb[1] += r1; if (TB) { vb += r2; vbo += r2; }
    if (nu > 1) { KRD(1); KRD(0); KRD(3); KRD(2); KRD(5); KRD(4); }
    if (TB) { dma<0, 0>(wb + r0, vK, srdK, 4 * KBLK); dma<1, 0>(wb + r0, vK, srdK, 4 * KBLK); }
    if (nu > 0) {
        asm volatile("s_nop 11" : "+v"(S));
        if (diagu == 0) mask(TB ? sub : 0);
        m = rowmax() * C2;
        const float nm = -m;
        FM(0); FM(1); FM(2); FM(3); FM(4); FM(5); FM(6); FM(7); FM(8); FM(9); FM(10); FM(11); FM(12); FM(13); FM(14); FM(15);
        EXP(0); EXP(1); EXP(2); EXP(3);
    } else X = f32x16{};
    const float nm = -m;

#define ROTATE() do { const int t_ = TB ? r2 : r0; if (TB) { r2 = r1; r1 = r0; r0 = t_; } else { r0 = r1; r1 = r2; r2 = t_; } so += (TB ? 2 : 1) * KBLK; ++u; } while (0)
#define BK_WAIT() LOOPWAIT()
    STAMP(4); STAMPC(8);
    unsigned barw = 0, barn = 0; (void)barw; (void)barn;
    int u = 0;
    unsigned so = 0;
    while (u + 1 < nu) {
        float ps0, ps1;
        const unsigned soK = so + (TB ? 4 : 3) * KBLK, soV = so + 2 * KBLK;
        const unsigned mK = (TB || u + 3 < NU) ? wb + r0 : LDS_NOWHERE, mV = TB ? wb + r1 : (u + 2 < NU ? wb + 3 * BLK + r2 : LDS_NOWHERE);
        const int dk = TB ? r0 - r1 : r2 - r1, dv = TB ? r1 - r2 : r1 - r0;
        if (TB) {
            QK(0);  EXP(4); EXP(5); ps0 = vadd(X[0], X[1]); SBAR();
            M0<2, 0>(mK); QK(1); LDL<2>(vK, srdK, soK); KRD(7); EXP(6); SBAR();
            QK(2);  KRD(6); EXP(7); EXP(8); ps1 = vadd(X[2], X[3]); SBAR();
            M0<0, BLK>(mK); QK(3); LDL<0>(vK1, srdK, soK); KRD(9); EXP(9); SBAR();
            QK(4);  KRD(8); EXP(10); CV(p0, 0, 0); ADD0(4); ADD1(5); SBAR();
            M0<1, BLK>(mK); QK(5); LDL<1>(vK1, srdK, soK); KRD(11); EXP(11); SBAR();
            QK(6);  KRD(10); EXP(12); CV(p0, 1, 2); CV(p0, 2, 4); SBAR();
            M0<2, BLK>(mK); QK(7); LDL<2>(vK1, srdK, soK); EXP(13); ADD0(6); SBAR();
            QK(8);
            LOOPSYNC();
            VRD(1); VRD(0); VRD(3); VRD(2);
            M0<0, 0>(mV); QK(9); LDL<0>(vV, srdV, soV); VRD(5); EXP(14); SBAR();
            QK(10); VRD(4); EXP(15); CV(p0, 3, 6); ADD1(7); ADD0(8); SBAR();
            M0<1, 0>(mV); QK(11); LDL<1>(vV, srdV, soV); ADD1(9); CV(p1, 0, 8); ADD0(10); SBAR();
            PV(0);  ADD1(11); CV(p1, 1, 10); ADD0(12); ADD1(13); CV(p1, 2, 12); SBAR();
            M0<2, 0>(mV); PV(1); LDL<2>(vV, srdV, soV); VRD(7); ADD0(14); SBAR();
            if (__builtin_expect(u + 1 == diagu, 0)) { asm volatile("s_nop 7" : "+v"(S)); mask(2 * u + 2 + sub); }
            PV(2);  VRD(6); ADD1(15); CV(p1, 3, 14); FM(0); FM(1); SBAR();
            M0<0, BLK>(mV); PV(3); LDL<0>(vV1, srdV, soV); VRD(9); FM(2); SBAR();
            PV(4);  VRD(8); FM(3); FM(4); FM(5); ps0 = vadd(ps0, ps1); SBAR();
            M0<1, BLK>(mV); PV(5); LDL<1>(vV1, srdV, soV); VRD(11); FM(6); SBAR();
            PV(6);  VRD(10); FM(7); FM(8); FM(9); l = vadd(l, ps0); SBAR();
            M0<2, BLK>(mV); PV(7); LDL<2>(vV1, srdV, soV); FM(10); FM(11); kb[0] += dk; kb[1] += dk; PIN(kb[0]); PIN(kb[1]); SBAR();
            PV(8);
        } else {
            QK(0);  EXP(4); EXP(5); ps0 = vadd(X[0], X[1]); SBAR();
            QK(1);  KRD(7); EXP(6); EXP(7); ps1 = vadd(X[2], X[3]); SBAR();
            QK(2);  KRD(6); EXP(8); CV(p0, 0, 0); ADD0(4); ADD1(5); SBAR();
            M0<0, 0>(mK); QK(3); LDL<0>(vK, srdK, soK); KRD(9); EXP(9); SBAR();
            QK(4);  KRD(8); EXP(10); CV(p0, 1, 2); CV(p0, 2, 4); SBAR();
            M0<1, 0>(mK); QK(5); LDL<1>(vK, srdK, soK); KRD(11); EXP(11); SBAR();
            QK(6);  KRD(10); EXP(12); ADD0(6); CV(p0, 3, 6); ADD1(7); SBAR();
            M0<2, 0>(mK); QK(7); LDL<2>(vK, srdK, soK); VRD(1); EXP(13); SBAR();
            QK(8);  VRD(0); EXP(14); ADD0(8); ADD1(9); SBAR();
            QK(9);  VRD(3); EXP(15); CV(p1, 0, 8); SBAR();
            QK(10); VRD(2); ADD0(10); ADD1(11); CV(p1, 1, 10); SBAR();
            QK(11); VRD(5); ADD0(12); ADD1(13); CV(p1, 2, 12); SBAR();
            M0<0, 0>(mV); PV(0); LDL<0>(vV, srdV, soV); VRD(4); ADD0(14); SBAR();
            PV(1);  VRD(7); ADD1(15); CV(p1, 3, 14); SBAR();
            if (__builtin_expect(u + 1 == diagu, 0)) { asm volatile("s_nop 7" : "+v"(S)); mask(u + 1); }
            M0<1, 0>(mV); PV(2); LDL<1>(vV, srdV, soV); VRD(6); FM(0); SBAR();
            PV(3);  VRD(9); FM(1); FM(2); FM(3); ps0 = vadd(ps0, ps1); SBAR();
            M0<2, 0>(mV); PV(4); LDL<2>(vV, srdV, soV); VRD(8); FM(4); SBAR();
            PV(5);  VRD(11); FM(5); FM(6); FM(7); l = vadd(l, ps0); SBAR();
            PV(6);  VRD(10); FM(8); FM(9); FM(10); SBAR();
            PV(7);  FM(11); FM(12); FM(13); kb[0] += dk; kb[1] += dk; PIN(kb[0]); PIN(kb[1]); SBAR();
            PV(8);
        }
        LOOPSYNC();
        KRD(1); KRD(0); KRD(3); KRD(2);
        if (TB) {
            M0<0, 0>(wb + r2); PV(9); LDL<0>(vK, srdK, soK + 2 * KBLK); KRD(5); FM(12); FM(13); SBAR();
            PV(10); KRD(4); FM(14); FM(15); EXP(0); vb += dv; vbo += dv; PIN(vb); PIN(vbo); SBAR();
            M0<1, 0>(wb + r2); PV(11); LDL<1>(vK, srdK, soK + 2 * KBLK); EXP(1); EXP(2); EXP(3); SBAR();
        } else {
            PV(9);  KRD(5); FM(14); FM(15); EXP(0); SBAR();
            PV(10); KRD(4); EXP(1); EXP(2); vb += dv; vbo += dv; PIN(vb); PIN(vbo); SBAR();
            PV(11); EXP(3); SBAR();
        }
        ROTATE();
    }
    STAMPC(9);
#define TAIL_HEAD() \
        const bool last = u == NU - 1; \
        if (!last) { \
            if (TB) { dma<2, 0>(wb + r0, vK, srdK, so + 4 * KBLK); dma3(wb + r0 + BLK, vK1, srdK, so + 4 * KBLK); } \
            else { dma3(u + 3 < NU ? wb + r0 : LDS_NOWHERE, vK, srdK, so + 3 * KBLK); dma3(u + 2 < NU ? wb + 3 * BLK + r2 : LDS_NOWHERE, vV, srdV, so + 2 * KBLK); } \
        }
#define TAIL_MID() \
        if (TB) { if (last) { WAIT_VM(0); } else { WAIT_VM(6); } BARRIER(); }
#define TAIL_END() \
        if (!last) { BK_WAIT(); BARRIER(); if (TB) { dma<0, 0>(wb + r2, vK, srdK, so + 6 * KBLK); dma<1, 0>(wb + r2, vK, srdK, so + 6 * KBLK); } }
    if (u < nu) {
        TAIL_HEAD()
#pragma unroll
        for (int e = 4; e < 8; ++e) EXP(e);
        float ps0 = vadd(X[0], X[1]), ps1 = vadd(X[2], X[3]);
        CV(p0, 0, 0); CV(p0, 1, 2); EXP(8); EXP(9); CV(p0, 2, 4); CV(p0, 3, 6);
        TAIL_MID()
        VRD(1); VRD(0); VRD(3); VRD(2); VRD(5); VRD(4);
        EXP(10); EXP(11); EXP(12); EXP(13); SBAR();
        const unsigned mVt = last ? LDS_NOWHERE : wb + r1;
        PV(0);  if (TB) dma<0, 0>(mVt, vV, srdV, so + 2 * KBLK); EXP(14); EXP(15); SBAR();
        PV(1);  VRD(7); ADD0(4); ADD1(5); SBAR();
        PV(2);  VRD(6); if (TB) dma<1, 0>(mVt, vV, srdV, so + 2 * KBLK); CV(p1, 0, 8); CV(p1, 1, 10); SBAR();
        PV(3);  VRD(9); CV(p1, 2, 12); CV(p1, 3, 14); SBAR();
        PV(4);  VRD(8); if (TB) dma<2, 0>(mVt, vV, srdV, so + 2 * KBLK); ADD0(6); ADD1(7); SBAR();
        PV(5);  VRD(11); ADD0(8); ADD1(9); ADD0(10); SBAR();
        PV(6);  VRD(10); if (TB) dma<0, BLK>(mVt, vV1, srdV, so + 2 * KBLK); ADD1(11); ADD0(12); SBAR();
        PV(7);  if (TB) dma<1, BLK>(mVt, vV1, srdV, so + 2 * KBLK); ADD1(13); SBAR();
        PV(8);  if (TB) dma<2, BLK>(mVt, vV1, srdV, so + 2 * KBLK); ADD0(14); ADD1(15); SBAR();
        PV(9); PV(10); PV(11);
        l += ps0 + ps1;
        TAIL_END()
        ROTATE();
    }
    while (u < NU) {
        TAIL_HEAD()
        TAIL_MID()
        if (TB && !last) { dma3(wb + r1, vV, srdV, so + 2 * KBLK); dma3(wb + r1 + BLK, vV1, srdV, so + 2 * KBLK); }
        TAIL_END()
        ROTATE();
    }
#undef TAIL_HEAD
#undef TAIL_MID
#undef TAIL_END
    OPIN("s_nop 15");
    int lane2;
    asm volatile("v_mbcnt_lo_u32_b32 %0, -1, 0\n\tv_mbcnt_hi_u32_b32 %0, -1, %0" : "=v"(lane2));

    const int l32e = lane2 & 31, hie = lane2 >> 5, r8e = lane2 >> 3, c8e = lane2 & 7, r16e = lane2 >> 4;
    STAMP(5);
    const int reg = TB ? (sub ? r2 : r1) + hsel * BLK : (wid < 3 ? wid * BLK : 3 * BLK + r0);
    l = swapsum(l);
    if (__builtin_expect(__any(!(l < LBIG)), 0)) {
        const unsigned mb = ldsb + reg;
        const int dr2 = (lane2 >> 2) & 7, dh2 = lane2 >> 5, dc2 = lane2 & 3, dg2 = (lane2 >> 4) & 1;
        const unsigned vK0 = dr2 * KROW + 64u * dh2 + 16u * (dc2 ^ dg2), vK1_ = dr2 * KROW + 64u * dh2 + 16u * (dc2 ^ (2 + dg2)), vV0 = dr2 * KROW + 64u * dh2 + 16u * dc2;
        kb[0] = lp + reg + (l32e >> 3) * 1024 + (l32e & 7) * 64 + 16 * (hie ^ ((l32e >> 2) & 3));
        kb[1] = lp + reg + (l32e >> 3) * 1024 + (l32e & 7) * 64 + 16 * ((2 + hie) ^ ((l32e >> 2) & 3));
        vb = lp + reg + (4 * hie + ((lane2 & 15) >> 2)) * 64 + 32 * ((lane2 >> 4) & 1) + 8 * (lane2 & 3);
        vbo = vb + (TB ? 512 - sub * 512 : 512); vb += TB ? sub * 512 : 0;
        bf16x8 q2[12];
        {
            const unsigned vQ0 = dr2 * QROW + 64u * dh2 + 16u * (dc2 ^ dg2), vQ1 = dr2 * QROW + 64u * dh2 + 16u * (dc2 ^ (2 + dg2));
            const unsigned qs = (unsigned)(q0 + (TB ? 0 : 32 * sub)) * QROW + hsel * 384;
#pragma unroll
            for (int g = 0; g < 4; ++g) dma3(mb + g * 1024, (g & 1) ? vQ1 : vQ0, srdQ, qs + g * 8 * QROW);
            WAIT_VM(0);
#pragma unroll
            for (int ks = 0; ks < 12; ++ks) q2[ks] = ldk(KADDR(ks));
        }
        auto scores = [&](int t) {
            const int b = TB ? 2 * t + sub : t;
            asm volatile("s_waitcnt lgkmcnt(0)" ::: "memory");
#pragma unroll
            for (int g = 0; g < 4; ++g) dma3(mb + g * 1024, (g & 1) ? vK1_ : vK0, srdK, b * KBLK + g * 8 * KROW);
            WAIT_VM(0);
            S = MFMA(ldk(KADDR(0)), q2[0], f32x16{});
#pragma unroll
            for (int ks = 1; ks < 12; ++ks) S = MFMA(ldk(KADDR(ks)), q2[ks], S);
            if (t == diagu) {
                const int k0 = b * 32 + 4 * hie - (q0 + (TB ? 0 : 32 * sub) + l32e);
#pragma unroll
                for (int r = 0; r < 16; ++r) if (k0 + (r & 3) + 8 * (r >> 2) > 0) S[r] = -__builtin_inff();
            }
        };
        float mx = -__builtin_inff();
#pragma unroll 1
        for (int t = 0; t < nu; ++t) { scores(t); asm volatile("s_nop 15" : "+v"(S)); mx = max2f(mx, rowmax()); }
        m = mx * C2;
#pragma unroll
        for (int i = 0; i < 6; ++i) o[i] = f32x16{};
        l = 0.f;
#pragma unroll 1
        for (int t = 0; t < nu; ++t) {
            scores(t);
            const int b = TB ? 2 * t + sub : t;
            asm volatile("s_waitcnt lgkmcnt(0)" ::: "memory");
#pragma unroll
            for (int g = 0; g < 4; ++g) dma3(mb + g * 1024, vV0, srdV, b * KBLK + g * 8 * KROW);
            float ps = 0.f;
#pragma unroll
            for (int e = 0; e < 16; ++e) { X[e] = __builtin_amdgcn_exp2f(__builtin_fmaf(S[e], C2, -m)); ps += X[e]; }
            l += ps;
            p0 = (u32x4){cvtpk(X[0], X[1]), cvtpk(X[2], X[3]), cvtpk(X[4], X[5]), cvtpk(X[6], X[7])};
            p1 = (u32x4){cvtpk(X[8], X[9]), cvtpk(X[10], X[11]), cvtpk(X[12], X[13]), cvtpk(X[14], X[15])};
            WAIT_VM(0);
            #pragma unroll
            for (int k = 0; k < 12; ++k) { VRD(k); o[k % 6] = MFMA(VFR(k), __builtin_bit_cast(bf16x8, k < 6 ? p0 : p1), o[k % 6]); }
        }
        l = swapsum(l);
        asm volatile("s_waitcnt lgkmcnt(0)" ::: "memory");
    }

    const __amdgpu_buffer_rsrc_t srdO = __builtin_amdgcn_make_buffer_rsrc((void*)((char*)O + (size_t)s0 * QROW + kvh * 768), (short)0, (int)((unsigned)L * QROW - kvh * 768), 0x00020000);
    const lds_ptr lw = (lds_ptr)lds;
    const unsigned vO = r8e * QROW + c8e * 16;
#define STG(base, db, g, lo, hi_) *(__attribute__((address_space(3))) u32x2*)((base) + ((db) >> 1) * 4096 + 16 * (((4 * (db) + (g)) & 7) ^ (l32e & 7))) = (u32x2){lo, hi_}
    if constexpr (!TB) {
        if (nu > 0) {
            float il = __builtin_amdgcn_rcpf(l);
            asm volatile("s_nop 1" : "+v"(il));
            const lds_ptr wp = lw + reg + l32e * 128 + 8 * hie;
            const lds_ptr rp = lw + reg + r8e * 128 + 16 * (c8e ^ r8e);
            const unsigned ob = (unsigned)(q0 + 32 * sub) * QROW + hsel * 384;
            u32x4 w[4];
            auto scale = [&](int db) {
                float y[16];
#pragma unroll
                for (int r = 0; r < 16; ++r) y[r] = vmul(o[db][r], il);
                unsigned c[8];
#pragma unroll
                for (int r = 0; r < 8; ++r) c[r] = vcvt(y[2 * r], y[2 * r + 1]);
#pragma unroll
                for (int g = 0; g < 4; ++g) STG(wp, db, g, c[2 * g], c[2 * g + 1]);
                SBAR();
            };
            auto rd = [&](int s) {
#pragma unroll
                for (int g = 0; g < 4; ++g) w[g] = *(const __attribute__((address_space(3))) u32x4*)(rp + s * 4096 + g * 1024);
                SBAR();
            };
            auto st_ = [&](int s) {
#pragma unroll
                for (int g = 0; g < 4; ++g) __builtin_amdgcn_raw_buffer_store_b128(w[g], srdO, vO + s * 128, ob + g * 8 * QROW, STAUX);
                SBAR();
            };
            scale(0); scale(1); rd(0); scale(2); st_(0); scale(3); rd(1); scale(4); st_(1); scale(5); rd(2); st_(2);
        }
    } else {
        const lds_ptr xp = lw + reg + lane2 * 16;
        const lds_ptr xq = lw + (sub ? r1 : r2) + hsel * BLK + lane2 * 16;
        __attribute__((address_space(3))) f32x2* ml = (__attribute__((address_space(3))) f32x2*)(lw + LDS_MISC);
        ml[wid * 64 + lane2] = (f32x2){m, l};
#pragma unroll
        for (int i = 0; i < 3; ++i)
#pragma unroll
            for (int g = 0; g < 4; ++g) *(__attribute__((address_space(3))) f32x4*)(xp + (4 * i + g) * 1024) = (f32x4){o[2 * i + 1][4 * g], o[2 * i + 1][4 * g + 1], o[2 * i + 1][4 * g + 2], o[2 * i + 1][4 * g + 3]};
        BARRIER();
        const f32x2 pm = ml[(wid ^ 2) * 64 + lane2];
        f32x4 x[12];
#pragma unroll
        for (int i = 0; i < 12; ++i) x[i] = *(const __attribute__((address_space(3))) f32x4*)(xq + i * 1024);
        SBAR();
        const float mm = max2f(m, pm[0]);
        const float fa = __builtin_amdgcn_exp2f(m - mm), fb = __builtin_amdgcn_exp2f(pm[0] - mm);
        const float il = __builtin_amdgcn_rcpf(l * fa + pm[1] * fb);
        float wa = fa * il, wb2 = fb * il;
        asm volatile("s_nop 1" : "+v"(wa), "+v"(wb2));
        const lds_ptr wp = lw + r0 + hsel * BLK + l32e * 128 + 8 * hie + 16 * ((4 * sub) ^ (l32e & 4));
#pragma unroll
        for (int i = 0; i < 3; ++i) {
            float y[16];
#pragma unroll
            for (int r = 0; r < 16; ++r) y[r] = vmul(x[4 * i + (r >> 2)][r & 3], wb2);
#pragma unroll
            for (int r = 0; r < 16; ++r) y[r] = vfma_v(o[2 * i][r], wa, y[r]);
            unsigned c[8];
#pragma unroll
            for (int r = 0; r < 8; ++r) c[r] = vcvt(y[2 * r], y[2 * r + 1]);
#pragma unroll
            for (int g = 0; g < 4; ++g) *(__attribute__((address_space(3))) u32x2*)(wp + i * 4096 + 16 * (g ^ (l32e & 3))) = (u32x2){c[2 * g], c[2 * g + 1]};
            SBAR();
        }
        BARRIER();
        const lds_ptr rp = lw + r0 + hsel * BLK + sub * 2048 + r8e * 128 + 16 * (c8e ^ r8e);
        const unsigned ob = (unsigned)(q0 + 16 * sub) * QROW + hsel * 384;
        u32x4 w[6];
#pragma unroll
        for (int i = 0; i < 6; ++i) w[i] = *(const __attribute__((address_space(3))) u32x4*)(rp + (i % 3) * 4096 + (i / 3) * 1024);
#pragma unroll
        for (int i = 0; i < 6; ++i) __builtin_amdgcn_raw_buffer_store_b128(w[i], srdO, vO + (i % 3) * 128, ob + (i / 3) * 8 * QROW, STAUX);
    }
#undef STG
    STAMP(6);
#undef EXP
#undef FM
#undef KRD
#undef VRD
#undef VFR
#undef QK
#undef PV
#undef ADD0
#undef ADD1
#undef ROTATE
#undef BK_WAIT
#undef CV
#undef KADDR
}

DI int wave_max63(int x) {
    asm volatile("s_nop 1\n\tv_max_i32_dpp %0, %0, %0 row_shr:1 row_mask:0xf bank_mask:0xf\n\ts_nop 1\n\tv_max_i32_dpp %0, %0, %0 row_shr:2 row_mask:0xf bank_mask:0xf\n\t"
                 "s_nop 1\n\tv_max_i32_dpp %0, %0, %0 row_shr:4 row_mask:0xf bank_mask:0xf\n\ts_nop 1\n\tv_max_i32_dpp %0, %0, %0 row_shr:8 row_mask:0xf bank_mask:0xf\n\t"
                 "s_nop 1\n\tv_max_i32_dpp %0, %0, %0 row_bcast:15 row_mask:0xa bank_mask:0xf\n\ts_nop 1\n\tv_max_i32_dpp %0, %0, %0 row_bcast:31 row_mask:0xc bank_mask:0xf\n\ts_nop 0" : "+v"(x));
    return x;
}
constexpr int first_pure_b() { int i = TH; while (WB * ((i + 2) >> 1) < WA * TH) ++i; return i; }
constexpr int I0 = first_pure_b();
constexpr int NMIX = (I0 - TH) + TH / 2;
static_assert(NMIX <= 12 && I0 < 60, "level table (16 levels; thresholds are inline constants)");
constexpr int level(int k) {
    if (k < 4) return (I0 + 3 - k) | 0x10000;
    k -= 4;
    if (k >= NMIX) return (int)0xffff0040;
    int i = I0 - 1, jj = TH / 2 - 1, out = 0;
    for (int lev = 0; lev <= k; ++lev) {
        const bool useB = i >= TH && (jj < 0 || WB * ((i + 2) >> 1) >= WA * (2 * jj + 2));
        if (useB) { out = i | 0x10000; --i; } else { out = 2 * jj; --jj; }
    }
    return out;
}

__global__ __launch_bounds__(256, 2) void attn_v14(const unsigned short* __restrict__ Q, const unsigned short* __restrict__ K,
                                                  const unsigned short* __restrict__ V, const int* __restrict__ cu,
                                                  unsigned short* __restrict__ O, int nseq, int T, unsigned long long* dbg) {
    extern __shared__ __attribute__((aligned(16))) char lds[];
    unsigned long long st[16];
    STAMP(0);
    const int lane = threadIdx.x & 63;
    const int kvh = blockIdx.x & 7, j = blockIdx.x >> 3;
    if (PFCODE > 0) {
        unsigned long long pc;
        asm volatile("s_getpc_b64 %0" : "=s"(pc));
        const __amdgpu_buffer_rsrc_t srdC = __builtin_amdgcn_make_buffer_rsrc((void*)(pc & ~127ull), (short)0, PFCODE, 0x00020000);
        const unsigned pm = (unsigned)(uintptr_t)lds + LDS_MISC + 1024;
        asm volatile("s_mov_b32 m0, %0\n\ts_nop 0\n\tbuffer_load_dword %1, %2, 0 offen lds\n\tbuffer_load_dword %1, %2, 0 offen offset:128 lds" :: "s"(pm), "v"(threadIdx.x * 256u), "s"(srdC) : "memory");
    }
    const int wv = rfl(threadIdx.x >> 6);
    unsigned vp, vq;
    LaneAddr la;
    {
        const int jj = j & 31, pj = (j >> 5) * (32 - PFSKIP) + jj - PFSKIP;
        constexpr int LPW = (8192 + 4 * (64 - 2 * PFSKIP) - 1) / (4 * (64 - 2 * PFSKIP));
        const unsigned li = (pj * 4 + wv) * LPW + lane, tk = li / 3u;
        vp = (tk * KROW + (li - 3u * tk) * 128u) | ((PFB && lane < LPW && j < 64 && jj >= PFSKIP) ? 0u : OOB);
        const unsigned lq = (j * 4 + wv) * 64 + lane, tq = lq / 6u;
        vq = (tq * QROW + (lq - 6u * tq) * 128u) | ((PFQ && j < 64) ? 0u : OOB);
        const int dr = (lane >> 2) & 7, dh = lane >> 5, dc = lane & 3, dg = (lane >> 4) & 1;
        la.vK = (8 * wv + dr) * KROW + 64u * dh + 16u * (dc ^ ((2 * wv + dg) & 3));
        la.vV = (8 * wv + dr) * KROW + 64u * dh + 16u * dc;
        la.vQ0 = dr * QROW + 64u * dh + 16u * (dc ^ dg);
        la.vQ1 = dr * QROW + 64u * dh + 16u * (dc ^ (2 + dg));
        la.vp = vp;
        asm volatile("" : "+v"(vp), "+v"(vq), "+v"(la.vK), "+v"(la.vV), "+v"(la.vQ0), "+v"(la.vQ1));
    }
    asm volatile("" :: "s"(Q), "s"(K), "s"(V), "s"(O), "s"(cu), "s"(nseq), "s"(T));
    int r = j < 32 ? j : (j < 64 ? 95 - j : j);
    int a, b;
    if (nseq <= 15) {
        const unsigned pm = (unsigned)(uintptr_t)lds + LDS_MISC + wv * 256;
        const __amdgpu_buffer_rsrc_t srdKa = __builtin_amdgcn_make_buffer_rsrc((void*)((const char*)K + kvh * 384), (short)0, (int)((unsigned)T * KROW - kvh * 384), 0x00020000);
        const __amdgpu_buffer_rsrc_t srdVa = __builtin_amdgcn_make_buffer_rsrc((void*)((const char*)V + kvh * 384), (short)0, (int)((unsigned)T * KROW - kvh * 384), 0x00020000);
        i32x16 c;
        if (PFSPLIT) asm volatile("s_load_dwordx16 %0, %1, 0x0\n\ts_mov_b32 m0, %2\n\ts_nop 0\n\tbuffer_load_dword %3, %4, 0 offen lds\n\ts_waitcnt lgkmcnt(0)"
                     : "=&s"(c) : "s"(cu), "s"(pm), "v"(vp), "s"(srdKa) : "memory");
        else asm volatile("s_load_dwordx16 %0, %1, 0x0\n\ts_mov_b32 m0, %2\n\ts_nop 0\n\tbuffer_load_dword %3, %4, 0 offen lds\n\tbuffer_load_dword %3, %5, 0 offen lds\n\ts_waitcnt lgkmcnt(0)"
                     : "=&s"(c) : "s"(cu), "s"(pm), "v"(vp), "s"(srdKa), "s"(srdVa) : "memory");
        asm volatile("v_mov_b32 %0, %1\n\tv_writelane_b32 %0, %2, 1\n\tv_writelane_b32 %0, %3, 2\n\tv_writelane_b32 %0, %4, 3\n\tv_writelane_b32 %0, %5, 4\n\tv_writelane_b32 %0, %6, 5\n\t"
                     "v_writelane_b32 %0, %7, 6\n\tv_writelane_b32 %0, %8, 7\n\tv_writelane_b32 %0, %9, 8\n\tv_writelane_b32 %0, %10, 9\n\tv_writelane_b32 %0, %11, 10\n\t"
                     "v_writelane_b32 %0, %12, 11\n\tv_writelane_b32 %0, %13, 12\n\tv_writelane_b32 %0, %14, 13\n\tv_writelane_b32 %0, %15, 14\n\tv_writelane_b32 %0, %16, 15\n\ts_nop 1"
                     : "=&v"(a) : "s"(c[0]), "s"(c[1]), "s"(c[2]), "s"(c[3]), "s"(c[4]), "s"(c[5]), "s"(c[6]), "s"(c[7]), "s"(c[8]), "s"(c[9]), "s"(c[10]), "s"(c[11]), "s"(c[12]), "s"(c[13]), "s"(c[14]), "s"(c[15]));
        b = __builtin_amdgcn_update_dpp(a, a, 0x101, 0xf, 0xf, false);
    } else {
        u32x2 ab;
        asm volatile("global_load_dwordx2 %0, %1, %2\n\ts_waitcnt vmcnt(0)" : "=&v"(ab) : "v"(min(lane, nseq - 1) * 4), "s"(cu) : "memory");
        a = ab[0]; b = ab[1];
    }
    const int n = lane < nseq ? (b - a + 31) >> 5 : 0;
    int code = -1;
    unsigned long long msk = 0;
    if (__builtin_expect(__ballot(n > I0 + 4) != 0, 0)) {
        const int nmax = __builtin_amdgcn_readlane(wave_max63(n), 63);
        for (int i = nmax - 1; i >= I0 + 4; --i) {
            msk = __ballot(n > i);
            const int c = __popcll(msk);
            if (r < c) { code = i | 0x10000; goto found; }
            r -= c;
        }
    }
    {
        int cnt;
        asm volatile("v_cmp_lt_i32_e64 %0, (%c5&65535), %4\n\ts_bcnt1_i32_b64 %2, %0\n\ts_mov_b32 %3, %c5\n\ts_cmp_lt_u32 %1, %2\n\ts_cbranch_scc1 1f\n\ts_sub_u32 %1, %1, %2\n\t"
                     "v_cmp_lt_i32_e64 %0, (%c6&65535), %4\n\ts_bcnt1_i32_b64 %2, %0\n\ts_mov_b32 %3, %c6\n\ts_cmp_lt_u32 %1, %2\n\ts_cbranch_scc1 1f\n\ts_sub_u32 %1, %1, %2\n\t"
                     "v_cmp_lt_i32_e64 %0, (%c7&65535), %4\n\ts_bcnt1_i32_b64 %2, %0\n\ts_mov_b32 %3, %c7\n\ts_cmp_lt_u32 %1, %2\n\ts_cbranch_scc1 1f\n\ts_sub_u32 %1, %1, %2\n\t"
                     "v_cmp_lt_i32_e64 %0, (%c8&65535), %4\n\ts_bcnt1_i32_b64 %2, %0\n\ts_mov_b32 %3, %c8\n\ts_cmp_lt_u32 %1, %2\n\ts_cbranch_scc1 1f\n\ts_sub_u32 %1, %1, %2\n\t"
                     "v_cmp_lt_i32_e64 %0, (%c9&65535), %4\n\ts_bcnt1_i32_b64 %2, %0\n\ts_mov_b32 %3, %c9\n\ts_cmp_lt_u32 %1, %2\n\ts_cbranch_scc1 1f\n\ts_sub_u32 %1, %1, %2\n\t"
                     "v_cmp_lt_i32_e64 %0, (%c10&65535), %4\n\ts_bcnt1_i32_b64 %2, %0\n\ts_mov_b32 %3, %c10\n\ts_cmp_lt_u32 %1, %2\n\ts_cbranch_scc1 1f\n\ts_sub_u32 %1, %1, %2\n\t"
                     "v_cmp_lt_i32_e64 %0, (%c11&65535), %4\n\ts_bcnt1_i32_b64 %2, %0\n\ts_mov_b32 %3, %c11\n\ts_cmp_lt_u32 %1, %2\n\ts_cbranch_scc1 1f\n\ts_sub_u32 %1, %1, %2\n\t"
                     "v_cmp_lt_i32_e64 %0, (%c12&65535), %4\n\ts_bcnt1_i32_b64 %2, %0\n\ts_mov_b32 %3, %c12\n\ts_cmp_lt_u32 %1, %2\n\ts_cbranch_scc1 1f\n\ts_sub_u32 %1, %1, %2\n\t"
                     "v_cmp_lt_i32_e64 %0, (%c13&65535), %4\n\ts_bcnt1_i32_b64 %2, %0\n\ts_mov_b32 %3, %c13\n\ts_cmp_lt_u32 %1, %2\n\ts_cbranch_scc1 1f\n\ts_sub_u32 %1, %1, %2\n\t"
                     "v_cmp_lt_i32_e64 %0, (%c14&65535), %4\n\ts_bcnt1_i32_b64 %2, %0\n\ts_mov_b32 %3, %c14\n\ts_cmp_lt_u32 %1, %2\n\ts_cbranch_scc1 1f\n\ts_sub_u32 %1, %1, %2\n\t"
                     "v_cmp_lt_i32_e64 %0, (%c15&65535), %4\n\ts_bcnt1_i32_b64 %2, %0\n\ts_mov_b32 %3, %c15\n\ts_cmp_lt_u32 %1, %2\n\ts_cbranch_scc1 1f\n\ts_sub_u32 %1, %1, %2\n\t"
                     "v_cmp_lt_i32_e64 %0, (%c16&65535), %4\n\ts_bcnt1_i32_b64 %2, %0\n\ts_mov_b32 %3, %c16\n\ts_cmp_lt_u32 %1, %2\n\ts_cbranch_scc1 1f\n\ts_sub_u32 %1, %1, %2\n\t"
                     "v_cmp_lt_i32_e64 %0, (%c17&65535), %4\n\ts_bcnt1_i32_b64 %2, %0\n\ts_mov_b32 %3, %c17\n\ts_cmp_lt_u32 %1, %2\n\ts_cbranch_scc1 1f\n\ts_sub_u32 %1, %1, %2\n\t"
                     "v_cmp_lt_i32_e64 %0, (%c18&65535), %4\n\ts_bcnt1_i32_b64 %2, %0\n\ts_mov_b32 %3, %c18\n\ts_cmp_lt_u32 %1, %2\n\ts_cbranch_scc1 1f\n\ts_sub_u32 %1, %1, %2\n\t"
                     "v_cmp_lt_i32_e64 %0, (%c19&65535), %4\n\ts_bcnt1_i32_b64 %2, %0\n\ts_mov_b32 %3, %c19\n\ts_cmp_lt_u32 %1, %2\n\ts_cbranch_scc1 1f\n\ts_sub_u32 %1, %1, %2\n\t"
                     "v_cmp_lt_i32_e64 %0, (%c20&65535), %4\n\ts_bcnt1_i32_b64 %2, %0\n\ts_mov_b32 %3, %c20\n\ts_cmp_lt_u32 %1, %2\n\ts_cbranch_scc1 1f\n\ts_sub_u32 %1, %1, %2\n\t"
                     "s_mov_b32 %3, -1\n1:"
                     : "=&s"(msk), "+s"(r), "=&s"(cnt), "=&s"(code) : "v"(n), "i"(level(0)), "i"(level(1)), "i"(level(2)), "i"(level(3)), "i"(level(4)), "i"(level(5)), "i"(level(6)), "i"(level(7)), "i"(level(8)), "i"(level(9)), "i"(level(10)), "i"(level(11)), "i"(level(12)), "i"(level(13)), "i"(level(14)), "i"(level(15)) : "scc");
    }
found:
    if (__builtin_expect(code < 0, 0)) { if (PFSPLIT) prefetch_v(V, kvh, T, vp, (unsigned)(uintptr_t)lds + LDS_MISC + wv * 256); asm volatile("s_waitcnt vmcnt(0)" ::: "memory"); return; }
    const int tb = code >> 16, idx = (code & 0xffff) >> (1 - tb);
    const unsigned below = __builtin_amdgcn_mbcnt_hi((unsigned)(msk >> 32), __builtin_amdgcn_mbcnt_lo((unsigned)msk, 0u));
    const unsigned long long hit = __ballot(below == (unsigned)r) & msk;
    const int sl = __builtin_ctzll(hit);
    const int s0 = min(max(__builtin_amdgcn_readlane(a, sl), 0), T), L = min(__builtin_amdgcn_readlane(b, sl), T) - s0;
    if (__builtin_expect(L <= 0, 0)) { asm volatile("s_waitcnt vmcnt(0)" ::: "memory"); return; }
    if (DELAY2 > 0 && j >= 32) __builtin_amdgcn_s_sleep(DELAY2);
    STAMP(1);
    if (tb) attn_item<true>(st, Q, K, V, O, kvh, s0, L, idx, lds, wv, la, T);
    else attn_item<false>(st, Q, K, V, O, kvh, s0, L, idx, lds, wv, la, T);
}

extern "C" void kernel_launch(void* const* d_in, const int* in_sizes, int n_in, void* d_out, int out_size, void* d_ws, size_t ws_size, hipStream_t stream) {
    const int T = in_sizes[0] / (HQ * D), nseq = in_sizes[3] - 1;
    int slots = (T + 31) / 32 + nseq;
    if (slots < 64) slots = 64;
    (void)hipFuncSetAttribute((const void*)attn_v14, hipFuncAttributeMaxDynamicSharedMemorySize, LDS_BYTES);
    attn_v14<<<slots * 8, 256, LDS_BYTES, stream>>>((const unsigned short*)d_in[0], (const unsigned short*)d_in[1], (const unsigned short*)d_in[2],
                                                 (const int*)d_in[3], (unsigned short*)d_out, nseq, T, (unsigned long long*)d_ws);
}
```

```cpp
#include <hip/hip_runtime.h>
#include <hip/hip_bf16.h>
#include <stdint.h>

typedef short bf16x8 __attribute__((ext_vector_type(8)));
typedef short s16x4 __attribute__((ext_vector_type(4)));
typedef float f32x16 __attribute__((ext_vector_type(16)));
typedef float f32x4 __attribute__((ext_vector_type(4)));
typedef float f32x2 __attribute__((ext_vector_type(2)));
typedef unsigned u32x4 __attribute__((ext_vector_type(4)));
typedef unsigned u32x2 __attribute__((ext_vector_type(2)));
typedef int i32x16 __attribute__((ext_vector_type(16)));
typedef __attribute__((address_space(3))) const char* lds_cptr;
typedef __attribute__((address_space(3))) char* lds_ptr;
typedef short v4i16_t __attribute__((ext_vector_type(4)));

constexpr int HQ = 16, HKV = 8, D = 192;
constexpr unsigned QROW = HQ * D * 2, KROW = HKV * D * 2;
constexpr unsigned KBLK = 32 * KROW;
constexpr int BLK = 12288;
constexpr int LDS_MISC = 6 * BLK;
constexpr int LDS_BYTES = LDS_MISC + 2048;
constexpr float C2 = 0.07216878364870322f * 1.4426950408889634f;
constexpr float LBIG = 1073741824.f;
#define TH 8
#define PFKV 0
#define PFFIRST 0
#define PFSKIP 0
#define QNT 0
#define PFCODE 0
#define PFSPLIT 0
#define STAUX 2
#define PFB 1
#define DELAY2 0
#define PFQ 0
#define WA 10
#define WB 11
constexpr unsigned LDS_NOWHERE = 0x30000u;
constexpr unsigned OOB = 0x80000000u;

#define DI __device__ __forceinline__
#define MFMA(a, b, c) __builtin_amdgcn_mfma_f32_32x32x16_bf16(a, b, c, 0, 0, 0)
#define SBAR() __builtin_amdgcn_sched_barrier(0)
#define PIN(x) asm volatile("" : "+v"(x))
typedef __bf16 bf16x2_t __attribute__((ext_vector_type(2)));
DI unsigned cvtpk(float lo, float hi) { const f32x2 v = {lo, hi}; return __builtin_bit_cast(unsigned, __builtin_convertvector(v, bf16x2_t)); }
DI float max3f(float a, float b, float c) { float r; asm("v_max3_f32 %0, %1, %2, %3" : "=v"(r) : "v"(a), "v"(b), "v"(c)); return r; }
DI float max2f(float a, float b) { float r; asm("v_max_f32_e32 %0, %1, %2" : "=v"(r) : "v"(a), "v"(b)); return r; }
DI float fmul_s(float a, float b) { float r; asm("v_mul_f32_e32 %0, %1, %2" : "=v"(r) : "v"(a), "v"(b)); return r; }
DI float fma_v(float a, float b, float c) { float r; asm("v_fma_f32 %0, %1, %2, %3" : "=v"(r) : "v"(a), "v"(b), "v"(c)); return r; }
DI void vexp(float& x) { asm volatile("v_exp_f32_e32 %0, %0" : "+v"(x)); }
DI float vadd(float a, float b) { float r; asm volatile("v_add_f32_e32 %0, %1, %2" : "=v"(r) : "v"(a), "v"(b)); return r; }
DI float vfma(float a, float b, float c) { float r; asm volatile("v_fma_f32 %0, %1, %2, %3" : "=v"(r) : "v"(a), "s"(b), "v"(c)); return r; }
DI float vmul(float a, float b) { float r; asm volatile("v_mul_f32_e32 %0, %1, %2" : "=v"(r) : "v"(a), "v"(b)); return r; }
DI float vfma_v(float a, float b, float c) { float r; asm volatile("v_fma_f32 %0, %1, %2, %3" : "=v"(r) : "v"(a), "v"(b), "v"(c)); return r; }
DI unsigned vcvt(float lo, float hi) { unsigned r; asm volatile("v_cvt_pk_bf16_f32 %0, %1, %2" : "=v"(r) : "v"(lo), "v"(hi)); return r; }
DI s16x4 vtr(lds_cptr p) { return __builtin_bit_cast(s16x4, __builtin_amdgcn_ds_read_tr16_b64_v4i16((__attribute__((address_space(3))) v4i16_t*)p)); }
DI bf16x8 ldk(lds_cptr p) { return *(const __attribute__((address_space(3))) bf16x8*)p; }
DI int rfl(int x) { return __builtin_amdgcn_readfirstlane(x); }
DI float swapmax(float x) { auto r = __builtin_amdgcn_permlane32_swap(__float_as_uint(x), __float_as_uint(x), false, false); return max2f(__uint_as_float(r[0]), __uint_as_float(r[1])); }
DI float swapsum(float x) { auto r = __builtin_amdgcn_permlane32_swap(__float_as_uint(x), __float_as_uint(x), false, false); return __uint_as_float(r[0]) + __uint_as_float(r[1]); }

template <int S, int OFF> DI void dma(unsigned mb, unsigned voff, __amdgpu_buffer_rsrc_t srd, unsigned soff) {
    asm volatile("s_add_u32 m0, %0, %5\n\ts_nop 0\n\tbuffer_load_dwordx4 %1, %2, %3 offen offset:%4 lds"
                 :: "s"(mb), "v"(voff), "s"(srd), "s"(soff), "i"(128 * S), "i"(OFF + S * (4096 - 128)) : "memory", "scc");
}
template <int S, int OFF> DI void setm0(unsigned mb) { asm volatile("s_add_u32 m0, %0, %1" :: "s"(mb), "i"(OFF + S * (4096 - 128)) : "scc"); __builtin_amdgcn_sched_barrier(0); }
template <int S> DI void ldm0(unsigned voff, __amdgpu_buffer_rsrc_t srd, unsigned soff) {
    asm volatile("buffer_load_dwordx4 %0, %1, %2 offen offset:%3 lds" :: "v"(voff), "s"(srd), "s"(soff), "i"(128 * S) : "memory");
}
template <int S> DI void dmaq(unsigned mb, unsigned voff, __amdgpu_buffer_rsrc_t srd, unsigned soff) {
    if (QNT == 1) asm volatile("s_add_u32 m0, %0, %5\n\ts_nop 0\n\tbuffer_load_dwordx4 %1, %2, %3 offen offset:%4 nt lds" :: "s"(mb), "v"(voff), "s"(srd), "s"(soff), "i"(128 * S), "i"(S * (4096 - 128)) : "memory", "scc");
    else if (QNT == 2) asm volatile("s_add_u32 m0, %0, %5\n\ts_nop 0\n\tbuffer_load_dwordx4 %1, %2, %3 offen offset:%4 sc1 lds" :: "s"(mb), "v"(voff), "s"(srd), "s"(soff), "i"(128 * S), "i"(S * (4096 - 128)) : "memory", "scc");
    else asm volatile("s_add_u32 m0, %0, %5\n\ts_nop 0\n\tbuffer_load_dwordx4 %1, %2, %3 offen offset:%4 lds" :: "s"(mb), "v"(voff), "s"(srd), "s"(soff), "i"(128 * S), "i"(S * (4096 - 128)) : "memory", "scc");
}
DI void dma3(unsigned mb, unsigned voff, __amdgpu_buffer_rsrc_t srd, unsigned soff) { dma<0, 0>(mb, voff, srd, soff); dma<1, 0>(mb, voff, srd, soff); dma<2, 0>(mb, voff, srd, soff); }
#define DMAL dma
#define LOOPBAR() BARRIER()
#define LOOPWAIT() WAIT_VM(6)
#define LOOPSYNC() asm volatile("s_waitcnt vmcnt(6) lgkmcnt(0)\n\ts_barrier" ::: "memory")
#define M0 setm0
#define LDL ldm0
#define WAIT_VM(n) asm volatile("s_waitcnt vmcnt(" #n ")" ::: "memory")
#define BARRIER() asm volatile("s_waitcnt lgkmcnt(0)\n\ts_barrier" ::: "memory")
#define OPIN(nop) asm volatile(nop : "+v"(o[0]), "+v"(o[1]), "+v"(o[2]), "+v"(o[3]), "+v"(o[4]), "+v"(o[5]))

#define STAMP(i)
#define STAMPC(i)

struct LaneAddr { unsigned vK, vV, vQ0, vQ1, vp; };
DI void prefetch_v(const unsigned short* V, int kvh, int T, unsigned vp, unsigned pm) {
    const __amdgpu_buffer_rsrc_t srd = __builtin_amdgcn_make_buffer_rsrc((void*)((const char*)V + kvh * 384), (short)0, (int)((unsigned)T * (HKV * D * 2) - kvh * 384), 0x00020000);
    asm volatile("s_mov_b32 m0, %0\n\ts_nop 0\n\tbuffer_load_dword %1, %2, 0 offen lds" :: "s"(pm), "v"(vp), "s"(srd) : "memory");
}

template <bool TB>
DI void attn_item(unsigned long long* st, const unsigned short* __restrict__ Q, const unsigned short* __restrict__ K, const unsigned short* __restrict__ V,
                  unsigned short* __restrict__ O, int kvh, int s0, int L, int idx, char* lds, int wid, const LaneAddr& la, int T) {
    const int tid = threadIdx.x, lane = tid & 63, l32 = lane & 31, hi = lane >> 5;
    const int hsel = wid & 1, sub = wid >> 1;
    const int n = (L + 31) >> 5;
    const int q0 = TB ? 32 * idx : 64 * idx;
    const int NB = TB ? idx + 1 : min(2 * idx + 2, n);
    const int NU = TB ? (NB + 1) >> 1 : NB;
    const int nu = TB ? (NB + 1 - sub) >> 1 : (2 * idx + sub < n ? 2 * idx + sub + 1 : 0);
    const int diagu = TB ? (((idx & 1) == sub) ? nu - 1 : -1) : nu - 1;

    const unsigned krec = (unsigned)min(L, 32 * NB) * KROW - kvh * 384;
    const __amdgpu_buffer_rsrc_t srdK = __builtin_amdgcn_make_buffer_rsrc((void*)((const char*)K + (size_t)s0 * KROW + kvh * 384), (short)0, (int)krec, 0x00020000);
    const __amdgpu_buffer_rsrc_t srdQ = __builtin_amdgcn_make_buffer_rsrc((void*)((const char*)Q + (size_t)s0 * QROW + kvh * 768), (short)0, (int)((unsigned)L * QROW - kvh * 768), 0x00020000);
    const unsigned ldsb = (unsigned)(uintptr_t)lds;
    const unsigned wb = ldsb + wid * 1024;
    const unsigned vK = la.vK, vV = la.vV;

    dma3(wb, vK, srdK, 0);
    if (TB) dma3(wb + BLK, vK, srdK, KBLK);
    {
        const unsigned qreg = TB ? 4 * BLK + hsel * BLK : (wid == 3 ? 2 * BLK : (3 + wid) * BLK);
        const unsigned qs = (unsigned)(q0 + (TB ? 16 * sub : 32 * sub)) * QROW + hsel * 384;
        const unsigned m = ldsb + qreg + (TB ? 2048 * sub : 0);
#pragma unroll
        for (int g = 0; g < (TB ? 2 : 4); ++g) { const unsigned vq_ = (g & 1) ? la.vQ1 : la.vQ0; dmaq<0>(m + g * 1024, vq_, srdQ, qs + g * 8 * QROW); dmaq<1>(m + g * 1024, vq_, srdQ, qs + g * 8 * QROW); dmaq<2>(m + g * 1024, vq_, srdQ, qs + g * 8 * QROW); }
    }
    if (PFSPLIT) {
        if (TB) { dma3(wb + 2 * BLK, vK, srdK, 2 * KBLK); dma3(wb + 3 * BLK, vK, srdK, 3 * KBLK); } else dma3(wb + BLK, vK, srdK, KBLK);
        prefetch_v(V, kvh, T, la.vp, ldsb + LDS_MISC + wid * 256);
    }
    SBAR();
    const __amdgpu_buffer_rsrc_t srdV = __builtin_amdgcn_make_buffer_rsrc((void*)((const char*)V + (size_t)s0 * KROW + kvh * 384), (short)0, (int)krec, 0x00020000);
    const int qrel = q0 + (TB ? 0 : 32 * sub) + l32;
    const lds_cptr lp = (lds_cptr)lds;
    lds_cptr kb[2], vb, vbo;
#pragma unroll
    for (int i = 0; i < 2; ++i) kb[i] = lp + (l32 >> 3) * 1024 + (l32 & 7) * 64 + 16 * ((2 * i + hi) ^ ((l32 >> 2) & 3));
    vb = lp + (4 * hi + ((lane & 15) >> 2)) * 64 + 32 * ((lane >> 4) & 1) + 8 * (lane & 3);
#define KADDR(ks) (kb[(ks) & 1] + ((ks) >> 2) * 4096 + (((ks) >> 1) & 1) * 512)
    f32x16 o[6];
#pragma unroll
    for (int i = 0; i < 6; ++i) o[i] = f32x16{};
    OPIN("");

    STAMP(2);
    bf16x8 qf[12];
    if (PFSPLIT) { if (TB) { WAIT_VM(7); } else { WAIT_VM(4); } } else { WAIT_VM(0); }
    BARRIER();
    const lds_cptr qb0 = kb[0] + (TB ? 4 * BLK + hsel * BLK : (wid == 3 ? 2 * BLK : (3 + wid) * BLK)), qb1 = qb0 + (kb[1] - kb[0]);
    STAMP(3);
    kb[0] += TB ? sub * BLK : 0; kb[1] += TB ? sub * BLK : 0;
    vb += TB ? sub * BLK : 3 * BLK;
    vbo = vb + (TB ? 512 - sub * 512 : 512);
    vb += TB ? sub * 512 : 0;

    f32x16 S, X;
    float m = 0.f, l = 0.f;
    auto mask = [&](int kblock) {
        const int d = qrel - kblock * 32 - 4 * hi;
        const float ninf = -__builtin_inff();
        unsigned long long k[16];
#define MK(r) asm volatile("v_cmp_le_i32_e64 %0, %2, %1" : "=s"(k[r]) : "v"(d), "i"(((r) & 3) + 8 * ((r) >> 2)))
#define MS(r) do { float t_ = S[r]; asm volatile("v_cndmask_b32_e64 %0, %1, %0, %2" : "+v"(t_) : "v"(ninf), "s"(k[r])); S[r] = t_; } while (0)
        MK(0); MK(1); MK(2); MK(3); MK(4); MK(5); MK(6); MK(7); MK(8); MK(9); MK(10); MK(11); MK(12); MK(13); MK(14); MK(15);
        MS(0); MS(1); MS(2); MS(3); MS(4); MS(5); MS(6); MS(7); MS(8); MS(9); MS(10); MS(11); MS(12); MS(13); MS(14); MS(15);
#undef MK
#undef MS
    };
    auto rowmax = [&]() -> float {
        const float a = max3f(S[0], S[1], S[2]), b = max3f(S[3], S[4], S[5]), c = max3f(S[6], S[7], S[8]), d = max3f(S[9], S[10], S[11]), e = max3f(S[12], S[13], S[14]);
        return swapmax(max2f(max3f(a, b, S[15]), max3f(c, d, e)));
    };
    const unsigned vK1 = vK + KBLK, vV1 = vV + KBLK;
    u32x4 p0, p1;
    bf16x8 kf[6];
    s16x4 vlo[6], vhi[6];
#define EXP(e) do { float t_ = X[e]; vexp(t_); X[e] = t_; } while (0)
#define FM(e) X[e] = vfma(S[e], C2, nm)
#define KRD(k) do { kf[(k) % 6] = ldk(KADDR(k)); SBAR(); } while (0)
#define VRD(k) do { const lds_cptr vp_ = ((k) & 1 ? vbo : vb) + (((k) % 6) >> 1) * 4096 + ((k) / 6) * 2048; vlo[(k) % 6] = vtr(vp_); vhi[(k) % 6] = vtr(vp_ + 1024); SBAR(); } while (0)
#define VFR(k) ((bf16x8){vlo[(k) % 6][0], vlo[(k) % 6][1], vlo[(k) % 6][2], vlo[(k) % 6][3], vhi[(k) % 6][0], vhi[(k) % 6][1], vhi[(k) % 6][2], vhi[(k) % 6][3]})
#define QK(k) do { S = MFMA(kf[(k) % 6], qf[k], (k) == 0 ? f32x16{} : S); PIN(S); SBAR(); } while (0)
#define PV(k) do { o[(k) % 6] = MFMA(VFR(k), __builtin_bit_cast(bf16x8, (k) < 6 ? p0 : p1), o[(k) % 6]); PIN(o[(k) % 6]); SBAR(); } while (0)
#define ADD0(e) ps0 = vadd(ps0, X[e])
#define ADD1(e) ps1 = vadd(ps1, X[e])
#define CV(P, i, e) P[i] = vcvt(X[e], X[(e) + 1])

    int r0 = 0, r1 = TB ? 2 * BLK : BLK, r2 = TB ? 4 * BLK : 2 * BLK;
    const unsigned mV1 = (TB || 1 < NB) ? wb + (TB ? 5 : 4) * BLK : LDS_NOWHERE, mK2 = 2 < NB ? wb + 2 * BLK : LDS_NOWHERE;
#define QRD(ks) do { qf[ks] = ldk(((ks) & 1 ? qb1 : qb0) + ((ks) >> 2) * 4096 + (((ks) >> 1) & 1) * 512); SBAR(); } while (0)
    if (nu > 0) {
        QRD(0); KRD(1); KRD(0); QRD(1); QRD(2); QRD(3);
        if (!PFSPLIT) { if (TB) dma<0, 0>(wb + 2 * BLK, vK, srdK, 2 * KBLK); else dma<0, 0>(wb + BLK, vK, srdK, KBLK); }
        KRD(3); KRD(2); QRD(4); QRD(5); QRD(6); QRD(7);
        if (!PFSPLIT) { if (TB) dma<1, 0>(wb + 2 * BLK, vK, srdK, 2 * KBLK); else dma<1, 0>(wb + BLK, vK, srdK, KBLK); }
        KRD(5); KRD(4); QRD(8); QRD(9); QRD(10); QRD(11);
        if (!PFSPLIT) { if (TB) dma<2, 0>(wb + 2 * BLK, vK, srdK, 2 * KBLK); else dma<2, 0>(wb + BLK, vK, srdK, KBLK); }
        SBAR();
        QK(0); if (TB && !PFSPLIT) dma<0, 0>(wb + 3 * BLK, vK, srdK, 3 * KBLK); SBAR();
        QK(1); KRD(7); if (TB && !PFSPLIT) dma<1, 0>(wb + 3 * BLK, vK, srdK, 3 * KBLK); SBAR();
        QK(2); KRD(6); if (TB && !PFSPLIT) dma<2, 0>(wb + 3 * BLK, vK, srdK, 3 * KBLK); SBAR();
        QK(3);
        BARRIER();
        if (TB) {
            QK(4); KRD(9); dma<0, 0>(wb + 4 * BLK, vV, srdV, 0); SBAR();
            QK(5); KRD(8); dma<1, 0>(wb + 4 * BLK, vV, srdV, 0); SBAR();
            QK(6); KRD(11); dma<2, 0>(wb + 4 * BLK, vV, srdV, 0); SBAR();
            QK(7); KRD(10); dma<0, 0>(mV1, vV, srdV, KBLK); SBAR();
            QK(8); dma<1, 0>(mV1, vV, srdV, KBLK); SBAR();
            QK(9);
            QK(10); dma<2, 0>(mV1, vV, srdV, KBLK); SBAR();
            QK(11);
        } else {
            QK(4); KRD(9); dma<0, 0>(wb + 3 * BLK, vV, srdV, 0); SBAR();
            QK(5); KRD(8); dma<1, 0>(wb + 3 * BLK, vV, srdV, 0); SBAR();
            QK(6); KRD(11); dma<2, 0>(wb + 3 * BLK, vV, srdV, 0); SBAR();
            QK(7); KRD(10); dma<0, 0>(mV1, vV, srdV, KBLK); dma<1, 0>(mV1, vV, srdV, KBLK); SBAR();
            QK(8); dma<2, 0>(mV1, vV, srdV, KBLK); SBAR();
            QK(9); dma<0, 0>(mK2, vK, srdK, 2 * KBLK); SBAR();
            QK(10); dma<1, 0>(mK2, vK, srdK, 2 * KBLK); SBAR();
            QK(11); dma<2, 0>(mK2, vK, srdK, 2 * KBLK); SBAR();
        }
    } else {
        if (!PFSPLIT) dma3(wb + BLK, vK, srdK, KBLK);
        BARRIER();
        dma3(wb + 3 * BLK, vV, srdV, 0); dma3(mV1, vV, srdV, KBLK); dma3(mK2, vK, srdK, 2 * KBLK);
    }
#undef QRD
    WAIT_VM(6); BARRIER();
    kb[0] += r1; kb[1] += r1; if (TB) { vb += r2; vbo += r2; }
    if (nu > 1) { KRD(1); KRD(0); KRD(3); KRD(2); KRD(5); KRD(4); }
    if (TB) { dma<0, 0>(wb + r0, vK, srdK, 4 * KBLK); dma<1, 0>(wb + r0, vK, srdK, 4 * KBLK); }
    if (nu > 0) {
        asm volatile("s_nop 11" : "+v"(S));
        if (diagu == 0) mask(TB ? sub : 0);
        m = rowmax() * C2;
        const float nm = -m;
        FM(0); FM(1); FM(2); FM(3); FM(4); FM(5); FM(6); FM(7); FM(8); FM(9); FM(10); FM(11); FM(12); FM(13); FM(14); FM(15);
        EXP(0); EXP(1); EXP(2); EXP(3);
    } else X = f32x16{};
    const float nm = -m;

#define ROTATE() do { const int t_ = TB ? r2 : r0; if (TB) { r2 = r1; r1 = r0; r0 = t_; } else { r0 = r1; r1 = r2; r2 = t_; } so += (TB ? 2 : 1) * KBLK; ++u; } while (0)
#define BK_WAIT() LOOPWAIT()
    STAMP(4); STAMPC(8);
    unsigned barw = 0, barn = 0; (void)barw; (void)barn;
    int u = 0;
    unsigned so = 0;
    while (u + 1 < nu) {
        float ps0, ps1;
        const unsigned soK = so + (TB ? 4 : 3) * KBLK, soV = so + 2 * KBLK;
        const unsigned mK = (TB || u + 3 < NU) ? wb + r0 : LDS_NOWHERE, mV = TB ? wb + r1 : (u + 2 < NU ? wb + 3 * BLK + r2 : LDS_NOWHERE);
        const int dk = TB ? r0 - r1 : r2 - r1, dv = TB ? r1 - r2 : r1 - r0;
        if (TB) {
            QK(0);  EXP(4); EXP(5); ps0 = vadd(X[0], X[1]); SBAR();
            M0<2, 0>(mK); QK(1); LDL<2>(vK, srdK, soK); KRD(7); EXP(6); SBAR();
            QK(2);  KRD(6); EXP(7); EXP(8); ps1 = vadd(X[2], X[3]); SBAR();
            M0<0, BLK>(mK); QK(3); LDL<0>(vK1, srdK, soK); KRD(9); EXP(9); SBAR();
            QK(4);  KRD(8); EXP(10); CV(p0, 0, 0); ADD0(4); ADD1(5); SBAR();
            M0<1, BLK>(mK); QK(5); LDL<1>(vK1, srdK, soK); KRD(11); EXP(11); SBAR();
            QK(6);  KRD(10); EXP(12); CV(p0, 1, 2); CV(p0, 2, 4); SBAR();
            M0<2, BLK>(mK); QK(7); LDL<2>(vK1, srdK, soK); EXP(13); ADD0(6); SBAR();
            QK(8);
            LOOPSYNC();
            VRD(1); VRD(0); VRD(3); VRD(2);
            M0<0, 0>(mV); QK(9); LDL<0>(vV, srdV, soV); VRD(5); EXP(14); SBAR();
            QK(10); VRD(4); EXP(15); CV(p0, 3, 6); ADD1(7); ADD0(8); SBAR();
            M0<1, 0>(mV); QK(11); LDL<1>(vV, srdV, soV); ADD1(9); CV(p1, 0, 8); ADD0(10); SBAR();
            PV(0);  ADD1(11); CV(p1, 1, 10); ADD0(12); ADD1(13); CV(p1, 2, 12); SBAR();
            M0<2, 0>(mV); PV(1); LDL<2>(vV, srdV, soV); VRD(7); ADD0(14); SBAR();
            if (__builtin_expect(u + 1 == diagu, 0)) { asm volatile("s_nop 7" : "+v"(S)); mask(2 * u + 2 + sub); }
            PV(2);  VRD(6); ADD1(15); CV(p1, 3, 14); FM(0); FM(1); SBAR();
            M0<0, BLK>(mV); PV(3); LDL<0>(vV1, srdV, soV); VRD(9); FM(2); SBAR();
            PV(4);  VRD(8); FM(3); FM(4); FM(5); ps0 = vadd(ps0, ps1); SBAR();
            M0<1, BLK>(mV); PV(5); LDL<1>(vV1, srdV, soV); VRD(11); FM(6); SBAR();
            PV(6);  VRD(10); FM(7); FM(8); FM(9); l = vadd(l, ps0); SBAR();
            M0<2, BLK>(mV); PV(7); LDL<2>(vV1, srdV, soV); FM(10); FM(11); kb[0] += dk; kb[1] += dk; PIN(kb[0]); PIN(kb[1]); SBAR();
            PV(8);
        } else {
            QK(0);  EXP(4); EXP(5); ps0 = vadd(X[0], X[1]); SBAR();
            QK(1);  KRD(7); EXP(6); EXP(7); ps1 = vadd(X[2], X[3]); SBAR();
            QK(2);  KRD(6); EXP(8); CV(p0, 0, 0); ADD0(4); ADD1(5); SBAR();
            M0<0, 0>(mK); QK(3); LDL<0>(vK, srdK, soK); KRD(9); EXP(9); SBAR();
            QK(4);  KRD(8); EXP(10); CV(p0, 1, 2); CV(p0, 2, 4); SBAR();
            M0<1, 0>(mK); QK(5); LDL<1>(vK, srdK, soK); KRD(11); EXP(11); SBAR();
            QK(6);  KRD(10); EXP(12); ADD0(6); CV(p0, 3, 6); ADD1(7); SBAR();
            M0<2, 0>(mK); QK(7); LDL<2>(vK, srdK, soK); VRD(1); EXP(13); SBAR();
            QK(8);  VRD(0); EXP(14); ADD0(8); ADD1(9); SBAR();
            QK(9);  VRD(3); EXP(15); CV(p1, 0, 8); SBAR();
            QK(10); VRD(2); ADD0(10); ADD1(11); CV(p1, 1, 10); SBAR();
            QK(11); VRD(5); ADD0(12); ADD1(13); CV(p1, 2, 12); SBAR();
            M0<0, 0>(mV); PV(0); LDL<0>(vV, srdV, soV); VRD(4); ADD0(14); SBAR();
            PV(1);  VRD(7); ADD1(15); CV(p1, 3, 14); SBAR();
            if (__builtin_expect(u + 1 == diagu, 0)) { asm volatile("s_nop 7" : "+v"(S)); mask(u + 1); }
            M0<1, 0>(mV); PV(2); LDL<1>(vV, srdV, soV); VRD(6); FM(0); SBAR();
            PV(3);  VRD(9); FM(1); FM(2); FM(3); ps0 = vadd(ps0, ps1); SBAR();
            M0<2, 0>(mV); PV(4); LDL<2>(vV, srdV, soV); VRD(8); FM(4); SBAR();
            PV(5);  VRD(11); FM(5); FM(6); FM(7); l = vadd(l, ps0); SBAR();
            PV(6);  VRD(10); FM(8); FM(9); FM(10); SBAR();
            PV(7);  FM(11); FM(12); FM(13); kb[0] += dk; kb[1] += dk; PIN(kb[0]); PIN(kb[1]); SBAR();
            PV(8);
        }
        LOOPSYNC();
        KRD(1); KRD(0); KRD(3); KRD(2);
        if (TB) {
            M0<0, 0>(wb + r2); PV(9); LDL<0>(vK, srdK, soK + 2 * KBLK); KRD(5); FM(12); FM(13); SBAR();
            PV(10); KRD(4); FM(14); FM(15); EXP(0); vb += dv; vbo += dv; PIN(vb); PIN(vbo); SBAR();
            M0<1, 0>(wb + r2); PV(11); LDL<1>(vK, srdK, soK + 2 * KBLK); EXP(1); EXP(2); EXP(3); SBAR();
        } else {
            PV(9);  KRD(5); FM(14); FM(15); EXP(0); SBAR();
            PV(10); KRD(4); EXP(1); EXP(2); vb += dv; vbo += dv; PIN(vb); PIN(vbo); SBAR();
            PV(11); EXP(3); SBAR();
        }
        ROTATE();
    }
    STAMPC(9);
#define TAIL_HEAD() \
        const bool last = u == NU - 1; \
        if (!last) { \
            if (TB) { dma<2, 0>(wb + r0, vK, srdK, so + 4 * KBLK); dma3(wb + r0 + BLK, vK1, srdK, so + 4 * KBLK); } \
            else { dma3(u + 3 < NU ? wb + r0 : LDS_NOWHERE, vK, srdK, so + 3 * KBLK); dma3(u + 2 < NU ? wb + 3 * BLK + r2 : LDS_NOWHERE, vV, srdV, so + 2 * KBLK); } \
        }
#define TAIL_MID() \
        if (TB) { if (last) { WAIT_VM(0); } else { WAIT_VM(6); } BARRIER(); }
#define TAIL_END() \
        if (!last) { BK_WAIT(); BARRIER(); if (TB) { dma<0, 0>(wb + r2, vK, srdK, so + 6 * KBLK); dma<1, 0>(wb + r2, vK, srdK, so + 6 * KBLK); } }
    if (u < nu) {
        TAIL_HEAD()
#pragma unroll
        for (int e = 4; e < 8; ++e) EXP(e);
        float ps0 = vadd(X[0], X[1]), ps1 = vadd(X[2], X[3]);
        CV(p0, 0, 0); CV(p0, 1, 2); EXP(8); EXP(9); CV(p0, 2, 4); CV(p0, 3, 6);
        TAIL_MID()
        VRD(1); VRD(0); VRD(3); VRD(2); VRD(5); VRD(4);
        EXP(10); EXP(11); EXP(12); EXP(13); SBAR();
        const unsigned mVt = last ? LDS_NOWHERE : wb + r1;
        PV(0);  if (TB) dma<0, 0>(mVt, vV, srdV, so + 2 * KBLK); EXP(14); EXP(15); SBAR();
        PV(1);  VRD(7); ADD0(4); ADD1(5); SBAR();
        PV(2);  VRD(6); if (TB) dma<1, 0>(mVt, vV, srdV, so + 2 * KBLK); CV(p1, 0, 8); CV(p1, 1, 10); SBAR();
        PV(3);  VRD(9); CV(p1, 2, 12); CV(p1, 3, 14); SBAR();
        PV(4);  VRD(8); if (TB) dma<2, 0>(mVt, vV, srdV, so + 2 * KBLK); ADD0(6); ADD1(7); SBAR();
        PV(5);  VRD(11); ADD0(8); ADD1(9); ADD0(10); SBAR();
        PV(6);  VRD(10); if (TB) dma<0, BLK>(mVt, vV1, srdV, so + 2 * KBLK); ADD1(11); ADD0(12); SBAR();
        PV(7);  if (TB) dma<1, BLK>(mVt, vV1, srdV, so + 2 * KBLK); ADD1(13); SBAR();
        PV(8);  if (TB) dma<2, BLK>(mVt, vV1, srdV, so + 2 * KBLK); ADD0(14); ADD1(15); SBAR();
        PV(9); PV(10); PV(11);
        l += ps0 + ps1;
        TAIL_END()
        ROTATE();
    }
    while (u < NU) {
        TAIL_HEAD()
        TAIL_MID()
        if (TB && !last) { dma3(wb + r1, vV, srdV, so + 2 * KBLK); dma3(wb + r1 + BLK, vV1, srdV, so + 2 * KBLK); }
        TAIL_END()
        ROTATE();
    }
#undef TAIL_HEAD
#undef TAIL_MID
#undef TAIL_END
    OPIN("s_nop 15");
    int lane2;
    asm volatile("v_mbcnt_lo_u32_b32 %0, -1, 0\n\tv_mbcnt_hi_u32_b32 %0, -1, %0" : "=v"(lane2));

    const int l32e = lane2 & 31, hie = lane2 >> 5, r8e = lane2 >> 3, c8e = lane2 & 7, r16e = lane2 >> 4;
    STAMP(5);
    const int reg = TB ? (sub ? r2 : r1) + hsel * BLK : (wid < 3 ? wid * BLK : 3 * BLK + r0);
    l = swapsum(l);
    if (__builtin_expect(__any(!(l < LBIG)), 0)) {
        const unsigned mb = ldsb + reg;
        const int dr2 = (lane2 >> 2) & 7, dh2 = lane2 >> 5, dc2 = lane2 & 3, dg2 = (lane2 >> 4) & 1;
        const unsigned vK0 = dr2 * KROW + 64u * dh2 + 16u * (dc2 ^ dg2), vK1_ = dr2 * KROW + 64u * dh2 + 16u * (dc2 ^ (2 + dg2)), vV0 = dr2 * KROW + 64u * dh2 + 16u * dc2;
        kb[0] = lp + reg + (l32e >> 3) * 1024 + (l32e & 7) * 64 + 16 * (hie ^ ((l32e >> 2) & 3));
        kb[1] = lp + reg + (l32e >> 3) * 1024 + (l32e & 7) * 64 + 16 * ((2 + hie) ^ ((l32e >> 2) & 3));
        vb = lp + reg + (4 * hie + ((lane2 & 15) >> 2)) * 64 + 32 * ((lane2 >> 4) & 1) + 8 * (lane2 & 3);
        vbo = vb + (TB ? 512 - sub * 512 : 512); vb += TB ? sub * 512 : 0;
        bf16x8 q2[12];
        {
            const unsigned vQ0 = dr2 * QROW + 64u * dh2 + 16u * (dc2 ^ dg2), vQ1 = dr2 * QROW + 64u * dh2 + 16u * (dc2 ^ (2 + dg2));
            const unsigned qs = (unsigned)(q0 + (TB ? 0 : 32 * sub)) * QROW + hsel * 384;
#pragma unroll
            for (int g = 0; g < 4; ++g) dma3(mb + g * 1024, (g & 1) ? vQ1 : vQ0, srdQ, qs + g * 8 * QROW);
            WAIT_VM(0);
#pragma unroll
            for (int ks = 0; ks < 12; ++ks) q2[ks] = ldk(KADDR(ks));
        }
        auto scores = [&](int t) {
            const int b = TB ? 2 * t + sub : t;
            asm volatile("s_waitcnt lgkmcnt(0)" ::: "memory");
#pragma unroll
            for (int g = 0; g < 4; ++g) dma3(mb + g * 1024, (g & 1) ? vK1_ : vK0, srdK, b * KBLK + g * 8 * KROW);
            WAIT_VM(0);
            S = MFMA(ldk(KADDR(0)), q2[0], f32x16{});
#pragma unroll
            for (int ks = 1; ks < 12; ++ks) S = MFMA(ldk(KADDR(ks)), q2[ks], S);
            if (t == diagu) {
                const int k0 = b * 32 + 4 * hie - (q0 + (TB ? 0 : 32 * sub) + l32e);
#pragma unroll
                for (int r = 0; r < 16; ++r) if (k0 + (r & 3) + 8 * (r >> 2) > 0) S[r] = -__builtin_inff();
            }
        };
        float mx = -__builtin_inff();
#pragma unroll 1
        for (int t = 0; t < nu; ++t) { scores(t); asm volatile("s_nop 15" : "+v"(S)); mx = max2f(mx, rowmax()); }
        m = mx * C2;
#pragma unroll
        for (int i = 0; i < 6; ++i) o[i] = f32x16{};
        l = 0.f;
#pragma unroll 1
        for (int t = 0; t < nu; ++t) {
            scores(t);
            const int b = TB ? 2 * t + sub : t;
            asm volatile("s_waitcnt lgkmcnt(0)" ::: "memory");
#pragma unroll
            for (int g = 0; g < 4; ++g) dma3(mb + g * 1024, vV0, srdV, b * KBLK + g * 8 * KROW);
            float ps = 0.f;
#pragma unroll
            for (int e = 0; e < 16; ++e) { X[e] = __builtin_amdgcn_exp2f(__builtin_fmaf(S[e], C2, -m)); ps += X[e]; }
            l += ps;
            p0 = (u32x4){cvtpk(X[0], X[1]), cvtpk(X[2], X[3]), cvtpk(X[4], X[5]), cvtpk(X[6], X[7])};
            p1 = (u32x4){cvtpk(X[8], X[9]), cvtpk(X[10], X[11]), cvtpk(X[12], X[13]), cvtpk(X[14], X[15])};
            WAIT_VM(0);
            #pragma unroll
            for (int k = 0; k < 12; ++k) { VRD(k); o[k % 6] = MFMA(VFR(k), __builtin_bit_cast(bf16x8, k < 6 ? p0 : p1), o[k % 6]); }
        }
        l = swapsum(l);
        asm volatile("s_waitcnt lgkmcnt(0)" ::: "memory");
    }

    const __amdgpu_buffer_rsrc_t srdO = __builtin_amdgcn_make_buffer_rsrc((void*)((char*)O + (size_t)s0 * QROW + kvh * 768), (short)0, (int)((unsigned)L * QROW - kvh * 768), 0x00020000);
    const lds_ptr lw = (lds_ptr)lds;
    const unsigned vO = r8e * QROW + c8e * 16;
#define STG(base, db, g, lo, hi_) *(__attribute__((address_space(3))) u32x2*)((base) + ((db) >> 1) * 4096 + 16 * (((4 * (db) + (g)) & 7) ^ (l32e & 7))) = (u32x2){lo, hi_}
    if constexpr (!TB) {
        if (nu > 0) {
            float il = __builtin_amdgcn_rcpf(l);
            asm volatile("s_nop 1" : "+v"(il));
            const lds_ptr wp = lw + reg + l32e * 128 + 8 * hie;
            const lds_ptr rp = lw + reg + r8e * 128 + 16 * (c8e ^ r8e);
            const unsigned ob = (unsigned)(q0 + 32 * sub) * QROW + hsel * 384;
            u32x4 w[4];
            auto scale = [&](int db) {
                float y[16];
#pragma unroll
                for (int r = 0; r < 16; ++r) y[r] = vmul(o[db][r], il);
                unsigned c[8];
#pragma unroll
                for (int r = 0; r < 8; ++r) c[r] = vcvt(y[2 * r], y[2 * r + 1]);
#pragma unroll
                for (int g = 0; g < 4; ++g) STG(wp, db, g, c[2 * g], c[2 * g + 1]);
                SBAR();
            };
            auto rd = [&](int s) {
#pragma unroll
                for (int g = 0; g < 4; ++g) w[g] = *(const __attribute__((address_space(3))) u32x4*)(rp + s * 4096 + g * 1024);
                SBAR();
            };
            auto st_ = [&](int s) {
#pragma unroll
                for (int g = 0; g < 4; ++g) __builtin_amdgcn_raw_buffer_store_b128(w[g], srdO, vO + s * 128, ob + g * 8 * QROW, STAUX);
                SBAR();
            };
            scale(0); scale(1); rd(0); scale(2); st_(0); scale(3); rd(1); scale(4); st_(1); scale(5); rd(2); st_(2);
        }
    } else {
        const lds_ptr xp = lw + reg + lane2 * 16;
        const lds_ptr xq = lw + (sub ? r1 : r2) + hsel * BLK + lane2 * 16;
        __attribute__((address_space(3))) f32x2* ml = (__attribute__((address_space(3))) f32x2*)(lw + LDS_MISC);
        ml[wid * 64 + lane2] = (f32x2){m, l};
#pragma unroll
        for (int i = 0; i < 3; ++i)
#pragma unroll
            for (int g = 0; g < 4; ++g) *(__attribute__((address_space(3))) f32x4*)(xp + (4 * i + g) * 1024) = (f32x4){o[2 * i + 1][4 * g], o[2 * i + 1][4 * g + 1], o[2 * i + 1][4 * g + 2], o[2 * i + 1][4 * g + 3]};
        BARRIER();
        const f32x2 pm = ml[(wid ^ 2) * 64 + lane2];
        f32x4 x[12];
#pragma unroll
        for (int i = 0; i < 12; ++i) x[i] = *(const __attribute__((address_space(3))) f32x4*)(xq + i * 1024);
        SBAR();
        const float mm = max2f(m, pm[0]);
        const float fa = __builtin_amdgcn_exp2f(m - mm), fb = __builtin_amdgcn_exp2f(pm[0] - mm);
        const float il = __builtin_amdgcn_rcpf(l * fa + pm[1] * fb);
        float wa = fa * il, wb2 = fb * il;
        asm volatile("s_nop 1" : "+v"(wa), "+v"(wb2));
        const lds_ptr wp = lw + r0 + hsel * BLK + l32e * 128 + 8 * hie + 16 * ((4 * sub) ^ (l32e & 4));
#pragma unroll
        for (int i = 0; i < 3; ++i) {
            float y[16];
#pragma unroll
            for (int r = 0; r < 16; ++r) y[r] = vmul(x[4 * i + (r >> 2)][r & 3], wb2);
#pragma unroll
            for (int r = 0; r < 16; ++r) y[r] = vfma_v(o[2 * i][r], wa, y[r]);
            unsigned c[8];
#pragma unroll
            for (int r = 0; r < 8; ++r) c[r] = vcvt(y[2 * r], y[2 * r + 1]);
#pragma unroll
            for (int g = 0; g < 4; ++g) *(__attribute__((address_space(3))) u32x2*)(wp + i * 4096 + 16 * (g ^ (l32e & 3))) = (u32x2){c[2 * g], c[2 * g + 1]};
            SBAR();
        }
        BARRIER();
        const lds_ptr rp = lw + r0 + hsel * BLK + sub * 2048 + r8e * 128 + 16 * (c8e ^ r8e);
        const unsigned ob = (unsigned)(q0 + 16 * sub) * QROW + hsel * 384;
        u32x4 w[6];
#pragma unroll
        for (int i = 0; i < 6; ++i) w[i] = *(const __attribute__((address_space(3))) u32x4*)(rp + (i % 3) * 4096 + (i / 3) * 1024);
#pragma unroll
        for (int i = 0; i < 6; ++i) __builtin_amdgcn_raw_buffer_store_b128(w[i], srdO, vO + (i % 3) * 128, ob + (i / 3) * 8 * QROW, STAUX);
    }
#undef STG
    STAMP(6);
#undef EXP
#undef FM
#undef KRD
#undef VRD
#undef VFR
#undef QK
#undef PV
#undef ADD0
#undef ADD1
#undef ROTATE
#undef BK_WAIT
#undef CV
#undef KADDR
}

DI int wave_max63(int x) {
    asm volatile("s_nop 1\n\tv_max_i32_dpp %0, %0, %0 row_shr:1 row_mask:0xf bank_mask:0xf\n\ts_nop 1\n\tv_max_i32_dpp %0, %0, %0 row_shr:2 row_mask:0xf bank_mask:0xf\n\t"
                 "s_nop 1\n\tv_max_i32_dpp %0, %0, %0 row_shr:4 row_mask:0xf bank_mask:0xf\n\ts_nop 1\n\tv_max_i32_dpp %0, %0, %0 row_shr:8 row_mask:0xf bank_mask:0xf\n\t"
                 "s_nop 1\n\tv_max_i32_dpp %0, %0, %0 row_bcast:15 row_mask:0xa bank_mask:0xf\n\ts_nop 1\n\tv_max_i32_dpp %0, %0, %0 row_bcast:31 row_mask:0xc bank_mask:0xf\n\ts_nop 0" : "+v"(x));
    return x;
}
constexpr int first_pure_b() { int i = TH; while (WB * ((i + 2) >> 1) < WA * TH) ++i; return i; }
constexpr int I0 = first_pure_b();
constexpr int NMIX = (I0 - TH) + TH / 2;
static_assert(NMIX <= 12 && I0 < 60, "level table (16 levels; thresholds are inline constants)");
constexpr int level(int k) {
    if (k < 4) return (I0 + 3 - k) | 0x10000;
    k -= 4;
    if (k >= NMIX) return (int)0xffff0040;
    int i = I0 - 1, jj = TH / 2 - 1, out = 0;
    for (int lev = 0; lev <= k; ++lev) {
        const bool useB = i >= TH && (jj < 0 || WB * ((i + 2) >> 1) >= WA * (2 * jj + 2));
        if (useB) { out = i | 0x10000; --i; } else { out = 2 * jj; --jj; }
    }
    return out;
}

__global__ __launch_bounds__(256, 2) void attn_v14(const unsigned short* __restrict__ Q, const unsigned short* __restrict__ K,
                                                  const unsigned short* __restrict__ V, const int* __restrict__ cu,
                                                  unsigned short* __restrict__ O, int nseq, int T, unsigned long long* dbg) {
    extern __shared__ __attribute__((aligned(16))) char lds[];
    unsigned long long st[16];
    STAMP(0);
    const int lane = threadIdx.x & 63;
    const int kvh = blockIdx.x & 7, j = blockIdx.x >> 3;
    if (PFCODE > 0) {
        unsigned long long pc;
        asm volatile("s_getpc_b64 %0" : "=s"(pc));
        const __amdgpu_buffer_rsrc_t srdC = __builtin_amdgcn_make_buffer_rsrc((void*)(pc & ~127ull), (short)0, PFCODE, 0x00020000);
        const unsigned pm = (unsigned)(uintptr_t)lds + LDS_MISC + 1024;
        asm volatile("s_mov_b32 m0, %0\n\ts_nop 0\n\tbuffer_load_dword %1, %2, 0 offen lds\n\tbuffer_load_dword %1, %2, 0 offen offset:128 lds" :: "s"(pm), "v"(threadIdx.x * 256u), "s"(srdC) : "memory");
    }
    const int wv = rfl(threadIdx.x >> 6);
    unsigned vp, vq;
    LaneAddr la;
    {
        const int jj = j & 31, pj = (j >> 5) * (32 - PFSKIP) + jj - PFSKIP;
        constexpr int LPW = (8192 + 4 * (64 - 2 * PFSKIP) - 1) / (4 * (64 - 2 * PFSKIP));
        const unsigned li = (pj * 4 + wv) * LPW + lane, tk = li / 3u;
        vp = (tk * KROW + (li - 3u * tk) * 128u) | ((PFB && lane < LPW && j < 64 && jj >= PFSKIP) ? 0u : OOB);
        const unsigned lq = (j * 4 + wv) * 64 + lane, tq = lq / 6u;
        vq = (tq * QROW + (lq - 6u * tq) * 128u) | ((PFQ && j < 64) ? 0u : OOB);
        const int dr = (lane >> 2) & 7, dh = lane >> 5, dc = lane & 3, dg = (lane >> 4) & 1;
        la.vK = (8 * wv + dr) * KROW + 64u * dh + 16u * (dc ^ ((2 * wv + dg) & 3));
        la.vV = (8 * wv + dr) * KROW + 64u * dh + 16u * dc;
        la.vQ0 = dr * QROW + 64u * dh + 16u * (dc ^ dg);
        la.vQ1 = dr * QROW + 64u * dh + 16u * (dc ^ (2 + dg));
        if (PFSPLIT == 2) {
            const unsigned l2 = (((j & 31) + (lane & 32)) * 4 + wv) * 32 + (lane & 31), t2 = l2 / 3u;
            la.vp = (t2 * KROW + (l2 - 3u * t2) * 128u) | ((PFB && j >= 32 && j < 64) ? 0u : OOB);
        } else la.vp = vp;
        asm volatile("" : "+v"(vp), "+v"(vq), "+v"(la.vp), "+v"(la.vK), "+v"(la.vV), "+v"(la.vQ0), "+v"(la.vQ1));
    }
    asm volatile("" :: "s"(Q), "s"(K), "s"(V), "s"(O), "s"(cu), "s"(nseq), "s"(T));
    int r = j < 32 ? j : (j < 64 ? 95 - j : j);
    int a, b;
    if (nseq <= 15) {
        const unsigned pm = (unsigned)(uintptr_t)lds + LDS_MISC + wv * 256;
        const __amdgpu_buffer_rsrc_t srdKa = __builtin_amdgcn_make_buffer_rsrc((void*)((const char*)K + kvh * 384), (short)0, (int)((unsigned)T * KROW - kvh * 384), 0x00020000);
        const __amdgpu_buffer_rsrc_t srdVa = __builtin_amdgcn_make_buffer_rsrc((void*)((const char*)V + kvh * 384), (short)0, (int)((unsigned)T * KROW - kvh * 384), 0x00020000);
        i32x16 c;
        if (PFSPLIT) asm volatile("s_load_dwordx16 %0, %1, 0x0\n\ts_mov_b32 m0, %2\n\ts_nop 0\n\tbuffer_load_dword %3, %4, 0 offen lds\n\ts_waitcnt lgkmcnt(0)"
                     : "=&s"(c) : "s"(cu), "s"(pm), "v"(vp), "s"(srdKa) : "memory");
        else asm volatile("s_load_dwordx16 %0, %1, 0x0\n\ts_mov_b32 m0, %2\n\ts_nop 0\n\tbuffer_load_dword %3, %4, 0 offen lds\n\tbuffer_load_dword %3, %5, 0 offen lds\n\ts_waitcnt lgkmcnt(0)"
                     : "=&s"(c) : "s"(cu), "s"(pm), "v"(vp), "s"(srdKa), "s"(srdVa) : "memory");
        asm volatile("v_mov_b32 %0, %1\n\tv_writelane_b32 %0, %2, 1\n\tv_writelane_b32 %0, %3, 2\n\tv_writelane_b32 %0, %4, 3\n\tv_writelane_b32 %0, %5, 4\n\tv_writelane_b32 %0, %6, 5\n\t"
                     "v_writelane_b32 %0, %7, 6\n\tv_writelane_b32 %0, %8, 7\n\tv_writelane_b32 %0, %9, 8\n\tv_writelane_b32 %0, %10, 9\n\tv_writelane_b32 %0, %11, 10\n\t"
                     "v_writelane_b32 %0, %12, 11\n\tv_writelane_b32 %0, %13, 12\n\tv_writelane_b32 %0, %14, 13\n\tv_writelane_b32 %0, %15, 14\n\tv_writelane_b32 %0, %16, 15\n\ts_nop 1"
                     : "=&v"(a) : "s"(c[0]), "s"(c[1]), "s"(c[2]), "s"(c[3]), "s"(c[4]), "s"(c[5]), "s"(c[6]), "s"(c[7]), "s"(c[8]), "s"(c[9]), "s"(c[10]), "s"(c[11]), "s"(c[12]), "s"(c[13]), "s"(c[14]), "s"(c[15]));
        b = __builtin_amdgcn_update_dpp(a, a, 0x101, 0xf, 0xf, false);
    } else {
        u32x2 ab;
        asm volatile("global_load_dwordx2 %0, %1, %2\n\ts_waitcnt vmcnt(0)" : "=&v"(ab) : "v"(min(lane, nseq - 1) * 4), "s"(cu) : "memory");
        a = ab[0]; b = ab[1];
    }
    const int n = lane < nseq ? (b - a + 31) >> 5 : 0;
    int code = -1;
    unsigned long long msk = 0;
    if (__builtin_expect(__ballot(n > I0 + 4) != 0, 0)) {
        const int nmax = __builtin_amdgcn_readlane(wave_max63(n), 63);
        for (int i = nmax - 1; i >= I0 + 4; --i) {
            msk = __ballot(n > i);
            const int c = __popcll(msk);
            if (r < c) { code = i | 0x10000; goto found; }
            r -= c;
        }
    }
    {
        int cnt;
        asm volatile("v_cmp_lt_i32_e64 %0, (%c5&65535), %4\n\ts_bcnt1_i32_b64 %2, %0\n\ts_mov_b32 %3, %c5\n\ts_cmp_lt_u32 %1, %2\n\ts_cbranch_scc1 1f\n\ts_sub_u32 %1, %1, %2\n\t"
                     "v_cmp_lt_i32_e64 %0, (%c6&65535), %4\n\ts_bcnt1_i32_b64 %2, %0\n\ts_mov_b32 %3, %c6\n\ts_cmp_lt_u32 %1, %2\n\ts_cbranch_scc1 1f\n\ts_sub_u32 %1, %1, %2\n\t"
                     "v_cmp_lt_i32_e64 %0, (%c7&65535), %4\n\ts_bcnt1_i32_b64 %2, %0\n\ts_mov_b32 %3, %c7\n\ts_cmp_lt_u32 %1, %2\n\ts_cbranch_scc1 1f\n\ts_sub_u32 %1, %1, %2\n\t"
                     "v_cmp_lt_i32_e64 %0, (%c8&65535), %4\n\ts_bcnt1_i32_b64 %2, %0\n\ts_mov_b32 %3, %c8\n\ts_cmp_lt_u32 %1, %2\n\ts_cbranch_scc1 1f\n\ts_sub_u32 %1, %1, %2\n\t"
                     "v_cmp_lt_i32_e64 %0, (%c9&65535), %4\n\ts_bcnt1_i32_b64 %2, %0\n\ts_mov_b32 %3, %c9\n\ts_cmp_lt_u32 %1, %2\n\ts_cbranch_scc1 1f\n\ts_sub_u32 %1, %1, %2\n\t"
                     "v_cmp_lt_i32_e64 %0, (%c10&65535), %4\n\ts_bcnt1_i32_b64 %2, %0\n\ts_mov_b32 %3, %c10\n\ts_cmp_lt_u32 %1, %2\n\ts_cbranch_scc1 1f\n\ts_sub_u32 %1, %1, %2\n\t"
                     "v_cmp_lt_i32_e64 %0, (%c11&65535), %4\n\ts_bcnt1_i32_b64 %2, %0\n\ts_mov_b32 %3, %c11\n\ts_cmp_lt_u32 %1, %2\n\ts_cbranch_scc1 1f\n\ts_sub_u32 %1, %1, %2\n\t"
                     "v_cmp_lt_i32_e64 %0, (%c12&65535), %4\n\ts_bcnt1_i32_b64 %2, %0\n\ts_mov_b32 %3, %c12\n\ts_cmp_lt_u32 %1, %2\n\ts_cbranch_scc1 1f\n\ts_sub_u32 %1, %1, %2\n\t"
                     "v_cmp_lt_i32_e64 %0, (%c13&65535), %4\n\ts_bcnt1_i32_b64 %2, %0\n\ts_mov_b32 %3, %c13\n\ts_cmp_lt_u32 %1, %2\n\ts_cbranch_scc1 1f\n\ts_sub_u32 %1, %1, %2\n\t"
                     "v_cmp_lt_i32_e64 %0, (%c14&65535), %4\n\ts_bcnt1_i32_b64 %2, %0\n\ts_mov_b32 %3, %c14\n\ts_cmp_lt_u32 %1, %2\n\ts_cbranch_scc1 1f\n\ts_sub_u32 %1, %1, %2\n\t"
                     "v_cmp_lt_i32_e64 %0, (%c15&65535), %4\n\ts_bcnt1_i32_b64 %2, %0\n\ts_mov_b32 %3, %c15\n\ts_cmp_lt_u32 %1, %2\n\ts_cbranch_scc1 1f\n\ts_sub_u32 %1, %1, %2\n\t"
                     "v_cmp_lt_i32_e64 %0, (%c16&65535), %4\n\ts_bcnt1_i32_b64 %2, %0\n\ts_mov_b32 %3, %c16\n\ts_cmp_lt_u32 %1, %2\n\ts_cbranch_scc1 1f\n\ts_sub_u32 %1, %1, %2\n\t"
                     "v_cmp_lt_i32_e64 %0, (%c17&65535), %4\n\ts_bcnt1_i32_b64 %2, %0\n\ts_mov_b32 %3, %c17\n\ts_cmp_lt_u32 %1, %2\n\ts_cbranch_scc1 1f\n\ts_sub_u32 %1, %1, %2\n\t"
                     "v_cmp_lt_i32_e64 %0, (%c18&65535), %4\n\ts_bcnt1_i32_b64 %2, %0\n\ts_mov_b32 %3, %c18\n\ts_cmp_lt_u32 %1, %2\n\ts_cbranch_scc1 1f\n\ts_sub_u32 %1, %1, %2\n\t"
                     "v_cmp_lt_i32_e64 %0, (%c19&65535), %4\n\ts_bcnt1_i32_b64 %2, %0\n\ts_mov_b32 %3, %c19\n\ts_cmp_lt_u32 %1, %2\n\ts_cbranch_scc1 1f\n\ts_sub_u32 %1, %1, %2\n\t"
                     "v_cmp_lt_i32_e64 %0, (%c20&65535), %4\n\ts_bcnt1_i32_b64 %2, %0\n\ts_mov_b32 %3, %c20\n\ts_cmp_lt_u32 %1, %2\n\ts_cbranch_scc1 1f\n\ts_sub_u32 %1, %1, %2\n\t"
                     "s_mov_b32 %3, -1\n1:"
                     : "=&s"(msk), "+s"(r), "=&s"(cnt), "=&s"(code) : "v"(n), "i"(level(0)), "i"(level(1)), "i"(level(2)), "i"(level(3)), "i"(level(4)), "i"(level(5)), "i"(level(6)), "i"(level(7)), "i"(level(8)), "i"(level(9)), "i"(level(10)), "i"(level(11)), "i"(level(12)), "i"(level(13)), "i"(level(14)), "i"(level(15)) : "scc");
    }
found:
    if (__builtin_expect(code < 0, 0)) { if (PFSPLIT) prefetch_v(V, kvh, T, la.vp, (unsigned)(uintptr_t)lds + LDS_MISC + wv * 256); asm volatile("s_waitcnt vmcnt(0)" ::: "memory"); return; }
    const int tb = code >> 16, idx = (code & 0xffff) >> (1 - tb);
    const unsigned below = __builtin_amdgcn_mbcnt_hi((unsigned)(msk >> 32), __builtin_amdgcn_mbcnt_lo((unsigned)msk, 0u));
    const unsigned long long hit = __ballot(below == (unsigned)r) & msk;
    const int sl = __builtin_ctzll(hit);
    const int s0 = min(max(__builtin_amdgcn_readlane(a, sl), 0), T), L = min(__builtin_amdgcn_readlane(b, sl), T) - s0;
    if (__builtin_expect(L <= 0, 0)) { asm volatile("s_waitcnt vmcnt(0)" ::: "memory"); return; }
    if (DELAY2 > 0 && j >= 32) __builtin_amdgcn_s_sleep(DELAY2);
    STAMP(1);
    if (tb) attn_item<true>(st, Q, K, V, O, kvh, s0, L, idx, lds, wv, la, T);
    else attn_item<false>(st, Q, K, V, O, kvh, s0, L, idx, lds, wv, la, T);
}

extern "C" void kernel_launch(void* const* d_in, const int* in_sizes, int n_in, void* d_out, int out_size, void* d_ws, size_t ws_size, hipStream_t stream) {
    const int T = in_sizes[0] / (HQ * D), nseq = in_sizes[3] - 1;
    int slots = (T + 31) / 32 + nseq;
    if (slots < 64) slots = 64;
    (void)hipFuncSetAttribute((const void*)attn_v14, hipFuncAttributeMaxDynamicSharedMemorySize, LDS_BYTES);
    attn_v14<<<slots * 8, 256, LDS_BYTES, stream>>>((const unsigned short*)d_in[0], (const unsigned short*)d_in[1], (const unsigned short*)d_in[2],
                                                 (const int*)d_in[3], (unsigned short*)d_out, nseq, T, (unsigned long long*)d_ws);
}
```

```cpp
#include <hip/hip_runtime.h>
#include <hip/hip_bf16.h>
#include <stdint.h>

typedef short bf16x8 __attribute__((ext_vector_type(8)));
typedef short s16x4 __attribute__((ext_vector_type(4)));
typedef float f32x16 __attribute__((ext_vector_type(16)));
typedef float f32x4 __attribute__((ext_vector_type(4)));
typedef float f32x2 __attribute__((ext_vector_type(2)));
typedef unsigned u32x4 __attribute__((ext_vector_type(4)));
typedef unsigned u32x2 __attribute__((ext_vector_type(2)));
typedef int i32x16 __attribute__((ext_vector_type(16)));
typedef __attribute__((address_space(3))) const char* lds_cptr;
typedef __attribute__((address_space(3))) char* lds_ptr;
typedef short v4i16_t __attribute__((ext_vector_type(4)));

constexpr int HQ = 16, HKV = 8, D = 192;
constexpr unsigned QROW = HQ * D * 2, KROW = HKV * D * 2;
constexpr unsigned KBLK = 32 * KROW;
constexpr int BLK = 12288;
constexpr int LDS_MISC = 6 * BLK;
constexpr int LDS_BYTES = LDS_MISC + 2048;
constexpr float C2 = 0.07216878364870322f * 1.4426950408889634f;
constexpr float LBIG = 1073741824.f;
#define TH 8
#define PFKV 0
#define PFFIRST 0
#define PFSKIP 0
#define QNT 0
#define STAUX 17
#define PFB 1
#define DELAY2 0
#define PFQ 0
#define WA 10
#define WB 11
constexpr unsigned LDS_NOWHERE = 0x30000u;
constexpr unsigned OOB = 0x80000000u;

#define DI __device__ __forceinline__
#define MFMA(a, b, c) __builtin_amdgcn_mfma_f32_32x32x16_bf16(a, b, c, 0, 0, 0)
#define SBAR() __builtin_amdgcn_sched_barrier(0)
#define PIN(x) asm volatile("" : "+v"(x))
typedef __bf16 bf16x2_t __attribute__((ext_vector_type(2)));
DI unsigned cvtpk(float lo, float hi) { const f32x2 v = {lo, hi}; return __builtin_bit_cast(unsigned, __builtin_convertvector(v, bf16x2_t)); }
DI float max3f(float a, float b, float c) { float r; asm("v_max3_f32 %0, %1, %2, %3" : "=v"(r) : "v"(a), "v"(b), "v"(c)); return r; }
DI float max2f(float a, float b) { float r; asm("v_max_f32_e32 %0, %1, %2" : "=v"(r) : "v"(a), "v"(b)); return r; }
DI float fmul_s(float a, float b) { float r; asm("v_mul_f32_e32 %0, %1, %2" : "=v"(r) : "v"(a), "v"(b)); return r; }
DI float fma_v(float a, float b, float c) { float r; asm("v_fma_f32 %0, %1, %2, %3" : "=v"(r) : "v"(a), "v"(b), "v"(c)); return r; }
DI void vexp(float& x) { asm volatile("v_exp_f32_e32 %0, %0" : "+v"(x)); }
DI float vadd(float a, float b) { float r; asm volatile("v_add_f32_e32 %0, %1, %2" : "=v"(r) : "v"(a), "v"(b)); return r; }
DI float vfma(float a, float b, float c) { float r; asm volatile("v_fma_f32 %0, %1, %2, %3" : "=v"(r) : "v"(a), "s"(b), "v"(c)); return r; }
DI float vmul(float a, float b) { float r; asm volatile("v_mul_f32_e32 %0, %1, %2" : "=v"(r) : "v"(a), "v"(b)); return r; }
DI float vfma_v(float a, float b, float c) { float r; asm volatile("v_fma_f32 %0, %1, %2, %3" : "=v"(r) : "v"(a), "v"(b), "v"(c)); return r; }
DI unsigned vcvt(float lo, float hi) { unsigned r; asm volatile("v_cvt_pk_bf16_f32 %0, %1, %2" : "=v"(r) : "v"(lo), "v"(hi)); return r; }
DI s16x4 vtr(lds_cptr p) { return __builtin_bit_cast(s16x4, __builtin_amdgcn_ds_read_tr16_b64_v4i16((__attribute__((address_space(3))) v4i16_t*)p)); }
DI bf16x8 ldk(lds_cptr p) { return *(const __attribute__((address_space(3))) bf16x8*)p; }
DI int rfl(int x) { return __builtin_amdgcn_readfirstlane(x); }
DI float swapmax(float x) { auto r = __builtin_amdgcn_permlane32_swap(__float_as_uint(x), __float_as_uint(x), false, false); return max2f(__uint_as_float(r[0]), __uint_as_float(r[1])); }
DI float swapsum(float x) { auto r = __builtin_amdgcn_permlane32_swap(__float_as_uint(x), __float_as_uint(x), false, false); return __uint_as_float(r[0]) + __uint_as_float(r[1]); }

template <int S, int OFF> DI void dma(unsigned mb, unsigned voff, __amdgpu_buffer_rsrc_t srd, unsigned soff) {
    asm volatile("s_add_u32 m0, %0, %5\n\ts_nop 0\n\tbuffer_load_dwordx4 %1, %2, %3 offen offset:%4 lds"
                 :: "s"(mb), "v"(voff), "s"(srd), "s"(soff), "i"(128 * S), "i"(OFF + S * (4096 - 128)) : "memory", "scc");
}
template <int S, int OFF> DI void setm0(unsigned mb) { asm volatile("s_add_u32 m0, %0, %1" :: "s"(mb), "i"(OFF + S * (4096 - 128)) : "scc"); __builtin_amdgcn_sched_barrier(0); }
template <int S> DI void ldm0(unsigned voff, __amdgpu_buffer_rsrc_t srd, unsigned soff) {
    asm volatile("buffer_load_dwordx4 %0, %1, %2 offen offset:%3 lds" :: "v"(voff), "s"(srd), "s"(soff), "i"(128 * S) : "memory");
}
template <int S> DI void dmaq(unsigned mb, unsigned voff, __amdgpu_buffer_rsrc_t srd, unsigned soff) {
    if (QNT == 1) asm volatile("s_add_u32 m0, %0, %5\n\ts_nop 0\n\tbuffer_load_dwordx4 %1, %2, %3 offen offset:%4 nt lds" :: "s"(mb), "v"(voff), "s"(srd), "s"(soff), "i"(128 * S), "i"(S * (4096 - 128)) : "memory", "scc");
    else if (QNT == 2) asm volatile("s_add_u32 m0, %0, %5\n\ts_nop 0\n\tbuffer_load_dwordx4 %1, %2, %3 offen offset:%4 sc1 lds" :: "s"(mb), "v"(voff), "s"(srd), "s"(soff), "i"(128 * S), "i"(S * (4096 - 128)) : "memory", "scc");
    else asm volatile("s_add_u32 m0, %0, %5\n\ts_nop 0\n\tbuffer_load_dwordx4 %1, %2, %3 offen offset:%4 lds" :: "s"(mb), "v"(voff), "s"(srd), "s"(soff), "i"(128 * S), "i"(S * (4096 - 128)) : "memory", "scc");
}
DI void dma3(unsigned mb, unsigned voff, __amdgpu_buffer_rsrc_t srd, unsigned soff) { dma<0, 0>(mb, voff, srd, soff); dma<1, 0>(mb, voff, srd, soff); dma<2, 0>(mb, voff, srd, soff); }
#define DMAL dma
#define LOOPBAR() BARRIER()
#define LOOPWAIT() WAIT_VM(6)
#define LOOPSYNC() asm volatile("s_waitcnt vmcnt(6) lgkmcnt(0)\n\ts_barrier" ::: "memory")
#define M0 setm0
#define LDL ldm0
#define WAIT_VM(n) asm volatile("s_waitcnt vmcnt(" #n ")" ::: "memory")
#define BARRIER() asm volatile("s_waitcnt lgkmcnt(0)\n\ts_barrier" ::: "memory")
#define OPIN(nop) asm volatile(nop : "+v"(o[0]), "+v"(o[1]), "+v"(o[2]), "+v"(o[3]), "+v"(o[4]), "+v"(o[5]))

#define STAMP(i)
#define STAMPC(i)

struct LaneAddr { unsigned vK, vV, vQ0, vQ1; };

template <bool TB>
DI void attn_item(unsigned long long* st, const unsigned short* __restrict__ Q, const unsigned short* __restrict__ K, const unsigned short* __restrict__ V,
                  unsigned short* __restrict__ O, int kvh, int s0, int L, int idx, char* lds, int wid, const LaneAddr& la) {
    const int tid = threadIdx.x, lane = tid & 63, l32 = lane & 31, hi = lane >> 5;
    const int hsel = wid & 1, sub = wid >> 1;
    const int n = (L + 31) >> 5;
    const int q0 = TB ? 32 * idx : 64 * idx;
    const int NB = TB ? idx + 1 : min(2 * idx + 2, n);
    const int NU = TB ? (NB + 1) >> 1 : NB;
    const int nu = TB ? (NB + 1 - sub) >> 1 : (2 * idx + sub < n ? 2 * idx + sub + 1 : 0);
    const int diagu = TB ? (((idx & 1) == sub) ? nu - 1 : -1) : nu - 1;

    const unsigned krec = (unsigned)min(L, 32 * NB) * KROW - kvh * 384;
    const __amdgpu_buffer_rsrc_t srdK = __builtin_amdgcn_make_buffer_rsrc((void*)((const char*)K + (size_t)s0 * KROW + kvh * 384), (short)0, (int)krec, 0x00020000);
    const __amdgpu_buffer_rsrc_t srdQ = __builtin_amdgcn_make_buffer_rsrc((void*)((const char*)Q + (size_t)s0 * QROW + kvh * 768), (short)0, (int)((unsigned)L * QROW - kvh * 768), 0x00020000);
    const unsigned ldsb = (unsigned)(uintptr_t)lds;
    const unsigned wb = ldsb + wid * 1024;
    const unsigned vK = la.vK, vV = la.vV;

    dma3(wb, vK, srdK, 0);
    if (TB) dma3(wb + BLK, vK, srdK, KBLK);
    {
        const unsigned qreg = TB ? 4 * BLK + hsel * BLK : (wid == 3 ? 2 * BLK : (3 + wid) * BLK);
        const unsigned qs = (unsigned)(q0 + (TB ? 16 * sub : 32 * sub)) * QROW + hsel * 384;
        const unsigned m = ldsb + qreg + (TB ? 2048 * sub : 0);
#pragma unroll
        for (int g = 0; g < (TB ? 2 : 4); ++g) { const unsigned vq_ = (g & 1) ? la.vQ1 : la.vQ0; dmaq<0>(m + g * 1024, vq_, srdQ, qs + g * 8 * QROW); dmaq<1>(m + g * 1024, vq_, srdQ, qs + g * 8 * QROW); dmaq<2>(m + g * 1024, vq_, srdQ, qs + g * 8 * QROW); }
    }
    SBAR();
    const __amdgpu_buffer_rsrc_t srdV = __builtin_amdgcn_make_buffer_rsrc((void*)((const char*)V + (size_t)s0 * KROW + kvh * 384), (short)0, (int)krec, 0x00020000);
    if (PFKV) {
        const int per = TB ? 8 : 16, tk = (lane * 43) >> 7;
        const unsigned vp = tk < per ? (unsigned)(q0 + wid * per + tk) * KROW + (lane - 3 * tk) * 128 : OOB;
        const unsigned pm = ldsb + LDS_MISC + wid * 256;
        asm volatile("s_mov_b32 m0, %0\n\ts_nop 0\n\tbuffer_load_dword %1, %2, 0 offen lds" :: "s"(pm), "v"(vp), "s"(srdK) : "memory");
        asm volatile("s_mov_b32 m0, %0\n\ts_nop 0\n\tbuffer_load_dword %1, %2, 0 offen lds" :: "s"(pm), "v"(vp), "s"(srdV) : "memory");
    }
    const int qrel = q0 + (TB ? 0 : 32 * sub) + l32;
    const lds_cptr lp = (lds_cptr)lds;
    lds_cptr kb[2], vb, vbo;
#pragma unroll
    for (int i = 0; i < 2; ++i) kb[i] = lp + (l32 >> 3) * 1024 + (l32 & 7) * 64 + 16 * ((2 * i + hi) ^ ((l32 >> 2) & 3));
    vb = lp + (4 * hi + ((lane & 15) >> 2)) * 64 + 32 * ((lane >> 4) & 1) + 8 * (lane & 3);
#define KADDR(ks) (kb[(ks) & 1] + ((ks) >> 2) * 4096 + (((ks) >> 1) & 1) * 512)
    f32x16 o[6];
#pragma unroll
    for (int i = 0; i < 6; ++i) o[i] = f32x16{};
    OPIN("");

    STAMP(2);
    bf16x8 qf[12];
    if (PFKV) { WAIT_VM(2); } else { WAIT_VM(0); }
    if (TB) BARRIER();
    {
        const int qo = TB ? 4 * BLK + hsel * BLK : (wid == 3 ? 2 * BLK : (3 + wid) * BLK);
#pragma unroll
        for (int ks = 0; ks < 12; ++ks) {
            qf[ks] = ldk(KADDR(ks) + qo);
            if (TB) {
                if (ks == 1) dma<0, 0>(wb + 2 * BLK, vK, srdK, 2 * KBLK); if (ks == 3) dma<1, 0>(wb + 2 * BLK, vK, srdK, 2 * KBLK); if (ks == 5) dma<2, 0>(wb + 2 * BLK, vK, srdK, 2 * KBLK);
                if (ks == 7) dma<0, 0>(wb + 3 * BLK, vK, srdK, 3 * KBLK); if (ks == 9) dma<1, 0>(wb + 3 * BLK, vK, srdK, 3 * KBLK); if (ks == 11) dma<2, 0>(wb + 3 * BLK, vK, srdK, 3 * KBLK);
            } else {
                if (ks == 3) dma<0, 0>(wb + BLK, vK, srdK, KBLK); if (ks == 7) dma<1, 0>(wb + BLK, vK, srdK, KBLK); if (ks == 11) dma<2, 0>(wb + BLK, vK, srdK, KBLK);
            }
        }
    }
    BARRIER();
    STAMP(3);
    kb[0] += TB ? sub * BLK : 0; kb[1] += TB ? sub * BLK : 0;
    vb += TB ? sub * BLK : 3 * BLK;
    vbo = vb + (TB ? 512 - sub * 512 : 512);
    vb += TB ? sub * 512 : 0;

    f32x16 S, X;
    float m = 0.f, l = 0.f;
    auto mask = [&](int kblock) {
        const int d = qrel - kblock * 32 - 4 * hi;
        const float ninf = -__builtin_inff();
        unsigned long long k[16];
#define MK(r) asm volatile("v_cmp_le_i32_e64 %0, %2, %1" : "=s"(k[r]) : "v"(d), "i"(((r) & 3) + 8 * ((r) >> 2)))
#define MS(r) do { float t_ = S[r]; asm volatile("v_cndmask_b32_e64 %0, %1, %0, %2" : "+v"(t_) : "v"(ninf), "s"(k[r])); S[r] = t_; } while (0)
        MK(0); MK(1); MK(2); MK(3); MK(4); MK(5); MK(6); MK(7); MK(8); MK(9); MK(10); MK(11); MK(12); MK(13); MK(14); MK(15);
        MS(0); MS(1); MS(2); MS(3); MS(4); MS(5); MS(6); MS(7); MS(8); MS(9); MS(10); MS(11); MS(12); MS(13); MS(14); MS(15);
#undef MK
#undef MS
    };
    auto rowmax = [&]() -> float {
        const float a = max3f(S[0], S[1], S[2]), b = max3f(S[3], S[4], S[5]), c = max3f(S[6], S[7], S[8]), d = max3f(S[9], S[10], S[11]), e = max3f(S[12], S[13], S[14]);
        return swapmax(max2f(max3f(a, b, S[15]), max3f(c, d, e)));
    };
    const unsigned vK1 = vK + KBLK, vV1 = vV + KBLK;
    u32x4 p0, p1;
    bf16x8 kf[6];
    s16x4 vlo[6], vhi[6];
#define EXP(e) do { float t_ = X[e]; vexp(t_); X[e] = t_; } while (0)
#define FM(e) X[e] = vfma(S[e], C2, nm)
#define KRD(k) do { kf[(k) % 6] = ldk(KADDR(k)); SBAR(); } while (0)
#define VRD(k) do { const lds_cptr vp_ = ((k) & 1 ? vbo : vb) + (((k) % 6) >> 1) * 4096 + ((k) / 6) * 2048; vlo[(k) % 6] = vtr(vp_); vhi[(k) % 6] = vtr(vp_ + 1024); SBAR(); } while (0)
#define VFR(k) ((bf16x8){vlo[(k) % 6][0], vlo[(k) % 6][1], vlo[(k) % 6][2], vlo[(k) % 6][3], vhi[(k) % 6][0], vhi[(k) % 6][1], vhi[(k) % 6][2], vhi[(k) % 6][3]})
#define QK(k) do { S = MFMA(kf[(k) % 6], qf[k], (k) == 0 ? f32x16{} : S); PIN(S); SBAR(); } while (0)
#define PV(k) do { o[(k) % 6] = MFMA(VFR(k), __builtin_bit_cast(bf16x8, (k) < 6 ? p0 : p1), o[(k) % 6]); PIN(o[(k) % 6]); SBAR(); } while (0)
#define ADD0(e) ps0 = vadd(ps0, X[e])
#define ADD1(e) ps1 = vadd(ps1, X[e])
#define CV(P, i, e) P[i] = vcvt(X[e], X[(e) + 1])

    int r0 = 0, r1 = TB ? 2 * BLK : BLK, r2 = TB ? 4 * BLK : 2 * BLK;
    const unsigned mV1 = (TB || 1 < NB) ? wb + (TB ? 5 : 4) * BLK : LDS_NOWHERE, mK2 = 2 < NB ? wb + 2 * BLK : LDS_NOWHERE;
    if (nu > 0) {
        KRD(1); KRD(0); KRD(3); KRD(2); KRD(5); KRD(4);
        if (TB) {
            QK(0); dma<0, 0>(wb + 4 * BLK, vV, srdV, 0); SBAR();
            QK(1); KRD(7); SBAR();
            QK(2); KRD(6); dma<1, 0>(wb + 4 * BLK, vV, srdV, 0); SBAR();
            QK(3); KRD(9); SBAR();
            QK(4); KRD(8); dma<2, 0>(wb + 4 * BLK, vV, srdV, 0); SBAR();
            QK(5); KRD(11); SBAR();
            QK(6); KRD(10); dma<0, 0>(mV1, vV, srdV, KBLK); SBAR();
            QK(7);
            QK(8); dma<1, 0>(mV1, vV, srdV, KBLK); SBAR();
            QK(9);
            QK(10); dma<2, 0>(mV1, vV, srdV, KBLK); SBAR();
            QK(11);
        } else {
            QK(0); dma<0, 0>(wb + 3 * BLK, vV, srdV, 0); SBAR();
            QK(1); KRD(7); dma<1, 0>(wb + 3 * BLK, vV, srdV, 0); SBAR();
            QK(2); KRD(6); dma<2, 0>(wb + 3 * BLK, vV, srdV, 0); SBAR();
            QK(3); KRD(9); SBAR();
            QK(4); KRD(8); dma<0, 0>(mV1, vV, srdV, KBLK); SBAR();
            QK(5); KRD(11); dma<1, 0>(mV1, vV, srdV, KBLK); SBAR();
            QK(6); KRD(10); dma<2, 0>(mV1, vV, srdV, KBLK); SBAR();
            QK(7);
            QK(8); dma<0, 0>(mK2, vK, srdK, 2 * KBLK); SBAR();
            QK(9); dma<1, 0>(mK2, vK, srdK, 2 * KBLK); SBAR();
            QK(10); dma<2, 0>(mK2, vK, srdK, 2 * KBLK); SBAR();
            QK(11);
        }
    } else {
        dma3(wb + 3 * BLK, vV, srdV, 0); dma3(mV1, vV, srdV, KBLK); dma3(mK2, vK, srdK, 2 * KBLK);
    }
    WAIT_VM(6); BARRIER();
    kb[0] += r1; kb[1] += r1; if (TB) { vb += r2; vbo += r2; }
    if (nu > 1) { KRD(1); KRD(0); KRD(3); KRD(2); KRD(5); KRD(4); }
    if (TB) { dma<0, 0>(wb + r0, vK, srdK, 4 * KBLK); dma<1, 0>(wb + r0, vK, srdK, 4 * KBLK); }
    if (nu > 0) {
        asm volatile("s_nop 11" : "+v"(S));
        if (diagu == 0) mask(TB ? sub : 0);
        m = rowmax() * C2;
        const float nm = -m;
        FM(0); FM(1); FM(2); FM(3); FM(4); FM(5); FM(6); FM(7); FM(8); FM(9); FM(10); FM(11); FM(12); FM(13); FM(14); FM(15);
        EXP(0); EXP(1); EXP(2); EXP(3);
    } else X = f32x16{};
    const float nm = -m;

#define ROTATE() do { const int t_ = TB ? r2 : r0; if (TB) { r2 = r1; r1 = r0; r0 = t_; } else { r0 = r1; r1 = r2; r2 = t_; } so += (TB ? 2 : 1) * KBLK; ++u; } while (0)
#define BK_WAIT() LOOPWAIT()
    STAMP(4); STAMPC(8);
    unsigned barw = 0, barn = 0; (void)barw; (void)barn;
    int u = 0;
    unsigned so = 0;
    while (u + 1 < nu) {
        float ps0, ps1;
        const unsigned soK = so + (TB ? 4 : 3) * KBLK, soV = so + 2 * KBLK;
        const unsigned mK = (TB || u + 3 < NU) ? wb + r0 : LDS_NOWHERE, mV = TB ? wb + r1 : (u + 2 < NU ? wb + 3 * BLK + r2 : LDS_NOWHERE);
        const int dk = TB ? r0 - r1 : r2 - r1, dv = TB ? r1 - r2 : r1 - r0;
        if (TB) {
            QK(0);  EXP(4); EXP(5); ps0 = vadd(X[0], X[1]); SBAR();
            M0<2, 0>(mK); QK(1); LDL<2>(vK, srdK, soK); KRD(7); EXP(6); SBAR();
            QK(2);  KRD(6); EXP(7); EXP(8); ps1 = vadd(X[2], X[3]); SBAR();
            M0<0, BLK>(mK); QK(3); LDL<0>(vK1, srdK, soK); KRD(9); EXP(9); SBAR();
            QK(4);  KRD(8); EXP(10); CV(p0, 0, 0); ADD0(4); ADD1(5); SBAR();
            M0<1, BLK>(mK); QK(5); LDL<1>(vK1, srdK, soK); KRD(11); EXP(11); SBAR();
            QK(6);  KRD(10); EXP(12); CV(p0, 1, 2); CV(p0, 2, 4); SBAR();
            M0<2, BLK>(mK); QK(7); LDL<2>(vK1, srdK, soK); EXP(13); ADD0(6); SBAR();
            QK(8);
            LOOPSYNC();
            VRD(1); VRD(0); VRD(3); VRD(2);
            M0<0, 0>(mV); QK(9); LDL<0>(vV, srdV, soV); VRD(5); EXP(14); SBAR();
            QK(10); VRD(4); EXP(15); CV(p0, 3, 6); ADD1(7); ADD0(8); SBAR();
            M0<1, 0>(mV); QK(11); LDL<1>(vV, srdV, soV); ADD1(9); CV(p1, 0, 8); ADD0(10); SBAR();
            PV(0);  ADD1(11); CV(p1, 1, 10); ADD0(12); ADD1(13); CV(p1, 2, 12); SBAR();
            M0<2, 0>(mV); PV(1); LDL<2>(vV, srdV, soV); VRD(7); ADD0(14); SBAR();
            if (__builtin_expect(u + 1 == diagu, 0)) { asm volatile("s_nop 7" : "+v"(S)); mask(2 * u + 2 + sub); }
            PV(2);  VRD(6); ADD1(15); CV(p1, 3, 14); FM(0); FM(1); SBAR();
            M0<0, BLK>(mV); PV(3); LDL<0>(vV1, srdV, soV); VRD(9); FM(2); SBAR();
            PV(4);  VRD(8); FM(3); FM(4); FM(5); ps0 = vadd(ps0, ps1); SBAR();
            M0<1, BLK>(mV); PV(5); LDL<1>(vV1, srdV, soV); VRD(11); FM(6); SBAR();
            PV(6);  VRD(10); FM(7); FM(8); FM(9); l = vadd(l, ps0); SBAR();
            M0<2, BLK>(mV); PV(7); LDL<2>(vV1, srdV, soV); FM(10); FM(11); kb[0] += dk; kb[1] += dk; PIN(kb[0]); PIN(kb[1]); SBAR();
            PV(8);
        } else {
            QK(0);  EXP(4); EXP(5); ps0 = vadd(X[0], X[1]); SBAR();
            QK(1);  KRD(7); EXP(6); EXP(7); ps1 = vadd(X[2], X[3]); SBAR();
            QK(2);  KRD(6); EXP(8); CV(p0, 0, 0); ADD0(4); ADD1(5); SBAR();
            M0<0, 0>(mK); QK(3); LDL<0>(vK, srdK, soK); KRD(9); EXP(9); SBAR();
            QK(4);  KRD(8); EXP(10); CV(p0, 1, 2); CV(p0, 2, 4); SBAR();
            M0<1, 0>(mK); QK(5); LDL<1>(vK, srdK, soK); KRD(11); EXP(11); SBAR();
            QK(6);  KRD(10); EXP(12); ADD0(6); CV(p0, 3, 6); ADD1(7); SBAR();
            M0<2, 0>(mK); QK(7); LDL<2>(vK, srdK, soK); VRD(1); EXP(13); SBAR();
            QK(8);  VRD(0); EXP(14); ADD0(8); ADD1(9); SBAR();
            QK(9);  VRD(3); EXP(15); CV(p1, 0, 8); SBAR();
            QK(10); VRD(2); ADD0(10); ADD1(11); CV(p1, 1, 10); SBAR();
            QK(11); VRD(5); ADD0(12); ADD1(13); CV(p1, 2, 12); SBAR();
            M0<0, 0>(mV); PV(0); LDL<0>(vV, srdV, soV); VRD(4); ADD0(14); SBAR();
            PV(1);  VRD(7); ADD1(15); CV(p1, 3, 14); SBAR();
            if (__builtin_expect(u + 1 == diagu, 0)) { asm volatile("s_nop 7" : "+v"(S)); mask(u + 1); }
            M0<1, 0>(mV); PV(2); LDL<1>(vV, srdV, soV); VRD(6); FM(0); SBAR();
            PV(3);  VRD(9); FM(1); FM(2); FM(3); ps0 = vadd(ps0, ps1); SBAR();
            M0<2, 0>(mV); PV(4); LDL<2>(vV, srdV, soV); VRD(8); FM(4); SBAR();
            PV(5);  VRD(11); FM(5); FM(6); FM(7); l = vadd(l, ps0); SBAR();
            PV(6);  VRD(10); FM(8); FM(9); FM(10); SBAR();
            PV(7);  FM(11); FM(12); FM(13); kb[0] += dk; kb[1] += dk; PIN(kb[0]); PIN(kb[1]); SBAR();
            PV(8);
        }
        LOOPSYNC();
        KRD(1); KRD(0); KRD(3); KRD(2);
        if (TB) {
            M0<0, 0>(wb + r2); PV(9); LDL<0>(vK, srdK, soK + 2 * KBLK); KRD(5); FM(12); FM(13); SBAR();
            PV(10); KRD(4); FM(14); FM(15); EXP(0); vb += dv; vbo += dv; PIN(vb); PIN(vbo); SBAR();
            M0<1, 0>(wb + r2); PV(11); LDL<1>(vK, srdK, soK + 2 * KBLK); EXP(1); EXP(2); EXP(3); SBAR();
        } else {
            PV(9);  KRD(5); FM(14); FM(15); EXP(0); SBAR();
            PV(10); KRD(4); EXP(1); EXP(2); vb += dv; vbo += dv; PIN(vb); PIN(vbo); SBAR();
            PV(11); EXP(3); SBAR();
        }
        ROTATE();
    }
    STAMPC(9);
#define TAIL_HEAD() \
        const bool last = u == NU - 1; \
        if (!last) { \
            if (TB) { dma<2, 0>(wb + r0, vK, srdK, so + 4 * KBLK); dma3(wb + r0 + BLK, vK1, srdK, so + 4 * KBLK); } \
            else { dma3(u + 3 < NU ? wb + r0 : LDS_NOWHERE, vK, srdK, so + 3 * KBLK); dma3(u + 2 < NU ? wb + 3 * BLK + r2 : LDS_NOWHERE, vV, srdV, so + 2 * KBLK); } \
        }
#define TAIL_MID() \
        if (TB) { if (last) { WAIT_VM(0); } else { WAIT_VM(6); } BARRIER(); }
#define TAIL_END() \
        if (!last) { BK_WAIT(); BARRIER(); if (TB) { dma<0, 0>(wb + r2, vK, srdK, so + 6 * KBLK); dma<1, 0>(wb + r2, vK, srdK, so + 6 * KBLK); } }
    if (u < nu) {
        TAIL_HEAD()
#pragma unroll
        for (int e = 4; e < 8; ++e) EXP(e);
        float ps0 = vadd(X[0], X[1]), ps1 = vadd(X[2], X[3]);
        CV(p0, 0, 0); CV(p0, 1, 2); EXP(8); EXP(9); CV(p0, 2, 4); CV(p0, 3, 6);
        TAIL_MID()
        VRD(1); VRD(0); VRD(3); VRD(2); VRD(5); VRD(4);
        EXP(10); EXP(11); EXP(12); EXP(13); SBAR();
        const unsigned mVt = last ? LDS_NOWHERE : wb + r1;
        PV(0);  if (TB) dma<0, 0>(mVt, vV, srdV, so + 2 * KBLK); EXP(14); EXP(15); SBAR();
        PV(1);  VRD(7); ADD0(4); ADD1(5); SBAR();
        PV(2);  VRD(6); if (TB) dma<1, 0>(mVt, vV, srdV, so + 2 * KBLK); CV(p1, 0, 8); CV(p1, 1, 10); SBAR();
        PV(3);  VRD(9); CV(p1, 2, 12); CV(p1, 3, 14); SBAR();
        PV(4);  VRD(8); if (TB) dma<2, 0>(mVt, vV, srdV, so + 2 * KBLK); ADD0(6); ADD1(7); SBAR();
        PV(5);  VRD(11); ADD0(8); ADD1(9); ADD0(10); SBAR();
        PV(6);  VRD(10); if (TB) dma<0, BLK>(mVt, vV1, srdV, so + 2 * KBLK); ADD1(11); ADD0(12); SBAR();
        PV(7);  if (TB) dma<1, BLK>(mVt, vV1, srdV, so + 2 * KBLK); ADD1(13); SBAR();
        PV(8);  if (TB) dma<2, BLK>(mVt, vV1, srdV, so + 2 * KBLK); ADD0(14); ADD1(15); SBAR();
        PV(9); PV(10); PV(11);
        l += ps0 + ps1;
        TAIL_END()
        ROTATE();
    }
    while (u < NU) {
        TAIL_HEAD()
        TAIL_MID()
        if (TB && !last) { dma3(wb + r1, vV, srdV, so + 2 * KBLK); dma3(wb + r1 + BLK, vV1, srdV, so + 2 * KBLK); }
        TAIL_END()
        ROTATE();
    }
#undef TAIL_HEAD
#undef TAIL_MID
#undef TAIL_END
    OPIN("s_nop 15");
    int lane2;
    asm volatile("v_mbcnt_lo_u32_b32 %0, -1, 0\n\tv_mbcnt_hi_u32_b32 %0, -1, %0" : "=v"(lane2));

    const int l32e = lane2 & 31, hie = lane2 >> 5, r8e = lane2 >> 3, c8e = lane2 & 7, r16e = lane2 >> 4;
    STAMP(5);
    const int reg = TB ? (sub ? r2 : r1) + hsel * BLK : (wid < 3 ? wid * BLK : 3 * BLK + r0);
    l = swapsum(l);
    if (__builtin_expect(__any(!(l < LBIG)), 0)) {
        const unsigned mb = ldsb + reg;
        const int dr2 = (lane2 >> 2) & 7, dh2 = lane2 >> 5, dc2 = lane2 & 3, dg2 = (lane2 >> 4) & 1;
        const unsigned vK0 = dr2 * KROW + 64u * dh2 + 16u * (dc2 ^ dg2), vK1_ = dr2 * KROW + 64u * dh2 + 16u * (dc2 ^ (2 + dg2)), vV0 = dr2 * KROW + 64u * dh2 + 16u * dc2;
        kb[0] = lp + reg + (l32e >> 3) * 1024 + (l32e & 7) * 64 + 16 * (hie ^ ((l32e >> 2) & 3));
        kb[1] = lp + reg + (l32e >> 3) * 1024 + (l32e & 7) * 64 + 16 * ((2 + hie) ^ ((l32e >> 2) & 3));
        vb = lp + reg + (4 * hie + ((lane2 & 15) >> 2)) * 64 + 32 * ((lane2 >> 4) & 1) + 8 * (lane2 & 3);
        vbo = vb + (TB ? 512 - sub * 512 : 512); vb += TB ? sub * 512 : 0;
        bf16x8 q2[12];
        {
            const unsigned vQ0 = dr2 * QROW + 64u * dh2 + 16u * (dc2 ^ dg2), vQ1 = dr2 * QROW + 64u * dh2 + 16u * (dc2 ^ (2 + dg2));
            const unsigned qs = (unsigned)(q0 + (TB ? 0 : 32 * sub)) * QROW + hsel * 384;
#pragma unroll
            for (int g = 0; g < 4; ++g) dma3(mb + g * 1024, (g & 1) ? vQ1 : vQ0, srdQ, qs + g * 8 * QROW);
            WAIT_VM(0);
#pragma unroll
            for (int ks = 0; ks < 12; ++ks) q2[ks] = ldk(KADDR(ks));
        }
        auto scores = [&](int t) {
            const int b = TB ? 2 * t + sub : t;
            asm volatile("s_waitcnt lgkmcnt(0)" ::: "memory");
#pragma unroll
            for (int g = 0; g < 4; ++g) dma3(mb + g * 1024, (g & 1) ? vK1_ : vK0, srdK, b * KBLK + g * 8 * KROW);
            WAIT_VM(0);
            S = MFMA(ldk(KADDR(0)), q2[0], f32x16{});
#pragma unroll
            for (int ks = 1; ks < 12; ++ks) S = MFMA(ldk(KADDR(ks)), q2[ks], S);
            if (t == diagu) {
                const int k0 = b * 32 + 4 * hie - (q0 + (TB ? 0 : 32 * sub) + l32e);
#pragma unroll
                for (int r = 0; r < 16; ++r) if (k0 + (r & 3) + 8 * (r >> 2) > 0) S[r] = -__builtin_inff();
            }
        };
        float mx = -__builtin_inff();
#pragma unroll 1
        for (int t = 0; t < nu; ++t) { scores(t); asm volatile("s_nop 15" : "+v"(S)); mx = max2f(mx, rowmax()); }
        m = mx * C2;
#pragma unroll
        for (int i = 0; i < 6; ++i) o[i] = f32x16{};
        l = 0.f;
#pragma unroll 1
        for (int t = 0; t < nu; ++t) {
            scores(t);
            const int b = TB ? 2 * t + sub : t;
            asm volatile("s_waitcnt lgkmcnt(0)" ::: "memory");
#pragma unroll
            for (int g = 0; g < 4; ++g) dma3(mb + g * 1024, vV0, srdV, b * KBLK + g * 8 * KROW);
            float ps = 0.f;
#pragma unroll
            for (int e = 0; e < 16; ++e) { X[e] = __builtin_amdgcn_exp2f(__builtin_fmaf(S[e], C2, -m)); ps += X[e]; }
            l += ps;
            p0 = (u32x4){cvtpk(X[0], X[1]), cvtpk(X[2], X[3]), cvtpk(X[4], X[5]), cvtpk(X[6], X[7])};
            p1 = (u32x4){cvtpk(X[8], X[9]), cvtpk(X[10], X[11]), cvtpk(X[12], X[13]), cvtpk(X[14], X[15])};
            WAIT_VM(0);
            #pragma unroll
            for (int k = 0; k < 12; ++k) { VRD(k); o[k % 6] = MFMA(VFR(k), __builtin_bit_cast(bf16x8, k < 6 ? p0 : p1), o[k % 6]); }
        }
        l = swapsum(l);
        asm volatile("s_waitcnt lgkmcnt(0)" ::: "memory");
    }

    const __amdgpu_buffer_rsrc_t srdO = __builtin_amdgcn_make_buffer_rsrc((void*)((char*)O + (size_t)s0 * QROW + kvh * 768), (short)0, (int)((unsigned)L * QROW - kvh * 768), 0x00020000);
    const lds_ptr lw = (lds_ptr)lds;
    const unsigned vO = r8e * QROW + c8e * 16;
#define STG(base, db, g, lo, hi_) *(__attribute__((address_space(3))) u32x2*)((base) + ((db) >> 1) * 4096 + 16 * (((4 * (db) + (g)) & 7) ^ (l32e & 7))) = (u32x2){lo, hi_}
    if constexpr (!TB) {
        if (nu > 0) {
            float il = __builtin_amdgcn_rcpf(l);
            asm volatile("s_nop 1" : "+v"(il));
            const lds_ptr wp = lw + reg + l32e * 128 + 8 * hie;
            const lds_ptr rp = lw + reg + r8e * 128 + 16 * (c8e ^ r8e);
            const unsigned ob = (unsigned)(q0 + 32 * sub) * QROW + hsel * 384;
            u32x4 w[4];
            auto scale = [&](int db) {
                float y[16];
#pragma unroll
                for (int r = 0; r < 16; ++r) y[r] = vmul(o[db][r], il);
                unsigned c[8];
#pragma unroll
                for (int r = 0; r < 8; ++r) c[r] = vcvt(y[2 * r], y[2 * r + 1]);
#pragma unroll
                for (int g = 0; g < 4; ++g) STG(wp, db, g, c[2 * g], c[2 * g + 1]);
                SBAR();
            };
            auto rd = [&](int s) {
#pragma unroll
                for (int g = 0; g < 4; ++g) w[g] = *(const __attribute__((address_space(3))) u32x4*)(rp + s * 4096 + g * 1024);
                SBAR();
            };
            auto st_ = [&](int s) {
#pragma unroll
                for (int g = 0; g < 4; ++g) __builtin_amdgcn_raw_buffer_store_b128(w[g], srdO, vO + s * 128, ob + g * 8 * QROW, STAUX);
                SBAR();
            };
            scale(0); scale(1); rd(0); scale(2); st_(0); scale(3); rd(1); scale(4); st_(1); scale(5); rd(2); st_(2);
        }
    } else {
        const lds_ptr xp = lw + reg + lane2 * 16;
        const lds_ptr xq = lw + (sub ? r1 : r2) + hsel * BLK + lane2 * 16;
        __attribute__((address_space(3))) f32x2* ml = (__attribute__((address_space(3))) f32x2*)(lw + LDS_MISC);
        ml[wid * 64 + lane2] = (f32x2){m, l};
#pragma unroll
        for (int i = 0; i < 3; ++i)
#pragma unroll
            for (int g = 0; g < 4; ++g) *(__attribute__((address_space(3))) f32x4*)(xp + (4 * i + g) * 1024) = (f32x4){o[2 * i + 1][4 * g], o[2 * i + 1][4 * g + 1], o[2 * i + 1][4 * g + 2], o[2 * i + 1][4 * g + 3]};
        BARRIER();
        const f32x2 pm = ml[(wid ^ 2) * 64 + lane2];
        f32x4 x[12];
#pragma unroll
        for (int i = 0; i < 12; ++i) x[i] = *(const __attribute__((address_space(3))) f32x4*)(xq + i * 1024);
        SBAR();
        const float mm = max2f(m, pm[0]);
        const float fa = __builtin_amdgcn_exp2f(m - mm), fb = __builtin_amdgcn_exp2f(pm[0] - mm);
        const float il = __builtin_amdgcn_rcpf(l * fa + pm[1] * fb);
        float wa = fa * il, wb2 = fb * il;
        asm volatile("s_nop 1" : "+v"(wa), "+v"(wb2));
        const lds_ptr wp = lw + r0 + hsel * BLK + l32e * 128 + 8 * hie + 16 * ((4 * sub) ^ (l32e & 4));
#pragma unroll
        for (int i = 0; i < 3; ++i) {
            float y[16];
#pragma unroll
            for (int r = 0; r < 16; ++r) y[r] = vmul(x[4 * i + (r >> 2)][r & 3], wb2);
#pragma unroll
            for (int r = 0; r < 16; ++r) y[r] = vfma_v(o[2 * i][r], wa, y[r]);
            unsigned c[8];
#pragma unroll
            for (int r = 0; r < 8; ++r) c[r] = vcvt(y[2 * r], y[2 * r + 1]);
#pragma unroll
            for (int g = 0; g < 4; ++g) *(__attribute__((address_space(3))) u32x2*)(wp + i * 4096 + 16 * (g ^ (l32e & 3))) = (u32x2){c[2 * g], c[2 * g + 1]};
            SBAR();
        }
        BARRIER();
        const lds_ptr rp = lw + r0 + hsel * BLK + sub * 2048 + r8e * 128 + 16 * (c8e ^ r8e);
        const unsigned ob = (unsigned)(q0 + 16 * sub) * QROW + hsel * 384;
        u32x4 w[6];
#pragma unroll
        for (int i = 0; i < 6; ++i) w[i] = *(const __attribute__((address_space(3))) u32x4*)(rp + (i % 3) * 4096 + (i / 3) * 1024);
#pragma unroll
        for (int i = 0; i < 6; ++i) __builtin_amdgcn_raw_buffer_store_b128(w[i], srdO, vO + (i % 3) * 128, ob + (i / 3) * 8 * QROW, STAUX);
    }
#undef STG
    STAMP(6);
#undef EXP
#undef FM
#undef KRD
#undef VRD
#undef VFR
#undef QK
#undef PV
#undef ADD0
#undef ADD1
#undef ROTATE
#undef BK_WAIT
#undef CV
#undef KADDR
}

DI int wave_max63(int x) {
    asm volatile("s_nop 1\n\tv_max_i32_dpp %0, %0, %0 row_shr:1 row_mask:0xf bank_mask:0xf\n\ts_nop 1\n\tv_max_i32_dpp %0, %0, %0 row_shr:2 row_mask:0xf bank_mask:0xf\n\t"
                 "s_nop 1\n\tv_max_i32_dpp %0, %0, %0 row_shr:4 row_mask:0xf bank_mask:0xf\n\ts_nop 1\n\tv_max_i32_dpp %0, %0, %0 row_shr:8 row_mask:0xf bank_mask:0xf\n\t"
                 "s_nop 1\n\tv_max_i32_dpp %0, %0, %0 row_bcast:15 row_mask:0xa bank_mask:0xf\n\ts_nop 1\n\tv_max_i32_dpp %0, %0, %0 row_bcast:31 row_mask:0xc bank_mask:0xf\n\ts_nop 0" : "+v"(x));
    return x;
}
constexpr int first_pure_b() { int i = TH; while (WB * ((i + 2) >> 1) < WA * TH) ++i; return i; }
constexpr int I0 = first_pure_b();
constexpr int NMIX = (I0 - TH) + TH / 2;
static_assert(NMIX <= 12 && I0 < 60, "level table (16 levels; thresholds are inline constants)");
constexpr int level(int k) {
    if (k < 4) return (I0 + 3 - k) | 0x10000;
    k -= 4;
    if (k >= NMIX) return (int)0xffff0040;
    int i = I0 - 1, jj = TH / 2 - 1, out = 0;
    for (int lev = 0; lev <= k; ++lev) {
        const bool useB = i >= TH && (jj < 0 || WB * ((i + 2) >> 1) >= WA * (2 * jj + 2));
        if (useB) { out = i | 0x10000; --i; } else { out = 2 * jj; --jj; }
    }
    return out;
}

__global__ __launch_bounds__(256, 2) void attn_v12(const unsigned short* __restrict__ Q, const unsigned short* __restrict__ K,
                                                  const unsigned short* __restrict__ V, const int* __restrict__ cu,
                                                  unsigned short* __restrict__ O, int nseq, int T, unsigned long long* dbg) {
    extern __shared__ __attribute__((aligned(16))) char lds[];
    unsigned long long st[16];
    STAMP(0);
    const int lane = threadIdx.x & 63;
    const int kvh = blockIdx.x & 7, j = blockIdx.x >> 3;
    const int wv = rfl(threadIdx.x >> 6);
    unsigned vp, vq;
    LaneAddr la;
    {
        const int jj = j & 31, pj = (j >> 5) * (32 - PFSKIP) + jj - PFSKIP;
        constexpr int LPW = (8192 + 4 * (64 - 2 * PFSKIP) - 1) / (4 * (64 - 2 * PFSKIP));
        const unsigned li = (pj * 4 + wv) * LPW + lane, tk = li / 3u;
        vp = (tk * KROW + (li - 3u * tk) * 128u) | ((PFB && lane < LPW && j < 64 && jj >= PFSKIP) ? 0u : OOB);
        const unsigned lq = (j * 4 + wv) * 64 + lane, tq = lq / 6u;
        vq = (tq * QROW + (lq - 6u * tq) * 128u) | ((PFQ && j < 64) ? 0u : OOB);
        const int dr = (lane >> 2) & 7, dh = lane >> 5, dc = lane & 3, dg = (lane >> 4) & 1;
        la.vK = (8 * wv + dr) * KROW + 64u * dh + 16u * (dc ^ ((2 * wv + dg) & 3));
        la.vV = (8 * wv + dr) * KROW + 64u * dh + 16u * dc;
        la.vQ0 = dr * QROW + 64u * dh + 16u * (dc ^ dg);
        la.vQ1 = dr * QROW + 64u * dh + 16u * (dc ^ (2 + dg));
        asm volatile("" : "+v"(vp), "+v"(vq), "+v"(la.vK), "+v"(la.vV), "+v"(la.vQ0), "+v"(la.vQ1));
    }
    asm volatile("" :: "s"(Q), "s"(K), "s"(V), "s"(O), "s"(cu), "s"(nseq), "s"(T));
    int r = j < 32 ? j : (j < 64 ? 95 - j : j);
    int a, b;
    if (nseq <= 15) {
        const unsigned pm = (unsigned)(uintptr_t)lds + LDS_MISC + wv * 256;
        const __amdgpu_buffer_rsrc_t srdKa = __builtin_amdgcn_make_buffer_rsrc((void*)((const char*)K + kvh * 384), (short)0, (int)((unsigned)T * KROW - kvh * 384), 0x00020000);
        const __amdgpu_buffer_rsrc_t srdVa = __builtin_amdgcn_make_buffer_rsrc((void*)((const char*)V + kvh * 384), (short)0, (int)((unsigned)T * KROW - kvh * 384), 0x00020000);
        const __amdgpu_buffer_rsrc_t srdQa = __builtin_amdgcn_make_buffer_rsrc((void*)((const char*)Q + kvh * 768), (short)0, (int)((unsigned)T * QROW - kvh * 768), 0x00020000);
        i32x16 c;
        if (PFQ) asm volatile("s_load_dwordx16 %0, %1, 0x0\n\ts_mov_b32 m0, %2\n\ts_nop 0\n\tbuffer_load_dword %6, %7, 0 offen lds\n\tbuffer_load_dword %3, %4, 0 offen lds\n\tbuffer_load_dword %3, %5, 0 offen lds\n\ts_waitcnt lgkmcnt(0)"
                     : "=&s"(c) : "s"(cu), "s"(pm), "v"(vp), "s"(srdKa), "s"(srdVa), "v"(vq), "s"(srdQa) : "memory");
        else asm volatile("s_load_dwordx16 %0, %1, 0x0\n\ts_mov_b32 m0, %2\n\ts_nop 0\n\tbuffer_load_dword %3, %4, 0 offen lds\n\tbuffer_load_dword %3, %5, 0 offen lds\n\ts_waitcnt lgkmcnt(0)"
                     : "=&s"(c) : "s"(cu), "s"(pm), "v"(vp), "s"(srdKa), "s"(srdVa) : "memory");
        asm volatile("v_mov_b32 %0, %1\n\tv_writelane_b32 %0, %2, 1\n\tv_writelane_b32 %0, %3, 2\n\tv_writelane_b32 %0, %4, 3\n\tv_writelane_b32 %0, %5, 4\n\tv_writelane_b32 %0, %6, 5\n\t"
                     "v_writelane_b32 %0, %7, 6\n\tv_writelane_b32 %0, %8, 7\n\tv_writelane_b32 %0, %9, 8\n\tv_writelane_b32 %0, %10, 9\n\tv_writelane_b32 %0, %11, 10\n\t"
                     "v_writelane_b32 %0, %12, 11\n\tv_writelane_b32 %0, %13, 12\n\tv_writelane_b32 %0, %14, 13\n\tv_writelane_b32 %0, %15, 14\n\tv_writelane_b32 %0, %16, 15\n\ts_nop 1"
                     : "=&v"(a) : "s"(c[0]), "s"(c[1]), "s"(c[2]), "s"(c[3]), "s"(c[4]), "s"(c[5]), "s"(c[6]), "s"(c[7]), "s"(c[8]), "s"(c[9]), "s"(c[10]), "s"(c[11]), "s"(c[12]), "s"(c[13]), "s"(c[14]), "s"(c[15]));
        b = __builtin_amdgcn_update_dpp(a, a, 0x101, 0xf, 0xf, false);
    } else {
        u32x2 ab;
        asm volatile("global_load_dwordx2 %0, %1, %2\n\ts_waitcnt vmcnt(0)" : "=&v"(ab) : "v"(min(lane, nseq - 1) * 4), "s"(cu) : "memory");
        a = ab[0]; b = ab[1];
    }
    const int n = lane < nseq ? (b - a + 31) >> 5 : 0;
    int code = -1;
    unsigned long long msk = 0;
    if (__builtin_expect(__ballot(n > I0 + 4) != 0, 0)) {
        const int nmax = __builtin_amdgcn_readlane(wave_max63(n), 63);
        for (int i = nmax - 1; i >= I0 + 4; --i) {
            msk = __ballot(n > i);
            const int c = __popcll(msk);
            if (r < c) { code = i | 0x10000; goto found; }
            r -= c;
        }
    }
    {
        int cnt;
        asm volatile("v_cmp_lt_i32_e64 %0, (%c5&65535), %4\n\ts_bcnt1_i32_b64 %2, %0\n\ts_mov_b32 %3, %c5\n\ts_cmp_lt_u32 %1, %2\n\ts_cbranch_scc1 1f\n\ts_sub_u32 %1, %1, %2\n\t"
                     "v_cmp_lt_i32_e64 %0, (%c6&65535), %4\n\ts_bcnt1_i32_b64 %2, %0\n\ts_mov_b32 %3, %c6\n\ts_cmp_lt_u32 %1, %2\n\ts_cbranch_scc1 1f\n\ts_sub_u32 %1, %1, %2\n\t"
                     "v_cmp_lt_i32_e64 %0, (%c7&65535), %4\n\ts_bcnt1_i32_b64 %2, %0\n\ts_mov_b32 %3, %c7\n\ts_cmp_lt_u32 %1, %2\n\ts_cbranch_scc1 1f\n\ts_sub_u32 %1, %1, %2\n\t"
                     "v_cmp_lt_i32_e64 %0, (%c8&65535), %4\n\ts_bcnt1_i32_b64 %2, %0\n\ts_mov_b32 %3, %c8\n\ts_cmp_lt_u32 %1, %2\n\ts_cbranch_scc1 1f\n\ts_sub_u32 %1, %1, %2\n\t"
                     "v_cmp_lt_i32_e64 %0, (%c9&65535), %4\n\ts_bcnt1_i32_b64 %2, %0\n\ts_mov_b32 %3, %c9\n\ts_cmp_lt_u32 %1, %2\n\ts_cbranch_scc1 1f\n\ts_sub_u32 %1, %1, %2\n\t"
                     "v_cmp_lt_i32_e64 %0, (%c10&65535), %4\n\ts_bcnt1_i32_b64 %2, %0\n\ts_mov_b32 %3, %c10\n\ts_cmp_lt_u32 %1, %2\n\ts_cbranch_scc1 1f\n\ts_sub_u32 %1, %1, %2\n\t"
                     "v_cmp_lt_i32_e64 %0, (%c11&65535), %4\n\ts_bcnt1_i32_b64 %2, %0\n\ts_mov_b32 %3, %c11\n\ts_cmp_lt_u32 %1, %2\n\ts_cbranch_scc1 1f\n\ts_sub_u32 %1, %1, %2\n\t"
                     "v_cmp_lt_i32_e64 %0, (%c12&65535), %4\n\ts_bcnt1_i32_b64 %2, %0\n\ts_mov_b32 %3, %c12\n\ts_cmp_lt_u32 %1, %2\n\ts_cbranch_scc1 1f\n\ts_sub_u32 %1, %1, %2\n\t"
                     "v_cmp_lt_i32_e64 %0, (%c13&65535), %4\n\ts_bcnt1_i32_b64 %2, %0\n\ts_mov_b32 %3, %c13\n\ts_cmp_lt_u32 %1, %2\n\ts_cbranch_scc1 1f\n\ts_sub_u32 %1, %1, %2\n\t"
                     "v_cmp_lt_i32_e64 %0, (%c14&65535), %4\n\ts_bcnt1_i32_b64 %2, %0\n\ts_mov_b32 %3, %c14\n\ts_cmp_lt_u32 %1, %2\n\ts_cbranch_scc1 1f\n\ts_sub_u32 %1, %1, %2\n\t"
                     "v_cmp_lt_i32_e64 %0, (%c15&65535), %4\n\ts_bcnt1_i32_b64 %2, %0\n\ts_mov_b32 %3, %c15\n\ts_cmp_lt_u32 %1, %2\n\ts_cbranch_scc1 1f\n\ts_sub_u32 %1, %1, %2\n\t"
                     "v_cmp_lt_i32_e64 %0, (%c16&65535), %4\n\ts_bcnt1_i32_b64 %2, %0\n\ts_mov_b32 %3, %c16\n\ts_cmp_lt_u32 %1, %2\n\ts_cbranch_scc1 1f\n\ts_sub_u32 %1, %1, %2\n\t"
                     "v_cmp_lt_i32_e64 %0, (%c17&65535), %4\n\ts_bcnt1_i32_b64 %2, %0\n\ts_mov_b32 %3, %c17\n\ts_cmp_lt_u32 %1, %2\n\ts_cbranch_scc1 1f\n\ts_sub_u32 %1, %1, %2\n\t"
                     "v_cmp_lt_i32_e64 %0, (%c18&65535), %4\n\ts_bcnt1_i32_b64 %2, %0\n\ts_mov_b32 %3, %c18\n\ts_cmp_lt_u32 %1, %2\n\ts_cbranch_scc1 1f\n\ts_sub_u32 %1, %1, %2\n\t"
                     "v_cmp_lt_i32_e64 %0, (%c19&65535), %4\n\ts_bcnt1_i32_b64 %2, %0\n\ts_mov_b32 %3, %c19\n\ts_cmp_lt_u32 %1, %2\n\ts_cbranch_scc1 1f\n\ts_sub_u32 %1, %1, %2\n\t"
                     "v_cmp_lt_i32_e64 %0, (%c20&65535), %4\n\ts_bcnt1_i32_b64 %2, %0\n\ts_mov_b32 %3, %c20\n\ts_cmp_lt_u32 %1, %2\n\ts_cbranch_scc1 1f\n\ts_sub_u32 %1, %1, %2\n\t"
                     "s_mov_b32 %3, -1\n1:"
                     : "=&s"(msk), "+s"(r), "=&s"(cnt), "=&s"(code) : "v"(n), "i"(level(0)), "i"(level(1)), "i"(level(2)), "i"(level(3)), "i"(level(4)), "i"(level(5)), "i"(level(6)), "i"(level(7)), "i"(level(8)), "i"(level(9)), "i"(level(10)), "i"(level(11)), "i"(level(12)), "i"(level(13)), "i"(level(14)), "i"(level(15)) : "scc");
    }
found:
    if (__builtin_expect(code < 0, 0)) { asm volatile("s_waitcnt vmcnt(0)" ::: "memory"); return; }
    const int tb = code >> 16, idx = (code & 0xffff) >> (1 - tb);
    const unsigned below = __builtin_amdgcn_mbcnt_hi((unsigned)(msk >> 32), __builtin_amdgcn_mbcnt_lo((unsigned)msk, 0u));
    const unsigned long long hit = __ballot(below == (unsigned)r) & msk;
    const int sl = __builtin_ctzll(hit);
    const int s0 = min(max(__builtin_amdgcn_readlane(a, sl), 0), T), L = min(__builtin_amdgcn_readlane(b, sl), T) - s0;
    if (__builtin_expect(L <= 0, 0)) { asm volatile("s_waitcnt vmcnt(0)" ::: "memory"); return; }
    if (DELAY2 > 0 && j >= 32) __builtin_amdgcn_s_sleep(DELAY2);
    STAMP(1);
    if (tb) attn_item<true>(st, Q, K, V, O, kvh, s0, L, idx, lds, wv, la);
    else attn_item<false>(st, Q, K, V, O, kvh, s0, L, idx, lds, wv, la);
}

extern "C" void kernel_launch(void* const* d_in, const int* in_sizes, int n_in, void* d_out, int out_size, void* d_ws, size_t ws_size, hipStream_t stream) {
    const int T = in_sizes[0] / (HQ * D), nseq = in_sizes[3] - 1;
    int slots = (T + 31) / 32 + nseq;
    if (slots < 64) slots = 64;
    (void)hipFuncSetAttribute((const void*)attn_v12, hipFuncAttributeMaxDynamicSharedMemorySize, LDS_BYTES);
    attn_v12<<<slots * 8, 256, LDS_BYTES, stream>>>((const unsigned short*)d_in[0], (const unsigned short*)d_in[1], (const unsigned short*)d_in[2],
                                                 (const int*)d_in[3], (unsigned short*)d_out, nseq, T, (unsigned long long*)d_ws);
}
```
